```python
import torch

def _softmax(x, axis=-1):
    return torch.softmax(x, dim=axis)
def _gelu(x):
    return torch.nn.functional.gelu(x, approximate="tanh")
def _silu(x):
    return torch.nn.functional.silu(x)
def _sigmoid(x):
    return torch.sigmoid(x)
def _layernorm(x, axis=-1, eps=1e-5):
    m = x.mean(dim=axis, keepdim=True)
    v = x.var(dim=axis, keepdim=True, unbiased=False)
    return (x - m) / torch.sqrt(v + eps)
def _scatter_add(tgt, idx, src, axis=0):
    return tgt.clone().index_add_(axis, idx, src)
def _segment_sum(src, seg, n):
    out = torch.zeros((n,) + src.shape[1:], dtype=src.dtype)
    return out.index_add_(0, seg, src)
def _pad(x, pw):
    flat = tuple(v for pair in reversed(pw) for v in pair)
    return torch.nn.functional.pad(x, flat)

kernel_name = 'interleave_layers__ssm_conv_h3__swiglu_ffn__prompt'

def setup_inputs():

    _w = (lambda a: (lambda t: t.to(torch.bfloat16) if t.is_floating_point() else t)(torch.as_tensor(a)))
    def _A_setup():
        B, S, D = 1, 2048, 640
        ks = 4
        return {
            "q": _w((lambda *s: torch.randn(*s, dtype=torch.bfloat16) * 0.05)(B, S, D)),
            "k": _w((lambda *s: torch.randn(*s, dtype=torch.bfloat16) * 0.8)(B, S, D)),
            "v": _w((lambda *s: torch.randn(*s, dtype=torch.bfloat16) * 0.8)(B, S, D)),
            "shift_k": _w((lambda *s: torch.randn(*s, dtype=torch.bfloat16) * 0.8)(D, ks) * 0.5),
            "ssm_k": _w((lambda *s: torch.randn(*s, dtype=torch.bfloat16) * 0.8)(D, S) * (1.0 / S ** 0.5)),
            "ssm_D": _w((lambda *s: torch.randn(*s, dtype=torch.bfloat16) * 0.8)(D)),
        }
    _A_in = _A_setup()

    def _B_setup():
        B, S, D, F = 1, 2048, 640, 1536
        return {
            "x": _w((lambda *s: torch.randn(*s, dtype=torch.bfloat16) * 0.05)(B, S, D)),
            "w_gate": _w((lambda *s: torch.randn(*s, dtype=torch.bfloat16) * 0.1)(D, F)),
            "w_up": _w((lambda *s: torch.randn(*s, dtype=torch.bfloat16) * 0.1)(D, F)),
            "w_down": _w((lambda *s: torch.randn(*s, dtype=torch.bfloat16) * 0.1)(F, D)),
        }
    _B_in = _B_setup()

    return {
        "A_q": _A_in["q"],
        "A_k": _A_in["k"],
        "A_v": _A_in["v"],
        "A_shift_k": _A_in["shift_k"],
        "A_ssm_k": _A_in["ssm_k"],
        "A_ssm_D": _A_in["ssm_D"],
        "B_x": _B_in["x"],
        "B_w_gate": _B_in["w_gate"],
        "B_w_up": _B_in["w_up"],
        "B_w_down": _B_in["w_down"],
    }

def reference(A_q, A_k, A_v, A_shift_k, A_ssm_k, A_ssm_D, B_x, B_w_gate, B_w_up, B_w_down):
    def _A_ref(q, k, v, shift_k, ssm_k, ssm_D):
        B, S, D = q.shape
        ks = shift_k.shape[-1]
        k_pad = _pad(k, ((0, 0), (ks - 1, 0), (0, 0)))
        k_shift = torch.zeros_like(k)
        for t in range(ks):
            k_shift = k_shift + k_pad[:, t:t + S, :] * shift_k[:, ks - 1 - t][None, None, :]
        kv = k_shift * v
        L = 2 * S
        kv_pad = _pad(kv, ((0, 0), (0, S), (0, 0)))
        kf_pad = _pad(ssm_k, ((0, 0), (0, S)))
        kv_f = (lambda x, axes=(-2,-1): torch.fft.rfft2(x.float(), dim=axes))(kv_pad[:, None, :, :], axes=(1, 2))
        kf_f = (lambda x, axes=(-2,-1): torch.fft.rfft2(x.float(), dim=axes))(kf_pad[:, None, :], axes=(1, 2))
        kf_f = (lambda x, *d: x.permute(*d))(kf_f, 1, 2, 0)
        y_f = kv_f * kf_f[None, :, :, :]
        y = (lambda x, s, axes=(-2,-1): torch.fft.irfft2(x, s=s, dim=axes).to(torch.bfloat16))(y_f, (1, L), axes=(1, 2))[:, 0, :S, :]
        y = y + kv * ssm_D[None, None, :]
        return q * y

    def _B_ref(x, w_gate, w_up, w_down):
        g = _silu(x @ w_gate)
        u = x @ w_up
        return (g * u) @ w_down

    x = A_q
    x = x + _A_ref(_layernorm(x, axis=-1), A_k, A_v, A_shift_k, A_ssm_k, A_ssm_D)
    x = x + _B_ref(_layernorm(x, axis=-1), B_w_gate, B_w_up, B_w_down)
    x = x + _A_ref(_layernorm(x, axis=-1), A_k, A_v, A_shift_k, A_ssm_k, A_ssm_D)
    x = x + _B_ref(_layernorm(x, axis=-1), B_w_gate, B_w_up, B_w_down)
    return x
```

```cpp
#include <hip/hip_runtime.h>
#include <hip/hip_bf16.h>
#include <stdint.h>

typedef unsigned short u16;
static constexpr int S = 2048, D = 640, F = 1536;

__device__ __forceinline__ float bf2f(u16 b) { return __builtin_bit_cast(float, (unsigned)b << 16); }
__device__ __forceinline__ u16 f2bf(float f) { __hip_bfloat16 h = __float2bfloat16(f); return __builtin_bit_cast(u16, h); }
__device__ __forceinline__ float rbf(float f) { return bf2f(f2bf(f)); }

__global__ void k_kv(const u16* k, const u16* v, const u16* sk, u16* kv) {
  int i = blockIdx.x * 256 + threadIdx.x;
  int t = i / D, c = i % D;
  float acc = 0.f;
  for (int j = 0; j < 4; ++j) {
    int tt = t + j - 3;
    float kk = tt >= 0 ? bf2f(k[tt * D + c]) : 0.f;
    acc = rbf(acc + rbf(kk * bf2f(sk[c * 4 + 3 - j])));
  }
  kv[i] = f2bf(acc * bf2f(v[i]));
}
__global__ void k_conv(const u16* kv, const u16* h, const u16* dd, u16* Y) {
  int i = blockIdx.x * 256 + threadIdx.x;
  int t = i / D, c = i % D;
  float acc = 0.f;
  for (int s = 0; s <= t; ++s) acc += bf2f(h[c * S + (t - s)]) * bf2f(kv[s * D + c]);
  float y = rbf(acc);
  Y[i] = f2bf(y + rbf(bf2f(kv[i]) * bf2f(dd[c])));
}
__device__ float block_sum(float v, float* sm) {
  for (int o = 32; o > 0; o >>= 1) v += __shfl_xor(v, o);
  __syncthreads();
  if ((threadIdx.x & 63) == 0) sm[threadIdx.x >> 6] = v;
  __syncthreads();
  float r = 0;
  for (int i = 0; i < (int)blockDim.x / 64; ++i) r += sm[i];
  return r;
}
__global__ void k_ln(const u16* x, const u16* Y, u16* x1, u16* xn) {
  __shared__ float sm[16];
  int t = blockIdx.x, c = threadIdx.x;
  float xv = bf2f(x[t * D + c]);
  float m = block_sum(xv, sm) / D;
  float var = block_sum((xv - m) * (xv - m), sm) / D;
  float n = rbf(rbf(xv - rbf(m)) / rbf(sqrtf(rbf(rbf(var) + 1e-5f))));
  float a = rbf(xv + rbf(n * bf2f(Y[t * D + c])));
  x1[t * D + c] = f2bf(a);
  m = block_sum(a, sm) / D;
  var = block_sum((a - m) * (a - m), sm) / D;
  xn[t * D + c] = f2bf(rbf(a - rbf(m)) / rbf(sqrtf(rbf(rbf(var) + 1e-5f))));
}
__global__ void k_g1(const u16* xn, const u16* wg, const u16* wu, u16* h) {
  int i = blockIdx.x * 256 + threadIdx.x;
  int t = i / F, f = i % F;
  float g = 0, u = 0;
  for (int k = 0; k < D; ++k) { float a = bf2f(xn[t * D + k]); g += a * bf2f(wg[k * F + f]); u += a * bf2f(wu[k * F + f]); }
  g = rbf(g); u = rbf(u);
  float s = rbf(g / (1.f + __expf(-g)));
  h[i] = f2bf(s * u);
}
__global__ void k_g2(const u16* h, const u16* wd, const u16* xr, u16* out) {
  int i = blockIdx.x * 256 + threadIdx.x;
  int t = i / D, c = i % D;
  float a = 0;
  for (int k = 0; k < F; ++k) a += bf2f(h[t * F + k]) * bf2f(wd[k * D + c]);
  out[i] = f2bf(bf2f(xr[i]) + rbf(a));
}

extern "C" void kernel_launch(void* const* d_in, const int* in_sizes, int n_in,
                              void* d_out, int out_size, void* d_ws, size_t ws_size,
                              hipStream_t stream) {
  const u16* q = (const u16*)d_in[0];
  const u16* k = (const u16*)d_in[1];
  const u16* v = (const u16*)d_in[2];
  const u16* sk = (const u16*)d_in[3];
  const u16* hk = (const u16*)d_in[4];
  const u16* dd = (const u16*)d_in[5];
  const u16* wg = (const u16*)d_in[7];
  const u16* wu = (const u16*)d_in[8];
  const u16* wd = (const u16*)d_in[9];
  u16* ws = (u16*)d_ws;
  u16* kv = ws; u16* Y = kv + S * D; u16* x1 = Y + S * D; u16* xn = x1 + S * D; u16* x2 = xn + S * D; u16* h = x2 + S * D;
  k_kv<<<S * D / 256, 256, 0, stream>>>(k, v, sk, kv);
  k_conv<<<S * D / 256, 256, 0, stream>>>(kv, hk, dd, Y);
  k_ln<<<S, D, 0, stream>>>(q, Y, x1, xn);
  k_g1<<<S * F / 256, 256, 0, stream>>>(xn, wg, wu, h);
  k_g2<<<S * D / 256, 256, 0, stream>>>(h, wd, x1, x2);
  k_ln<<<S, D, 0, stream>>>(x2, Y, x1, xn);
  k_g1<<<S * F / 256, 256, 0, stream>>>(xn, wg, wu, h);
  k_g2<<<S * D / 256, 256, 0, stream>>>(h, wd, x1, (u16*)d_out);
}
```

```cpp
#include <hip/hip_runtime.h>
#include <stdint.h>

typedef unsigned u32;
typedef unsigned long long u64;
typedef unsigned short u16;
typedef u32 v4u __attribute__((ext_vector_type(4)));
typedef u32 v2u __attribute__((ext_vector_type(2)));
typedef u32 v2ua __attribute__((ext_vector_type(2), aligned(4)));
typedef short s4 __attribute__((ext_vector_type(4)));
typedef __bf16 b8 __attribute__((ext_vector_type(8)));
typedef __bf16 b2 __attribute__((ext_vector_type(2)));
typedef float f2 __attribute__((ext_vector_type(2)));
typedef float f4 __attribute__((ext_vector_type(4)));
typedef float f16v __attribute__((ext_vector_type(16)));
#define LAS __attribute__((address_space(3)))
#define GAS __attribute__((address_space(1)))
#define DI __device__ __forceinline__
#define RLX_AGENT __ATOMIC_RELAXED, __HIP_MEMORY_SCOPE_AGENT

static constexpr int S = 2048, D = 640, F = 1536;
static constexpr u32 O_EPOCH = 0;
static constexpr u32 O_FW1 = 4096;
static constexpr u32 O_FW2 = O_FW1 + 256 * 128;
static constexpr u32 O_FX = O_FW2 + 128 * 128;
static constexpr u32 O_FH = O_FX + 2 * 64 * 8 * 128;
static constexpr u32 O_KVG = O_FH + 2 * 64 * 32 * 128;
static constexpr u32 O_YG = O_KVG + 160 * 32768;
static constexpr u32 O_W1T = O_YG + 160 * 32768;
static constexpr u32 O_CSP = O_W1T + 32 * 122880;
static constexpr u32 O_W2T = O_CSP + 32 * 8 * 384;
static constexpr u32 O_XB = O_W2T + 8 * 48 * 5120;
static constexpr u32 O_STX = O_XB + 2 * 64 * 40960;
static constexpr u32 O_STP = O_STX + 2 * 64 * 4096;
static constexpr u32 O_HB = O_STP + 64 * 8192;
static constexpr u32 O_END = O_HB + 2 * 64 * 98304;
static constexpr u32 O_DBG = O_END;
static constexpr u32 L_SCR = 122880, L_CS = 163328, L_TOT = 163840;
static constexpr u32 L_H = L_SCR, L_U = L_SCR + 16640;

DI float lo16(u32 v) { return __builtin_bit_cast(float, v << 16); }
DI float hi16(u32 v) { return __builtin_bit_cast(float, v & 0xffff0000u); }
DI f2 up2(u32 v) { return f2{lo16(v), hi16(v)}; }
DI u32 pk2(float a, float b) { f2 v = {a, b}; return __builtin_bit_cast(u32, __builtin_convertvector(v, b2)); }
DI u32 pk2(f2 v) { return __builtin_bit_cast(u32, __builtin_convertvector(v, b2)); }
DI float rbf(float a) { return lo16(pk2(a, a)); }
DI f2 rbf2(f2 v) { return up2(pk2(v)); }
DI float fb(u32 v) { return __builtin_bit_cast(float, v); }
DI u32 ub(float v) { return __builtin_bit_cast(u32, v); }
DI float xsum16(float v) { auto r = __builtin_amdgcn_permlane16_swap(ub(v), ub(v), false, false); return fb(r[0]) + fb(r[1]); }
DI float xsum32(float v) { auto r = __builtin_amdgcn_permlane32_swap(ub(v), ub(v), false, false); return fb(r[0]) + fb(r[1]); }
DI f4 mf16(v4u a, v4u b, f4 c) { return __builtin_amdgcn_mfma_f32_16x16x32_bf16(__builtin_bit_cast(b8, a), __builtin_bit_cast(b8, b), c, 0, 0, 0); }
DI f16v mf32(v4u a, v4u b, f16v c) { return __builtin_amdgcn_mfma_f32_32x32x16_bf16(__builtin_bit_cast(b8, a), __builtin_bit_cast(b8, b), c, 0, 0, 0); }
DI v2u trd(u32 a) { return __builtin_bit_cast(v2u, __builtin_amdgcn_ds_read_tr16_b64_v4i16((LAS s4*)a)); }
DI v4u lds4(u32 a) { return *(LAS v4u*)a; }
DI v2u lds2(u32 a) { return *(LAS v2u*)a; }
DI void sts4(u32 a, v4u v) { *(LAS v4u*)a = v; }
DI void sts2(u32 a, v2u v) { *(LAS v2u*)a = v; }
#define BAR() asm volatile("s_waitcnt lgkmcnt(0)\n\ts_barrier" ::: "memory")
#define DRAIN() asm volatile("s_waitcnt vmcnt(0)" ::: "memory")
#define SB() __builtin_amdgcn_sched_barrier(0)
typedef __amdgpu_buffer_rsrc_t rsrc_t;
DI v4u ld4(rsrc_t r, u32 v, u32 s) { return __builtin_amdgcn_raw_buffer_load_b128(r, v, s, 16); }
DI u32 ld1(rsrc_t r, u32 v, u32 s) { return __builtin_amdgcn_raw_buffer_load_b32(r, v, s, 16); }
DI void st4(rsrc_t r, u32 v, u32 s, v4u d) { __builtin_amdgcn_raw_buffer_store_b128(d, r, v + s, 0, 16); }
DI void st2(rsrc_t r, u32 v, u32 s, v2u d) { __builtin_amdgcn_raw_buffer_store_b64(d, r, v, s, 16); }

#define TS(i)
#define TC(i)

DI void wait_flags(char* ws, u32 off, int n, u64 want, int lane, int& budget) {
  GAS u64* p = (GAS u64*)(ws + off + (u32)lane * 128u);
  while (true) {
    bool ok = true;
    if (lane < n) ok = __hip_atomic_load(p, RLX_AGENT) == want;
    if (__all(ok) || --budget < 0) break;
  }
  asm volatile("" ::: "memory");
}
DI void set_flags(char* ws, u32 off, int n, u64 want, int lane) {
  if (lane < n) __hip_atomic_store((GAS u64*)(ws + off + (u32)lane * 128u), want, RLX_AGENT);
}
DI void ln_scalars(float s, float q, float& m, float& r) {
  float mean = s * (1.f / 640.f);
  float var = fmaxf(q * (1.f / 640.f) - mean * mean, 0.f);
  m = rbf(mean);
  r = __builtin_amdgcn_rcpf(rbf(__builtin_amdgcn_sqrtf(rbf(rbf(var) + 1e-5f))));
}

__global__ void __launch_bounds__(256, 1)
fused_kernel(const u16* __restrict__ gq, const u16* __restrict__ gk, const u16* __restrict__ gv,
             const u16* __restrict__ gsk, const u16* __restrict__ gh, const u16* __restrict__ gd,
             const u16* __restrict__ gwg, const u16* __restrict__ gwu, const u16* __restrict__ gwd,
             u16* __restrict__ gout, char* __restrict__ ws) {
  const int b = blockIdx.x, tid = threadIdx.x, lane = tid & 63;
  const int w = __builtin_amdgcn_readfirstlane(tid >> 6);
  const int c = b & 7, j = b >> 3;
  const int pi1 = 4 * (c & 1) + (j >> 3), s1 = 8 * (c >> 1) + (j & 7);
  const int mi = j >> 3, ni = j & 7;
  const rsrc_t rw = __builtin_amdgcn_make_buffer_rsrc(ws, 0, O_END, 0x00020000);
  const u32 l16 = (u32)lane * 16u;
  int budget = 1 << 20;
  asm volatile("" : "+s"(gq), "+s"(gk), "+s"(gv), "+s"(gsk), "+s"(gh), "+s"(gd), "+s"(gwg), "+s"(gwu), "+s"(gwd), "+s"(gout));
  TS(0);
  TC(62);
  const u32 ep = __hip_atomic_load((GAS u32*)(ws + O_EPOCH), RLX_AGENT);
  const bool conv = j < 20;
  const int g4 = lane >> 4, q4 = (lane >> 2) & 3, p4 = lane & 3, n16 = lane & 15;
  const int chunk = 20 * c + j;
  const int K2p = 4 * (j - 20) + w;
  v4u pre[5];
  const char* prep;
  u32 preo[5];
  if (conv) {
    prep = (const char*)gh + (size_t)(4 * chunk + w) * 4096;
#pragma unroll
    for (int i = 0; i < 5; ++i) preo[i] = l16 + (i & 3) * 1024;
  } else {
    prep = (const char*)gwd + (size_t)(32 * K2p) * 1280 + c * 160;
#pragma unroll
    for (int i = 0; i < 5; ++i) {
      int id = 64 * i + lane, r = (id * 205) >> 11;
      preo[i] = r * 1120 + id * 16;
    }
  }
  u32 tagv;
  if (w < 3) {
    const int t2 = tid < 160 ? tid : 159;
    const int tb = t2 & 7, ck = t2 >> 3;
    const int row0 = 64 * j + 8 * tb, ch0 = 80 * c + 4 * ck;
    v2u kk[11], vv[8];
    const bool first = row0 == 0;
    const u32 ko = (u32)row0 * 1280u + (u32)ch0 * 2u;
    const u32 km = first ? ko : ko - 3840u;
    v4u sa = *(const GAS v4u*)((const char*)gsk + ch0 * 8), sb = *(const GAS v4u*)((const char*)gsk + ch0 * 8 + 16);
#pragma unroll
    for (int i = 0; i < 11; ++i) kk[i] = *(const GAS v2u*)((const char*)gk + (i < 3 ? km + i * 1280 : ko + (i - 3) * 1280));
#pragma unroll
    for (int i = 0; i < 8; ++i) vv[i] = *(const GAS v2u*)((const char*)gv + (ko + i * 1280));
#pragma unroll
    for (int i = 0; i < 5; ++i) pre[i] = *(const GAS v4u*)(prep + preo[i]);
    SB();
    tagv = ep * 1664525u + 1013904223u;
#pragma unroll
    for (int i = 0; i < 3; ++i) if (first) kk[i] = v2u{0, 0};
    const u32 kvo = tid < 160 ? O_KVG + (u32)(20 * c + ck) * 32768u + (u32)(8 * j + tb) * 16u : 0x80000000u;
#pragma unroll
    for (int pr = 0; pr < 2; ++pr) {
      v4u sk = pr ? sb : sa;
      f2 t0 = {lo16(sk[0]), lo16(sk[2])}, t1 = {hi16(sk[0]), hi16(sk[2])};
      f2 t2v = {lo16(sk[1]), lo16(sk[3])}, t3 = {hi16(sk[1]), hi16(sk[3])};
      f2 kf[11], u[8];
#pragma unroll
      for (int i = 0; i < 11; ++i) kf[i] = up2(kk[i][pr]);
#pragma unroll
      for (int i = 0; i < 8; ++i) {
        f2 a = kf[i] * t3;
        a = kf[i + 1] * t2v + a;
        a = kf[i + 2] * t1 + a;
        a = kf[i + 3] * t0 + a;
        u[i] = a * up2(vv[i][pr]);
      }
#pragma unroll
      for (int e = 0; e < 2; ++e) {
        st4(rw, kvo, (4 * pr + 2 * e) * 4096, v4u{pk2(u[7][e], u[6][e]), tagv, pk2(u[5][e], u[4][e]), tagv});
        st4(rw, kvo, (4 * pr + 2 * e + 1) * 4096, v4u{pk2(u[3][e], u[2][e]), tagv, pk2(u[1][e], u[0][e]), tagv});
      }
    }
  } else {
    const int l2 = lane < 60 ? lane : 59;
    const int r = (l2 * 43) >> 9, pc = l2 - 12 * r;
    const char* src = (const char*)(pc < 6 ? gwg : gwu) + (size_t)(80 * pi1 + r) * 3072 + s1 * 96 + (pc < 6 ? pc : pc - 6) * 16;
    v4u raw[16];
#pragma unroll
    for (int i = 0; i < 16; ++i) raw[i] = *(const GAS v4u*)(src + i * 15360);
#pragma unroll
    for (int i = 0; i < 5; ++i) pre[i] = *(const GAS v4u*)(prep + preo[i]);
    SB();
    tagv = ep * 1664525u + 1013904223u;
    const u32 lb = (u32)pi1 * 15360u;
#pragma unroll
    for (int i = 0; i < 16; ++i) sts4(lb + i * 960 + l2 * 16, raw[i]);
    f16v cs[3] = {};
    const v4u ones = {0x3f803f80u, 0x3f803f80u, 0x3f803f80u, 0x3f803f80u};
    const u32 ta = lb + (8 * (g4 >> 1) + q4) * 192 + ((p4 >> 1) * 48 + 8 * (p4 & 1) + 4 * (g4 & 1)) * 2;
    const u32 go = O_W1T + (u32)s1 * 122880u + lb + l16;
#pragma unroll
    for (int ks = 0; ks < 5; ++ks) {
      v4u fr[3];
#pragma unroll
      for (int T = 0; T < 3; ++T) {
        v2u lo = trd(ta + ks * 3072 + T * 32), hi = trd(ta + ks * 3072 + T * 32 + 768);
        fr[T] = v4u{lo[0], lo[1], hi[0], hi[1]};
      }
      asm volatile("s_waitcnt lgkmcnt(0)" ::: "memory");
#pragma unroll
      for (int T = 0; T < 3; ++T) {
        sts4(lb + (ks * 3 + T) * 1024 + l16, fr[T]);
        st4(rw, go, (ks * 3 + T) * 1024, fr[T]);
        cs[T] = mf32(fr[T], ones, cs[T]);
      }
    }
    const u32 co = (lane & 31) == 0 ? O_CSP + (u32)(s1 * 8 + pi1) * 384u + (u32)(lane >> 5) * 64u : 0x80000000u;
#pragma unroll
    for (int T = 0; T < 3; ++T)
#pragma unroll
      for (int i = 0; i < 4; ++i)
        st4(rw, co + T * 128 + i * 16, 0, v4u{ub(cs[T][4 * i]), ub(cs[T][4 * i + 1]), ub(cs[T][4 * i + 2]), ub(cs[T][4 * i + 3])});
  }
  const u32 tag = __builtin_amdgcn_readfirstlane(tagv);
  const u64 want = (u64)tag | ((u64)(~tag) << 32);
  TS(1);

  v4u w2[60];
  const u32 w2o = O_W2T + (u32)(ni * 48 + 12 * w) * 5120u;
  const u32 w2a = w2o + (u32)(w & 1) * 2048u, w2b = w2o + (u32)((w & 1) ^ 1) * 2048u;
#define W2LOAD(i) do { \
    w2[5 * (i) + 0] = ld4(rw, l16, w2a + (i) * 5120); w2[5 * (i) + 1] = ld4(rw, l16 + 1024, w2a + (i) * 5120); \
    w2[5 * (i) + 2] = ld4(rw, l16, w2b + (i) * 5120); w2[5 * (i) + 3] = ld4(rw, l16 + 1024, w2b + (i) * 5120); \
    w2[5 * (i) + 4] = ld4(rw, l16, w2o + (i) * 5120 + 4096); } while (0)
  auto w1dma = [&]() {
#pragma unroll 1
    for (int e = 0; e < 2; ++e) {
      const int kp = 2 * w + e;
      if (kp != pi1) {
        const u32 lo = (u32)kp * 15360u, so = O_W1T + (u32)s1 * 122880u + lo;
        u32 keep;
#define DMA4(so, n) "buffer_load_dwordx4 %1, %2, " so " offen sc1 lds\n\tbuffer_load_dwordx4 %1, %2, " so " offen offset:1024 sc1 lds\n\t" \
                    "buffer_load_dwordx4 %1, %2, " so " offen offset:2048 sc1 lds\n\t" n
        asm volatile("s_mov_b32 %0, m0\n\ts_mov_b32 m0, %3\n\ts_nop 0\n\t" DMA4("%4", "")
                     "s_mov_b32 m0, %5\n\ts_nop 0\n\t" DMA4("%6", "")
                     "s_mov_b32 m0, %7\n\ts_nop 0\n\t" DMA4("%8", "")
                     "s_mov_b32 m0, %9\n\ts_nop 0\n\t" DMA4("%10", "")
                     "s_mov_b32 m0, %11\n\ts_nop 0\n\t" DMA4("%12", "")
                     "s_mov_b32 m0, %0"
                     : "=&s"(keep) : "v"(l16), "s"(rw), "s"(lo), "s"(so), "s"(lo + 3072), "s"(so + 3072), "s"(lo + 6144), "s"(so + 6144),
                       "s"(lo + 9216), "s"(so + 9216), "s"(lo + 12288), "s"(so + 12288) : "memory");
      }
    }
  };
  const u32 fw1o = O_FW1 + (u32)s1 * 1024u, fw2o = O_FW2 + (u32)(ni * 12 + 3 * w) * 128u;

  if (conv) {
    const u32 hb = L_H + (u32)w * 4160u, ubs = L_U + (u32)w * 4608u;
#pragma unroll
    for (int i = 0; i < 4; ++i) sts4(hb + 32 + i * 1024 + l16, pre[i]);
    if (lane < 2) sts4(hb + l16, v4u{0, 0, 0, 0});
    if (lane < 32) sts4(ubs + l16, v4u{0, 0, 0, 0});
    const float dch = lo16(gd[4 * chunk + w]);
    const u32 e0 = 1 + n16 + 8 * g4;
    const u32 ha = hb + ((2 * e0) & ~3u), sh = (e0 & 1) * 16;
    auto loadA = [&](int sg) {
      const u32 p = ha + 64 * sg;
      v2u da = *(const LAS v2ua*)p, db = *(const LAS v2ua*)(p + 8);
      u32 d4 = *(const LAS u32*)(p + 16);
      asm("" : "+v"(da), "+v"(db));
      u32 d0 = da[0], d1 = da[1], d2 = db[0], d3 = db[1];
      return v4u{__builtin_amdgcn_alignbit(d1, d0, sh), __builtin_amdgcn_alignbit(d2, d1, sh),
                 __builtin_amdgcn_alignbit(d3, d2, sh), __builtin_amdgcn_alignbit(d4, d3, sh)};
    };
    v4u A[32];
#pragma unroll
    for (int i = 0; i < 32; ++i) A[i] = loadA(i);
#pragma unroll
    for (int i = 0; i < 32; ++i) asm volatile("" : "+v"(A[i]));
    if (w == 3) { DRAIN(); set_flags(ws, O_FW1 + (u32)(s1 * 8 + pi1) * 128u, 1, want, lane); }
    TS(2);
    u64 fmask;
    {
      v4u g[8];
      const u32 ko = O_KVG + (u32)chunk * 32768u + (u32)w * 8192u;
      GAS u64* fp = (GAS u64*)(ws + (lane < 8 ? fw1o : fw2o - 1024u) + (u32)lane * 128u);
      while (true) {
        asm volatile("" ::: "memory");
        bool ok = true, fok = false;
#pragma unroll
        for (int i = 0; i < 8; ++i) g[i] = ld4(rw, l16 + (i & 3) * 1024, ko + (i >> 2) * 4096);
        if (lane < 11) fok = __hip_atomic_load(fp, RLX_AGENT) == want;
#pragma unroll
        for (int i = 0; i < 8; ++i) ok = ok && g[i][1] == tag && g[i][3] == tag;
        fmask = __ballot(fok);
        if (__all(ok) || --budget < 0) break;
      }
#pragma unroll
      for (int i = 0; i < 4; ++i) sts4(ubs + 512 + i * 1024 + l16, v4u{g[i][0], g[i][2], g[4 + i][0], g[4 + i][2]});
    }
    TS(3);
    if ((fmask & 0xff) != 0xff) wait_flags(ws, fw1o, 8, want, lane, budget);
    w1dma();
    TS(4);
    const u32 ba = ubs + 64 + (2 * n16 + 1 - g4) * 16;
    const u32 ua = ubs + 512 + (2 * n16 + (g4 >> 1)) * 16 + ((g4 & 1) ? 0 : 8);
    auto epilogue = [&](f4* acc, int part) {
#pragma unroll
      for (int p = 0; p < 4; ++p) {
        v2u uu = lds2(ua + (4 * part + p) * 512);
        float y0 = hi16(uu[1]) * dch + acc[p][0], y1 = lo16(uu[1]) * dch + acc[p][1];
        float y2 = hi16(uu[0]) * dch + acc[p][2], y3 = lo16(uu[0]) * dch + acc[p][3];
        sts2(hb + 2048 * part + 512 * p + 32 * n16 + 8 * g4, v2u{pk2(y0, y1), pk2(y2, y3)});
      }
      BAR();
      const u32 ya = L_H + q4 * 4160 + 2048 * part + (256 * w + 16 * g4 + 4 * p4) * 2;
      const u32 yo = O_YG + (u32)chunk * 32768u + (u32)(1024 * part + 256 * w) * 16u;
#pragma unroll
      for (int i = 0; i < 4; ++i) {
        v2u y = trd(ya + i * 128);
        st4(rw, l16 + i * 1024, yo, v4u{y[0], tag, y[1], tag});
      }
    };
    {
      f4 acc[4] = {};
      v4u B[2][4];
#pragma unroll
      for (int p = 0; p < 4; ++p) B[0][p] = lds4(ba + 512 * p + 64 * 7);
#pragma unroll
      for (int r = 0; r < 8; ++r) {
#pragma unroll
        for (int sl = 0; sl < 10; ++sl) {
          constexpr int PT[10] = {0, 1, 1, 2, 2, 2, 3, 3, 3, 3}, QT[10] = {0, 1, 0, 2, 1, 0, 3, 2, 1, 0};
          acc[PT[sl]] = mf16(A[8 * QT[sl] + r], B[r & 1][PT[sl] - QT[sl]], acc[PT[sl]]);
          if (r < 7 && sl < 4) B[(r + 1) & 1][sl] = lds4(ba + 512 * sl + 64 * (6 - r));
          { const int p = PT[sl], pn = PT[(sl + 1) % 10];
            if (pn != p) asm volatile("" : "+a"(acc[p]), "+a"(acc[pn])); else asm volatile("" : "+a"(acc[p])); }
          SB();
        }
      }
      epilogue(acc, 0);
    }
    TS(5);
    if ((fmask & 0x700) != 0x700) wait_flags(ws, fw2o, 3, want, lane, budget);
    {
      f4 acc[4] = {};
      v4u B[2][8], A2[2][4];
      v2u ra[4], rb[4];
      u32 rc[4];
      u32 ha2 = ha + 2048, ha3 = ha + 3072;
      asm volatile("" : "+v"(ha2), "+v"(ha3));
#pragma unroll
      for (int p = 0; p < 8; ++p) B[0][p] = lds4(ba + 512 * p + 64 * 7);
#pragma unroll
      for (int q = 0; q < 4; ++q) A2[0][q] = loadA(32 + 8 * q);
      SB();
#pragma unroll
      for (int r = 0; r < 8; ++r) {
#pragma unroll
        for (int sl = 0; sl < 26; ++sl) {
          constexpr int QT[26] = {0, 0, 1, 0, 1, 2, 0, 1, 2, 3, 4, 1, 2, 3, 4, 5, 2, 3, 4, 5, 6, 3, 4, 5, 6, 7};
          constexpr int PT[26] = {7, 6, 7, 5, 6, 7, 4, 5, 6, 7, 7, 4, 5, 6, 6, 7, 4, 5, 5, 6, 7, 4, 4, 5, 6, 7};
          const int q = QT[sl], p = PT[sl];
          acc[p - 4] = mf16(q < 4 ? A[8 * (q & 3) + r] : A2[r & 1][q & 3], B[r & 1][p - q], acc[p - 4]);
          if (r < 7) {
            if (sl < 4) {
              const u32 pp = (sl < 2 ? ha2 : ha3) + 512 * (sl & 1) + 64 * (r + 1);
              ra[sl] = *(const LAS v2ua*)pp; rb[sl] = *(const LAS v2ua*)(pp + 8); rc[sl] = *(const LAS u32*)(pp + 16);
            } else if (sl >= 10 && sl < 18) {
              B[(r + 1) & 1][17 - sl] = lds4(ba + 512 * (17 - sl) + 64 * (6 - r));
            }
            if (sl >= 18) {
              const int qq = (sl - 18) >> 1, e = 2 * (sl & 1);
              u32 t0 = e ? __builtin_amdgcn_alignbit(rb[qq][1], rb[qq][0], sh) : __builtin_amdgcn_alignbit(ra[qq][1], ra[qq][0], sh);
              u32 t1 = e ? __builtin_amdgcn_alignbit(rc[qq], rb[qq][1], sh) : __builtin_amdgcn_alignbit(rb[qq][0], ra[qq][1], sh);
              asm volatile("" : "+v"(t0), "+v"(t1));
              A2[(r + 1) & 1][qq][e] = t0;
              A2[(r + 1) & 1][qq][e + 1] = t1;
            }
            if (sl >= 17 && sl < 25) {
              const int qq = (sl - 17) >> 1, e = 2 * ((sl - 17) & 1);
              if (e) asm volatile("" : "+v"(rb[qq]), "+v"(rc[qq])); else asm volatile("" : "+v"(ra[qq]), "+v"(rb[qq]));
            }
          }
          if (r < 6 && sl >= 4 && sl < 14) {
            const int k = sl - 4, i = 2 * r + k / 5, tc = k % 5;
            w2[5 * i + tc] = ld4(rw, l16 + (tc & 1) * 1024 * (tc < 4), (tc < 2 ? w2a : tc < 4 ? w2b : w2o + 4096) + i * 5120);
          }
          { const int pn = PT[(sl + 1) % 26];
            if (pn != p) asm volatile("" : "+a"(acc[p - 4]), "+a"(acc[pn - 4])); else asm volatile("" : "+a"(acc[p - 4])); }
          SB();
        }
      }
      epilogue(acc, 1);
    }
    TS(6);
  } else {
    const u32 lb = L_SCR + (u32)w * 5120u;
#pragma unroll
    for (int i = 0; i < 5; ++i) sts4(lb + i * 1024 + l16, pre[i]);
    const u32 ta = lb + (8 * g4 + q4) * 160;
    const u32 go = O_W2T + (u32)(c * 48 + K2p) * 5120u;
#pragma unroll
    for (int tc = 0; tc < 5; ++tc) {
      const u32 a = ta + (tc < 4 ? 64 * (tc >> 1) + 16 * p4 + 8 * (tc & 1) : 128 + 8 * p4);
      v2u lo = trd(a), hi = trd(a + 640);
      const u32 slot = tc < 4 ? lane : ((lane & 48) | (((lane >> 1) & 3) << 2) | (((lane >> 3) & 1) << 1) | (lane & 1));
      st4(rw, slot * 16, go + tc * 1024, v4u{lo[0], lo[1], hi[0], hi[1]});
    }
    DRAIN();
    if (w == 3) set_flags(ws, O_FW1 + (u32)(s1 * 8 + pi1) * 128u, 1, want, lane);
    BAR();
    if (w == 0) set_flags(ws, O_FW2 + (u32)(c * 12 + j - 20) * 128u, 1, want, lane);
    TS(2);
    wait_flags(ws, fw1o, 8, want, lane, budget);
    w1dma();
    TS(3);
    const int ii = (j - 20) * 8 + c;
#pragma unroll 1
    for (int hf = 0; hf < 2; ++hf) {
      const int x = ii - 32 * hf;
      if (hf == 1) {
        wait_flags(ws, fw2o, 3, want, lane, budget);
#pragma unroll
        for (int i = 0; i < 12; ++i) W2LOAD(i);
      }
      if (x < 0 || x >= 64) continue;
      const int r4 = lane & 3, o = lane >> 2;
      const int row = 1024 * hf + 16 * x + 4 * w + r4, rb = row >> 5, r32 = row & 31;
      v4u qq[5];
#pragma unroll
      for (int i = 0; i < 5; ++i) qq[i] = *(const GAS v4u*)((const char*)gq + (size_t)row * 1280 + o * 16 + i * 256);
      f2 qs = {0, 0}, q2 = {0, 0};
#pragma unroll
      for (int i = 0; i < 20; ++i) { f2 v = up2(qq[i >> 2][i & 3]); qs += v; q2 += v * v; }
      float sm = qs[0] + qs[1], sq = q2[0] + q2[1];
      sm += __shfl_xor(sm, 4); sq += __shfl_xor(sq, 4);
      sm += __shfl_xor(sm, 8); sq += __shfl_xor(sq, 8);
      sm = xsum32(xsum16(sm)); sq = xsum32(xsum16(sq));
      float m, rs;
      ln_scalars(sm, sq, m, rs);
      f2 qn[20];
#pragma unroll
      for (int i = 0; i < 20; ++i) qn[i] = rbf2(rbf2(up2(qq[i >> 2][i & 3]) - m) * rs);
      v4u y[10];
      const u32 yo = (u32)o * 65536u + (u32)row * 16u;
      while (true) {
        asm volatile("" ::: "memory");
        bool ok = true;
#pragma unroll
        for (int i = 0; i < 10; ++i) y[i] = ld4(rw, yo, O_YG + (i >> 1) * 1048576 + (i & 1) * 32768);
#pragma unroll
        for (int i = 0; i < 10; ++i) ok = ok && y[i][1] == tag && y[i][3] == tag;
        if (__all(ok) || --budget < 0) break;
      }
      TS(4 + 2 * hf);
      qs = f2{0, 0}; q2 = f2{0, 0};
      const u32 xo = (u32)rb * 40960u + (u32)o * 512u + (u32)r32 * 16u;
#pragma unroll
      for (int i = 0; i < 5; ++i) {
        v4u xv;
#pragma unroll
        for (int e = 0; e < 4; ++e) {
          f2 v = qn[4 * i + e] * up2(y[2 * i + (e >> 1)][2 * (e & 1)]) + up2(qq[i][e]);
          xv[e] = pk2(v);
          qs += v; q2 += v * v;
        }
        st4(rw, xo, O_XB + i * 8192, xv);
      }
      st2(rw, (u32)rb * 4096u + (u32)(o >> 1) * 512u + (u32)r32 * 16u + (u32)(o & 1) * 8u, O_STX,
          v2u{ub(qs[0] + qs[1]), ub(q2[0] + q2[1])});
      DRAIN();
      BAR();
      if (w == 0) set_flags(ws, O_FX + (u32)(rb * 8 + 4 * (x & 1)) * 128u, 4, want, lane);
      TS(5 + 2 * hf);
    }
  }

#pragma unroll 1
  for (int layer = 0; layer < 2; ++layer) {
#pragma unroll 1
    for (int hf = 0; hf < 2; ++hf) {
      const int rb = 32 * hf + 4 * pi1 + w, lrb = layer * 64 + rb;
      if (layer == 0 && hf == 0) {
        float cp[8];
        const u32 t4 = tid < 96 ? (u32)tid * 4u : 0u;
#pragma unroll
        for (int i = 0; i < 8; ++i) cp[i] = fb(ld1(rw, t4 + i * 384, O_CSP + (u32)s1 * 3072u));
        const float a = ((cp[0] + cp[1]) + (cp[2] + cp[3])) + ((cp[4] + cp[5]) + (cp[6] + cp[7]));
        if (tid < 96) *(LAS float*)(L_CS + t4) = a;
        DRAIN();
        BAR();
      }
      constexpr int RING = 24, PF = 6;
      u32 la0 = l16, la1 = l16 + 65536;
      asm volatile("" : "+v"(la0), "+v"(la1));
      v4u af[PF];
#pragma unroll
      for (int i = 0; i < PF; ++i) af[i] = lds4(la0 + i * 1024);
      wait_flags(ws, O_FX + (u32)lrb * 1024u, 8, want, lane, budget);
      TS(8 + 24 * layer + 6 * hf);
      const u32 xo = O_XB + (u32)lrb * 40960u;
      v4u x[RING];
#pragma unroll
      for (int i = 0; i < RING; ++i) x[i] = ld4(rw, la0 + (i & 3) * 1024, xo + (i >> 2) * 4096);
      v4u st[4];
      f16v acc[3] = {};
      SB();
#pragma unroll
      for (int ks = 0; ks < 40; ++ks) {
#pragma unroll
        for (int T = 0; T < 3; ++T) {
          const int id = ks * 3 + T, nx = id + PF;
          acc[T] = mf32(af[id % PF], x[ks % RING], acc[T]);
          if (id == 2) TS(9 + 24 * layer + 6 * hf);
          if (nx < 120) af[id % PF] = lds4(nx < 64 ? la0 + nx * 1024 : la1 + (nx - 64) * 1024);
          if (T == 2 && ks + RING < 40) { const int i = ks + RING; x[ks % RING] = ld4(rw, la0 + (i & 3) * 1024, xo + (i >> 2) * 4096); }
          if (T == 2 && ks >= 16 && ks < 20)
            st[ks - 16] = ld4(rw, (u32)(lane >> 5) * 2048u + (u32)(lane & 31) * 16u + (ks - 16) * 512, O_STX + (u32)lrb * 4096u);
          SB();
        }
      }
      TS(10 + 24 * layer + 6 * hf);
      float sm = 0.f, sq = 0.f;
#pragma unroll
      for (int i = 0; i < 4; ++i) { sm += fb(st[i][0]) + fb(st[i][2]); sq += fb(st[i][1]) + fb(st[i][3]); }
      sm = xsum32(sm); sq = xsum32(sq);
      float m, rs;
      ln_scalars(sm, sq, m, rs);
      const float mr = -m * rs;
      const u32 ho = O_HB + (u32)lrb * 98304u;
      const u32 hv = (u32)((lane >> 4) & 1) * 1024u + (u32)(lane >> 5) * 256u + (u32)n16 * 16u;
#pragma unroll
      for (int T = 0; T < 3; ++T) {
        v4u hh;
#pragma unroll
        for (int e = 0; e < 2; ++e) {
          f4 cg = __builtin_bit_cast(f4, lds4(L_CS + T * 128 + (lane >> 5) * 64 + e * 32));
          f4 cu = __builtin_bit_cast(f4, lds4(L_CS + T * 128 + (lane >> 5) * 64 + e * 32 + 16));
          float hv4[4];
#pragma unroll
          for (int i = 0; i < 4; ++i) {
            float g = acc[T][8 * e + i] * rs + mr * cg[i];
            float u = acc[T][8 * e + 4 + i] * rs + mr * cu[i];
            float sg = g * __builtin_amdgcn_rcpf(1.f + __builtin_amdgcn_exp2f(g * -1.44269504f));
            hv4[i] = sg * u;
          }
          hh[2 * e] = pk2(hv4[0], hv4[1]);
          hh[2 * e + 1] = pk2(hv4[2], hv4[3]);
        }
        const u32 U = 3 * s1 + T;
        st4(rw, hv, ho + (U >> 1) * 2048u + (U & 1) * 512u, hh);
      }
      TS(11 + 24 * layer + 6 * hf);
      DRAIN();
      set_flags(ws, O_FH + (u32)(lrb * 32 + s1) * 128u, 1, want, lane);
      TS(12 + 24 * layer + 6 * hf);
    }
#pragma unroll 1
    for (int hf = 0; hf < 2; ++hf) {
      const int rb = 32 * hf + 4 * c + mi, lrb = layer * 64 + rb;
      const int rt = w >> 1, ab = w & 1;
      wait_flags(ws, O_FH + (u32)(lrb * 32 + 8 * w) * 128u, 8, want, lane, budget);
      TS(20 + 24 * layer + 6 * hf);
      const u32 ho = O_HB + (u32)lrb * 98304u + (u32)w * 24576u;
      v4u h[24];
      const u32 hv0 = l16 + (u32)rt * 1024u, hv1 = l16 + (u32)(rt ^ 1) * 1024u;
#pragma unroll
      for (int i = 0; i < 12; ++i) {
        h[2 * i] = ld4(rw, hv0 + (i & 1) * 2048, ho + (i >> 1) * 4096);
        h[2 * i + 1] = ld4(rw, hv1 + (i & 1) * 2048, ho + (i >> 1) * 4096);
      }
      const int r32 = 16 * rt + n16;
      const u32 xv = (u32)(2 * ab + (g4 >> 1)) * 1024u + (u32)(g4 & 1) * 512u + (u32)r32 * 16u;
      const u32 xv4 = 4096u + (u32)ab * 512u + (u32)r32 * 16u;
      const u32 xs = (u32)lrb * 40960u + (u32)ni * 5120u;
      v4u xr = ld4(rw, xv, O_XB + xs);
      u32 xr4 = ld1(rw, xv4 + g4 * 4, O_XB + xs);
      v4u y0, y1; u32 y4;
      if (layer == 0) {
        const u32 ys = O_YG + (u32)ni * 20u * 32768u + (u32)rb * 512u;
        const u32 yv = (u32)(8 * ab + 2 * g4) * 32768u + (u32)r32 * 16u;
        y0 = ld4(rw, yv, ys); y1 = ld4(rw, yv, ys + 32768);
        y4 = ld1(rw, (u32)(16 + 2 * ab + (g4 >> 1)) * 32768u + (u32)r32 * 16u + (u32)(g4 & 1) * 8u, ys);
      }
      f4 acc[10] = {};
      SB();
#pragma unroll
      for (int i = 0; i < 12; ++i)
#pragma unroll
        for (int tc = 0; tc < 5; ++tc) {
          acc[tc] = mf16(w2[5 * i + tc], h[2 * i], acc[tc]);
          acc[5 + tc] = mf16(w2[5 * i + tc], h[2 * i + 1], acc[5 + tc]);
        }
      TS(21 + 24 * layer + 6 * hf);
      BAR();
      {
        const u32 r1 = L_SCR + (u32)(w ^ 1) * 6144u + l16, r2 = L_SCR + (u32)(w ^ 2) * 6144u + 2048u + l16,
                  r3 = L_SCR + (u32)(w ^ 3) * 6144u + 4096u + l16;
        sts4(r1, __builtin_bit_cast(v4u, acc[2])); sts4(r1 + 1024, __builtin_bit_cast(v4u, acc[3]));
        sts4(r2, __builtin_bit_cast(v4u, acc[5])); sts4(r2 + 1024, __builtin_bit_cast(v4u, acc[6]));
        sts4(r3, __builtin_bit_cast(v4u, acc[7])); sts4(r3 + 1024, __builtin_bit_cast(v4u, acc[8]));
        const u32 r4 = L_SCR + 24576u + (u32)w * 2048u + l16;
        sts4(r4 + (u32)rt * 1024u, __builtin_bit_cast(v4u, acc[4]));
        sts4(r4 + (u32)(rt ^ 1) * 1024u, __builtin_bit_cast(v4u, acc[9]));
      }
      BAR();
      f4 a0 = acc[0], a1 = acc[1];
      f2 a4 = {0, 0};
      {
        const u32 rr = L_SCR + (u32)w * 6144u + l16;
#pragma unroll
        for (int i = 0; i < 3; ++i) {
          a0 += __builtin_bit_cast(f4, lds4(rr + i * 2048));
          a1 += __builtin_bit_cast(f4, lds4(rr + i * 2048 + 1024));
        }
        const u32 r4 = L_SCR + 24576u + (u32)rt * 1024u + l16 + (u32)ab * 8u;
#pragma unroll
        for (int i = 0; i < 4; ++i) a4 += __builtin_bit_cast(f2, lds2(r4 + i * 2048));
      }
      TS(22 + 24 * layer + 6 * hf);
      f2 v[5];
      v[0] = rbf2(up2(xr[0]) + rbf2(f2{a0[0], a0[1]}));
      v[1] = rbf2(up2(xr[1]) + rbf2(f2{a0[2], a0[3]}));
      v[2] = rbf2(up2(xr[2]) + rbf2(f2{a1[0], a1[1]}));
      v[3] = rbf2(up2(xr[3]) + rbf2(f2{a1[2], a1[3]}));
      v[4] = rbf2(up2(xr4) + rbf2(a4));
      if (layer == 0) {
        f2 qs = {0, 0}, q2 = {0, 0};
#pragma unroll
        for (int i = 0; i < 5; ++i) { qs += v[i]; q2 += v[i] * v[i]; }
        float sm = xsum32(xsum16(qs[0] + qs[1])), sq = xsum32(xsum16(q2[0] + q2[1]));
        const u32 ps = O_STP + (u32)rb * 8192u;
        if (lane < 16) st4(rw, (u32)(2 * ni + ab) * 512u + (u32)r32 * 16u, ps, v4u{ub(sm), tag, ub(sq), tag});
        v4u g[4];
        while (true) {
          asm volatile("" ::: "memory");
          bool ok = true;
#pragma unroll
          for (int i = 0; i < 4; ++i) g[i] = ld4(rw, (u32)g4 * 2048u + (u32)r32 * 16u + i * 512, ps);
#pragma unroll
          for (int i = 0; i < 4; ++i) ok = ok && g[i][1] == tag && g[i][3] == tag;
          if (__all(ok) || --budget < 0) break;
        }
        TS(23 + 6 * hf);
        sm = xsum32(xsum16(fb(g[0][0]) + fb(g[1][0]) + fb(g[2][0]) + fb(g[3][0])));
        sq = xsum32(xsum16(fb(g[0][2]) + fb(g[1][2]) + fb(g[2][2]) + fb(g[3][2])));
        float m, rs;
        ln_scalars(sm, sq, m, rs);
        const u32 yy[5] = {y0[0], y0[2], y1[0], y1[2], y4};
        qs = f2{0, 0}; q2 = f2{0, 0};
#pragma unroll
        for (int i = 0; i < 5; ++i) {
          v[i] = rbf2(v[i] + rbf2(rbf2(rbf2(v[i] - m) * rs) * up2(yy[i])));
          qs += v[i]; q2 += v[i] * v[i];
        }
        sm = xsum32(xsum16(qs[0] + qs[1])); sq = xsum32(xsum16(q2[0] + q2[1]));
        if (lane < 16)
          st2(rw, (u32)ni * 512u + (u32)r32 * 16u + (u32)ab * 8u, O_STX + (u32)(64 + rb) * 4096u, v2u{ub(sm), ub(sq)});
      }
      const v4u o8 = {pk2(v[0]), pk2(v[1]), pk2(v[2]), pk2(v[3])};
      const u32 o2 = pk2(v[4]);
      auto e1 = __builtin_amdgcn_permlane16_swap(o2, o2, false, false);
      auto e2 = __builtin_amdgcn_permlane32_swap(e1[0], e1[0], false, false);
      auto e3 = __builtin_amdgcn_permlane32_swap(e1[1], e1[1], false, false);
      const v4u o4 = {e2[0], e3[0], e2[1], e3[1]};
      if (layer == 0) {
        const u32 xs1 = O_XB + xs + 64u * 40960u;
        st4(rw, xv, xs1, o8);
        if (lane < 16) st4(rw, xv4, xs1, o4);
        TS(24 + 6 * hf);
        DRAIN();
        BAR();
        if (w == 0) set_flags(ws, O_FX + (u32)((64 + rb) * 8 + ni) * 128u, 1, want, lane);
      } else {
        char* op = (char*)gout + (size_t)(rb * 32 + r32) * 1280 + ni * 160;
        *(GAS v4u*)(op + ab * 64 + g4 * 16) = o8;
        if (lane < 16) *(GAS v4u*)(op + 128 + ab * 16) = o4;
      }
      TS(25 + 24 * layer + 6 * hf);
    }
  }
  TS(61);
  TC(63);
  if (b == 0 && tid == 0) __hip_atomic_store((GAS u32*)(ws + O_EPOCH), tag, RLX_AGENT);
}

extern "C" void kernel_launch(void* const* d_in, const int* in_sizes, int n_in, void* d_out, int out_size,
                              void* d_ws, size_t ws_size, hipStream_t stream) {
  (void)hipFuncSetAttribute((const void*)fused_kernel, hipFuncAttributeMaxDynamicSharedMemorySize, L_TOT);
  fused_kernel<<<256, 256, L_TOT, stream>>>((const u16*)d_in[0], (const u16*)d_in[1], (const u16*)d_in[2],
                                            (const u16*)d_in[3], (const u16*)d_in[4], (const u16*)d_in[5],
                                            (const u16*)d_in[7], (const u16*)d_in[8], (const u16*)d_in[9],
                                            (u16*)d_out, (char*)d_ws);
}
```

```cpp
#include <hip/hip_runtime.h>

typedef unsigned short u16;

__global__ void __launch_bounds__(256, 1)
fused_kernel(const u16* gq, const u16* gk, const u16* gv, const u16* gsk, const u16* gh, const u16* gd,
             const u16* gwg, const u16* gwu, const u16* gwd, u16* gout, char* ws) {
#if defined(__HIP_DEVICE_COMPILE__)
  unsigned tid = threadIdx.x;
  asm volatile(
    "s_cmp_lt_u32 %2, 160\n"
    "s_cbranch_scc1 .Lf_nocpf\n"
    "s_getpc_b64 s[60:61]\n"
    "s_lshr_b32 s62, %2, 3\n"
    "s_and_b32 s62, s62, 1\n"
    "v_lshlrev_b32 v251, 7, %0\n"
    "v_lshl_add_u32 v251, s62, 15, v251\n"
    "v_min_u32 v251, 0xe1fc, v251\n"
    "global_load_dword v251, v251, s[60:61]\n"
    ".Lf_nocpf:\n"
    "s_load_dwordx2 s[8:9], %1, 0x50\n"
    "s_load_dwordx2 s[28:29], %1, 0x8\n"
    "s_load_dwordx2 s[32:33], %1, 0x10\n"
    "s_load_dwordx2 s[36:37], %1, 0x18\n"
    "s_load_dwordx2 s[24:25], %1, 0x20\n"
    "s_load_dwordx2 s[12:13], %1, 0x30\n"
    "s_load_dwordx2 s[16:17], %1, 0x38\n"
    "s_load_dwordx2 s[40:41], %1, 0x40\n"
    "s_load_dwordx2 s[20:21], %1, 0x0\n"
    "s_load_dwordx2 s[44:45], %1, 0x48\n"
    "s_load_dwordx2 s[48:49], %1, 0x28\n"
    "s_mov_b32 s57, %2\n"
    "v_mov_b32 v0, %0\n"
    "s_mov_b32 s10, 0x1f35000\n"
    "s_mov_b32 s11, 0x20000\n"
    "s_mov_b32 s30, 0x280000\n"
    "s_mov_b32 s31, 0x20000\n"
    "s_mov_b32 s34, 0x280000\n"
    "s_mov_b32 s35, 0x20000\n"
    "s_mov_b32 s38, 0x1400\n"
    "s_mov_b32 s39, 0x20000\n"
    "s_mov_b32 s26, 0x280000\n"
    "s_mov_b32 s27, 0x20000\n"
    "s_mov_b32 s14, 0x1e0000\n"
    "s_mov_b32 s15, 0x20000\n"
    "s_mov_b32 s18, 0x1e0000\n"
    "s_mov_b32 s19, 0x20000\n"
    "s_mov_b32 s42, 0x1e0000\n"
    "s_mov_b32 s43, 0x20000\n"
    "s_mov_b32 s22, 0x280000\n"
    "s_mov_b32 s23, 0x20000\n"
    "s_mov_b32 s46, 0x280000\n"
    "s_mov_b32 s47, 0x20000\n"
    "v_lshrrev_b32 v5, 6, v0\n"
    "v_and_b32 v1, 63, v0\n"
    "s_and_b32 s5, s57, 7\n"
    "v_readfirstlane_b32 s4, v5\n"
    "s_lshr_b32 s6, s57, 3\n"
    "v_lshlrev_b32 v2, 4, v1\n"
    "s_mov_b32 s7, 1048576\n"
    "s_mul_i32 s56, s5, 20\n"
    "s_add_u32 s56, s56, s6\n"
    "s_cmp_ge_u32 s6, 20\n"
    "s_cbranch_scc1 .Lf_idle1\n"
    "s_mul_i32 s57, s6, 52\n"
    "s_lshr_b32 s57, s57, 8\n"
    "s_mul_i32 s58, s57, 5\n"
    "s_sub_u32 s62, s6, s58\n"
    "s_lshl_b32 s61, s5, 2\n"
    "s_add_u32 s61, s61, s57\n"
    "s_lshl_b32 s61, s61, 2\n"
    "s_add_u32 s61, s61, s4\n"
    "s_mul_i32 s57, s61, 20480\n"
    "s_lshl_b32 s58, s62, 8\n"
    "s_add_u32 s57, s57, s58\n"
    "v_lshrrev_b32 v8, 4, v1\n"
    "v_lshlrev_b32 v9, 5, v8\n"
    "v_and_b32 v10, 0xf0, v2\n"
    "v_xor_b32 v9, v9, v10\n"
    "v_mul_u32_u24 v10, 1280, v8\n"
    "v_add3_u32 v16, v9, v10, s57\n"
    "s_mul_i32 s95, s4, 30720\n"
    "v_subrev_u32 v18, 5120, v16\n"
    "v_xor_b32 v17, 0x80, v16\n"
    "s_movk_i32 s68, 4096\n"
    "s_movk_i32 s69, 8192\n"
    "s_movk_i32 s70, 12288\n"
    "s_lshl_b32 s58, s62, 10\n"
    "s_add_u32 m0, s95, 9216\n"
    "s_waitcnt lgkmcnt(0)\n"
    "buffer_load_dwordx4 v2, s[36:39], s58 offen lds\n"
    "s_mov_b32 m0, s95\n"
    "s_nop 0\n"
    "buffer_load_dwordx4 v18, s[28:31], 0 offen lds\n"
    "s_add_u32 m0, m0, 1024\n"
    "s_nop 0\n"
    "buffer_load_dwordx4 v16, s[28:31], 0 offen offset:0 lds\n"
    "buffer_load_dwordx4 v17, s[28:31], s68 offen offset:1024 lds\n"
    "buffer_load_dwordx4 v17, s[28:31], s69 offen offset:2048 lds\n"
    "buffer_load_dwordx4 v16, s[28:31], s70 offen offset:3072 lds\n"
    "buffer_load_dword v253, off, s[8:11], 0 sc1\n"
    "s_add_u32 m0, m0, 4096\n"
    "s_nop 0\n"
    "buffer_load_dwordx4 v16, s[32:35], 0 offen offset:0 lds\n"
    "buffer_load_dwordx4 v16, s[32:35], s68 offen offset:1024 lds\n"
    "buffer_load_dwordx4 v17, s[32:35], s69 offen offset:2048 lds\n"
    "buffer_load_dwordx4 v17, s[32:35], s70 offen offset:3072 lds\n"
    "v_and_b32 v26, 15, v1\n"
    "v_lshrrev_b32 v27, 4, v1\n"
    "v_bfe_u32 v28, v1, 2, 2\n"
    "v_and_b32 v29, 3, v1\n"
    "v_and_b32 v30, 1, v1\n"
    "v_lshrrev_b32 v31, 3, v1\n"
    "v_lshlrev_b32 v32, 1, v28\n"
    "v_xor_b32 v32, v32, v27\n"
    "v_lshl_add_u32 v32, v30, 3, v32\n"
    "v_lshl_add_u32 v32, v32, 4, s95\n"
    "v_lshl_add_u32 v32, v28, 8, v32\n"
    "v_lshl_add_u32 v32, v30, 11, v32\n"
    "v_and_b32 v33, 2, v1\n"
    "v_lshl_add_u32 v20, v33, 2, v32\n"
    "v_xor_b32 v21, 64, v20\n"
    "v_xor_b32 v22, 0x80, v20\n"
    "v_xor_b32 v23, 0xc0, v20\n"
    "v_lshl_add_u32 v32, v31, 5, s95\n"
    "v_lshl_add_u32 v24, v29, 3, v32\n"
    "v_and_b32 v32, 0x70, v2\n"
    "v_lshl_add_u32 v25, v31, 15, v32\n"
    "v_lshl_add_u32 v254, v31, 8, v32\n"
    "s_lshl_b32 s63, s62, 20\n"
    "s_lshl_b32 s57, s61, 8\n"
    "s_add_u32 s63, s63, s57\n"
    "s_add_u32 s63, s63, 1363968\n"
    "s_lshl_b32 s57, s56, 2\n"
    "s_add_u32 s57, s57, s4\n"
    "s_lshl_b32 s58, s57, 1\n"
    "s_and_b32 s58, s58, -4\n"
    "s_load_dword s64, s[48:49], s58\n"
    "s_lshl_b32 s58, s57, 12\n"
    "s_add_u32 m0, s95, 14240\n"
    "s_nop 0\n"
    "buffer_load_dwordx4 v2, s[24:27], s58 offen offset:0 lds\n"
    "buffer_load_dwordx4 v2, s[24:27], s58 offen offset:1024 lds\n"
    "buffer_load_dwordx4 v2, s[24:27], s58 offen offset:2048 lds\n"
    "buffer_load_dwordx4 v2, s[24:27], s58 offen offset:3072 lds\n"
    "s_add_u32 m0, m0, 4132\n"
    "s_nop 0\n"
    "buffer_load_dwordx4 v2, s[24:27], s58 offen offset:0 lds\n"
    "buffer_load_dwordx4 v2, s[24:27], s58 offen offset:1024 lds\n"
    "buffer_load_dwordx4 v2, s[24:27], s58 offen offset:2048 lds\n"
    "buffer_load_dwordx4 v2, s[24:27], s58 offen offset:3072 lds\n"
    "s_lshr_b32 s54, s6, 3\n"
    "s_and_b32 s55, s6, 7\n"
    "s_and_b32 s52, s5, 1\n"
    "s_lshl_b32 s52, s52, 2\n"
    "s_lshr_b32 s53, s5, 1\n"
    "s_add_u32 s97, s52, s53\n"
    "s_and_b32 s57, s6, 3\n"
    "s_add_u32 s52, s52, s57\n"
    "s_and_b32 s98, s6, 4\n"
    "s_add_u32 s98, s98, s54\n"
    "s_lshl_b32 s53, s53, 3\n"
    "s_add_u32 s53, s53, s98\n"
    "s_mul_i32 s98, s98, 5\n"
    "s_lshr_b32 s98, s98, 2\n"
    "s_add_u32 s98, s98, s57\n"
    "s_sub_u32 s57, s98, 10\n"
    "s_cselect_b32 s98, s98, s57\n"
    "s_mul_i32 s76, s55, 48\n"
    "s_mul_i32 s57, s4, 12\n"
    "s_add_u32 s76, s76, s57\n"
    "s_lshl_b32 s78, s76, 7\n"
    "s_add_u32 s78, s78, 4096\n"
    "s_mul_i32 s76, s76, 5120\n"
    "s_add_u32 s76, s76, 11849728\n"
    "s_lshr_b32 s93, s53, 2\n"
    "s_mul_i32 s93, s93, 384\n"
    "s_and_b32 s57, s53, 3\n"
    "s_lshl_b32 s58, s57, 7\n"
    "s_cmp_eq_u32 s57, 3\n"
    "s_cselect_b32 s58, 96, s58\n"
    "s_cselect_b32 s99, 96, 0\n"
    "s_add_u32 s93, s93, s58\n"
    "s_mul_i32 s57, s6, 13\n"
    "s_lshr_b32 s57, s57, 6\n"
    "s_mul_i32 s57, s57, 5\n"
    "s_sub_u32 s98, s6, s57\n"
    "s_mul_i32 s57, s4, 491520\n"
    "s_add_u32 s93, s93, s57\n"
    "s_add_u32 s94, s93, 491520\n"
    "s_mul_i32 s57, s98, 98304\n"
    "s_add_u32 s77, s93, s57\n"
    "s_mul_i32 s95, s4, 30720\n"
    "s_mul_i32 s92, s98, 6144\n"
    "s_add_u32 s92, s92, s95\n"
    "v_add_u32 v3, 0x10000, v2\n"
    "v_lshlrev_b32 v4, 8, v1\n"
    "s_and_b32 s57, s4, 1\n"
    "s_lshl_b32 s57, s57, 11\n"
    "v_add_u32 v6, s57, v2\n"
    "v_xor_b32 v7, 0x800, v6\n"
    "s_lshr_b32 s57, s57, 6\n"
    "s_or_b32 s57, s57, 0x1000\n"
    "v_xor_b32 v15, s57, v2\n"
    "v_cmp_gt_u32 vcc, 8, v1\n"
    "v_mov_b32 v0, 0x70000000\n"
    "s_nop 0\n"
    "v_cndmask_b32 v4, v0, v4, vcc\n"
    "s_lshl_b32 s57, s56, 15\n"
    "s_lshl_b32 s58, s4, 12\n"
    "s_add_u32 s57, s57, s58\n"
    "s_add_u32 s66, s57, 1363968\n"
    "s_add_u32 s67, s66, 16384\n"
    "s_add_u32 s65, s57, 6606848\n"
    "v_add_u32 v32, 1, v26\n"
    "v_and_b32 v9, 1, v32\n"
    "v_bfe_u32 v33, v32, 1, 1\n"
    "v_lshl_add_u32 v33, v9, 1, v33\n"
    "v_lshlrev_b32 v9, 4, v9\n"
    "v_mul_u32_u24 v33, 4128, v33\n"
    "v_lshl_add_u32 v33, v28, 3, v33\n"
    "v_lshl_add_u32 v33, v27, 7, v33\n"
    "s_add_u32 s57, s95, 14216\n"
    "v_add_u32 v8, s57, v33\n"
    "v_add_u32 v5, 2048, v8\n"
    "v_add_u32 v14, s57, v2\n"
    "v_and_b32 v32, 3, v31\n"
    "v_min_u32 v33, 1, v32\n"
    "v_mul_u32_u24 v32, 1032, v32\n"
    "v_and_b32 v34, 7, v1\n"
    "v_min_u32 v34, 6, v34\n"
    "v_add3_u32 v32, v32, v33, v34\n"
    "v_lshl_add_u32 v32, v32, 2, s95\n"
    "v_mov_b32 v34, 0\n"
    "v_mov_b32 v35, 0\n"
    "ds_write_b32 v32, v34 offset:14212\n"
    "s_mul_i32 s57, s4, 4640\n"
    "s_add_u32 s57, s57, 126080\n"
    "v_lshrrev_b32 v32, 5, v1\n"
    "v_mul_u32_u24 v32, 2064, v32\n"
    "v_lshl_add_u32 v32, v1, 3, v32\n"
    "v_add_u32 v32, s57, v32\n"
    "ds_write_b64 v32, v[34:35]\n"
    "v_lshlrev_b32 v32, 2, v27\n"
    "v_sub_u32 v32, v26, v32\n"
    "v_add_u32 v32, 13, v32\n"
    "v_lshl_add_u32 v10, v32, 4, s57\n"
    "v_mul_u32_u24 v32, 4640, v29\n"
    "v_and_b32 v33, 1, v28\n"
    "v_mul_u32_u24 v33, 2320, v33\n"
    "v_lshl_add_u32 v33, v31, 4, v33\n"
    "s_lshl_b32 s57, s4, 8\n"
    "s_add_u32 s57, s57, 126336\n"
    "v_add3_u32 v13, v32, v33, s57\n"
    "s_mul_i32 s57, s4, 2368\n"
    "s_add_u32 s57, s57, 144640\n"
    "v_mul_u32_u24 v32, 144, v27\n"
    "v_lshl_add_u32 v32, v26, 3, v32\n"
    "v_add_u32 v11, s57, v32\n"
    "s_mul_i32 s57, s4, 576\n"
    "s_add_u32 s57, s57, 144640\n"
    "v_mul_u32_u24 v32, 2368, v28\n"
    "v_mul_u32_u24 v33, 144, v29\n"
    "v_lshl_add_u32 v33, v27, 3, v33\n"
    "v_add3_u32 v12, v32, v33, s57\n"
    "v_lshlrev_b32 v36, 3, v1\n"
    "v_and_b32 v36, 8, v36\n"
    "v_add_u32 v36, 122880, v36\n"
    "ds_write_b64 v36, v[34:35]\n"
    "s_waitcnt vmcnt(18)\n"
    "ds_read_b64 v[100:101], v24 offset:9216\n"
    "ds_read_b64 v[102:103], v24 offset:9472\n"
    "ds_read_b64 v[104:105], v24 offset:9728\n"
    "ds_read_b64 v[106:107], v24 offset:9984\n"
    "s_waitcnt lgkmcnt(0)\n"
    "v_alignbit_b32 v100, v100, v100, 16\n"
    "v_alignbit_b32 v101, v101, v101, 16\n"
    "v_alignbit_b32 v102, v102, v102, 16\n"
    "v_alignbit_b32 v103, v103, v103, 16\n"
    "v_alignbit_b32 v104, v104, v104, 16\n"
    "v_alignbit_b32 v105, v105, v105, 16\n"
    "v_alignbit_b32 v106, v106, v106, 16\n"
    "v_alignbit_b32 v107, v107, v107, 16\n"
    "s_waitcnt vmcnt(13)\n"
    "ds_read_b64_tr_b16 v[36:37], v20 offset:0\n"
    "ds_read_b64_tr_b16 v[38:39], v20 offset:1024\n"
    "ds_read_b64_tr_b16 v[40:41], v22 offset:2048\n"
    "ds_read_b64_tr_b16 v[42:43], v21 offset:0\n"
    "ds_read_b64_tr_b16 v[44:45], v21 offset:1024\n"
    "ds_read_b64_tr_b16 v[46:47], v23 offset:2048\n"
    "ds_read_b64_tr_b16 v[48:49], v22 offset:0\n"
    "ds_read_b64_tr_b16 v[50:51], v22 offset:1024\n"
    "ds_read_b64_tr_b16 v[52:53], v20 offset:2048\n"
    "ds_read_b64_tr_b16 v[54:55], v23 offset:0\n"
    "ds_read_b64_tr_b16 v[56:57], v23 offset:1024\n"
    "ds_read_b64_tr_b16 v[58:59], v21 offset:2048\n"
    "s_waitcnt lgkmcnt(11)\n"
    "v_dot2_f32_bf16 v109, v37, v101, 0\n"
    "s_waitcnt lgkmcnt(10)\n"
    "v_dot2_f32_bf16 v111, v38, v101, 0\n"
    "v_dot2_f32_bf16 v113, v39, v101, 0\n"
    "s_waitcnt lgkmcnt(9)\n"
    "v_dot2_f32_bf16 v115, v40, v101, 0\n"
    "v_alignbit_b32 v60, v37, v36, 16\n"
    "v_alignbit_b32 v61, v38, v37, 16\n"
    "v_alignbit_b32 v62, v39, v38, 16\n"
    "v_alignbit_b32 v63, v40, v39, 16\n"
    "v_alignbit_b32 v64, v41, v40, 16\n"
    "v_dot2_f32_bf16 v109, v38, v100, v109\n"
    "v_dot2_f32_bf16 v111, v39, v100, v111\n"
    "v_dot2_f32_bf16 v113, v40, v100, v113\n"
    "v_dot2_f32_bf16 v115, v41, v100, v115\n"
    "v_dot2_f32_bf16 v108, v60, v101, 0\n"
    "v_dot2_f32_bf16 v110, v61, v101, 0\n"
    "v_dot2_f32_bf16 v112, v62, v101, 0\n"
    "v_dot2_f32_bf16 v114, v63, v101, 0\n"
    "v_dot2_f32_bf16 v108, v61, v100, v108\n"
    "v_dot2_f32_bf16 v110, v62, v100, v110\n"
    "v_dot2_f32_bf16 v112, v63, v100, v112\n"
    "v_dot2_f32_bf16 v114, v64, v100, v114\n"
    "s_waitcnt lgkmcnt(8)\n"
    "v_dot2_f32_bf16 v117, v43, v103, 0\n"
    "s_waitcnt lgkmcnt(7)\n"
    "v_dot2_f32_bf16 v119, v44, v103, 0\n"
    "v_dot2_f32_bf16 v121, v45, v103, 0\n"
    "s_waitcnt lgkmcnt(6)\n"
    "v_dot2_f32_bf16 v123, v46, v103, 0\n"
    "v_alignbit_b32 v66, v43, v42, 16\n"
    "v_alignbit_b32 v67, v44, v43, 16\n"
    "v_alignbit_b32 v68, v45, v44, 16\n"
    "v_alignbit_b32 v69, v46, v45, 16\n"
    "v_alignbit_b32 v70, v47, v46, 16\n"
    "v_dot2_f32_bf16 v117, v44, v102, v117\n"
    "v_dot2_f32_bf16 v119, v45, v102, v119\n"
    "v_dot2_f32_bf16 v121, v46, v102, v121\n"
    "v_dot2_f32_bf16 v123, v47, v102, v123\n"
    "v_dot2_f32_bf16 v116, v66, v103, 0\n"
    "v_dot2_f32_bf16 v118, v67, v103, 0\n"
    "v_dot2_f32_bf16 v120, v68, v103, 0\n"
    "v_dot2_f32_bf16 v122, v69, v103, 0\n"
    "v_dot2_f32_bf16 v116, v67, v102, v116\n"
    "v_dot2_f32_bf16 v118, v68, v102, v118\n"
    "v_dot2_f32_bf16 v120, v69, v102, v120\n"
    "v_dot2_f32_bf16 v122, v70, v102, v122\n"
    "s_waitcnt lgkmcnt(5)\n"
    "v_dot2_f32_bf16 v125, v49, v105, 0\n"
    "s_waitcnt lgkmcnt(4)\n"
    "v_dot2_f32_bf16 v127, v50, v105, 0\n"
    "v_dot2_f32_bf16 v129, v51, v105, 0\n"
    "s_waitcnt lgkmcnt(3)\n"
    "v_dot2_f32_bf16 v131, v52, v105, 0\n"
    "v_alignbit_b32 v72, v49, v48, 16\n"
    "v_alignbit_b32 v73, v50, v49, 16\n"
    "v_alignbit_b32 v74, v51, v50, 16\n"
    "v_alignbit_b32 v75, v52, v51, 16\n"
    "v_alignbit_b32 v76, v53, v52, 16\n"
    "v_dot2_f32_bf16 v125, v50, v104, v125\n"
    "v_dot2_f32_bf16 v127, v51, v104, v127\n"
    "v_dot2_f32_bf16 v129, v52, v104, v129\n"
    "v_dot2_f32_bf16 v131, v53, v104, v131\n"
    "v_dot2_f32_bf16 v124, v72, v105, 0\n"
    "v_dot2_f32_bf16 v126, v73, v105, 0\n"
    "v_dot2_f32_bf16 v128, v74, v105, 0\n"
    "v_dot2_f32_bf16 v130, v75, v105, 0\n"
    "v_dot2_f32_bf16 v124, v73, v104, v124\n"
    "v_dot2_f32_bf16 v126, v74, v104, v126\n"
    "v_dot2_f32_bf16 v128, v75, v104, v128\n"
    "v_dot2_f32_bf16 v130, v76, v104, v130\n"
    "s_waitcnt lgkmcnt(2)\n"
    "v_dot2_f32_bf16 v133, v55, v107, 0\n"
    "s_waitcnt lgkmcnt(1)\n"
    "v_dot2_f32_bf16 v135, v56, v107, 0\n"
    "v_dot2_f32_bf16 v137, v57, v107, 0\n"
    "s_waitcnt lgkmcnt(0)\n"
    "v_dot2_f32_bf16 v139, v58, v107, 0\n"
    "v_alignbit_b32 v78, v55, v54, 16\n"
    "v_alignbit_b32 v79, v56, v55, 16\n"
    "v_alignbit_b32 v80, v57, v56, 16\n"
    "v_alignbit_b32 v81, v58, v57, 16\n"
    "v_alignbit_b32 v82, v59, v58, 16\n"
    "v_dot2_f32_bf16 v133, v56, v106, v133\n"
    "v_dot2_f32_bf16 v135, v57, v106, v135\n"
    "v_dot2_f32_bf16 v137, v58, v106, v137\n"
    "v_dot2_f32_bf16 v139, v59, v106, v139\n"
    "v_dot2_f32_bf16 v132, v78, v107, 0\n"
    "v_dot2_f32_bf16 v134, v79, v107, 0\n"
    "v_dot2_f32_bf16 v136, v80, v107, 0\n"
    "v_dot2_f32_bf16 v138, v81, v107, 0\n"
    "v_dot2_f32_bf16 v132, v79, v106, v132\n"
    "v_dot2_f32_bf16 v134, v80, v106, v134\n"
    "v_dot2_f32_bf16 v136, v81, v106, v136\n"
    "v_dot2_f32_bf16 v138, v82, v106, v138\n"
    "s_waitcnt vmcnt(8)\n"
    "ds_read_b64_tr_b16 v[84:85], v20 offset:5120\n"
    "ds_read_b64_tr_b16 v[86:87], v20 offset:6144\n"
    "ds_read_b64_tr_b16 v[88:89], v21 offset:5120\n"
    "ds_read_b64_tr_b16 v[90:91], v21 offset:6144\n"
    "ds_read_b64_tr_b16 v[92:93], v22 offset:5120\n"
    "ds_read_b64_tr_b16 v[94:95], v22 offset:6144\n"
    "ds_read_b64_tr_b16 v[96:97], v23 offset:5120\n"
    "ds_read_b64_tr_b16 v[98:99], v23 offset:6144\n"
    "v_readfirstlane_b32 s50, v253\n"
    "s_nop 0\n"
    "s_mul_i32 s50, s50, 0x19660d\n"
    "s_add_u32 s50, s50, 0x3c6ef35f\n"
    "s_not_b32 s51, s50\n"
    "v_mov_b32 v177, s50\n"
    "v_mov_b32 v179, s50\n"
    "v_mov_b32 v181, s50\n"
    "v_mov_b32 v183, s50\n"
    "v_mov_b32 v185, s50\n"
    "v_mov_b32 v187, s50\n"
    "v_mov_b32 v189, s50\n"
    "v_mov_b32 v191, s50\n"
    "v_mov_b32 v193, s50\n"
    "v_mov_b32 v195, s50\n"
    "v_mov_b32 v197, s50\n"
    "v_mov_b32 v199, s50\n"
    "v_mov_b32 v201, s50\n"
    "v_mov_b32 v203, s50\n"
    "v_mov_b32 v205, s50\n"
    "v_mov_b32 v207, s50\n"
    "s_waitcnt lgkmcnt(7)\n"
    "v_lshlrev_b32 v140, 16, v84\n"
    "v_and_b32 v141, 0xffff0000, v84\n"
    "v_lshlrev_b32 v142, 16, v85\n"
    "v_and_b32 v143, 0xffff0000, v85\n"
    "s_waitcnt lgkmcnt(6)\n"
    "v_lshlrev_b32 v144, 16, v86\n"
    "v_and_b32 v145, 0xffff0000, v86\n"
    "v_lshlrev_b32 v146, 16, v87\n"
    "v_and_b32 v147, 0xffff0000, v87\n"
    "v_pk_mul_f32 v[108:109], v[108:109], v[140:141]\n"
    "v_pk_mul_f32 v[110:111], v[110:111], v[142:143]\n"
    "v_pk_mul_f32 v[112:113], v[112:113], v[144:145]\n"
    "v_pk_mul_f32 v[114:115], v[114:115], v[146:147]\n"
    "v_cvt_pk_bf16_f32 v176, v115, v114\n"
    "v_cvt_pk_bf16_f32 v178, v111, v110\n"
    "v_cvt_pk_bf16_f32 v180, v113, v112\n"
    "v_cvt_pk_bf16_f32 v182, v109, v108\n"
    "buffer_store_dwordx4 v[176:179], v25, s[8:11], s63 offen offset:0 sc1\n"
    "buffer_store_dwordx4 v[180:183], v25, s[8:11], s63 offen offset:128 sc1\n"
    "s_add_u32 s63, s63, 262144\n"
    "s_waitcnt lgkmcnt(5)\n"
    "v_lshlrev_b32 v140, 16, v88\n"
    "v_and_b32 v141, 0xffff0000, v88\n"
    "v_lshlrev_b32 v142, 16, v89\n"
    "v_and_b32 v143, 0xffff0000, v89\n"
    "s_waitcnt lgkmcnt(4)\n"
    "v_lshlrev_b32 v144, 16, v90\n"
    "v_and_b32 v145, 0xffff0000, v90\n"
    "v_lshlrev_b32 v146, 16, v91\n"
    "v_and_b32 v147, 0xffff0000, v91\n"
    "v_pk_mul_f32 v[116:117], v[116:117], v[140:141]\n"
    "v_pk_mul_f32 v[118:119], v[118:119], v[142:143]\n"
    "v_pk_mul_f32 v[120:121], v[120:121], v[144:145]\n"
    "v_pk_mul_f32 v[122:123], v[122:123], v[146:147]\n"
    "v_cvt_pk_bf16_f32 v184, v123, v122\n"
    "v_cvt_pk_bf16_f32 v186, v119, v118\n"
    "v_cvt_pk_bf16_f32 v188, v121, v120\n"
    "v_cvt_pk_bf16_f32 v190, v117, v116\n"
    "buffer_store_dwordx4 v[184:187], v25, s[8:11], s63 offen offset:0 sc1\n"
    "buffer_store_dwordx4 v[188:191], v25, s[8:11], s63 offen offset:128 sc1\n"
    "s_add_u32 s63, s63, 262144\n"
    "s_waitcnt lgkmcnt(3)\n"
    "v_lshlrev_b32 v140, 16, v92\n"
    "v_and_b32 v141, 0xffff0000, v92\n"
    "v_lshlrev_b32 v142, 16, v93\n"
    "v_and_b32 v143, 0xffff0000, v93\n"
    "s_waitcnt lgkmcnt(2)\n"
    "v_lshlrev_b32 v144, 16, v94\n"
    "v_and_b32 v145, 0xffff0000, v94\n"
    "v_lshlrev_b32 v146, 16, v95\n"
    "v_and_b32 v147, 0xffff0000, v95\n"
    "v_pk_mul_f32 v[124:125], v[124:125], v[140:141]\n"
    "v_pk_mul_f32 v[126:127], v[126:127], v[142:143]\n"
    "v_pk_mul_f32 v[128:129], v[128:129], v[144:145]\n"
    "v_pk_mul_f32 v[130:131], v[130:131], v[146:147]\n"
    "v_cvt_pk_bf16_f32 v192, v131, v130\n"
    "v_cvt_pk_bf16_f32 v194, v127, v126\n"
    "v_cvt_pk_bf16_f32 v196, v129, v128\n"
    "v_cvt_pk_bf16_f32 v198, v125, v124\n"
    "buffer_store_dwordx4 v[192:195], v25, s[8:11], s63 offen offset:0 sc1\n"
    "buffer_store_dwordx4 v[196:199], v25, s[8:11], s63 offen offset:128 sc1\n"
    "s_add_u32 s63, s63, 262144\n"
    "s_waitcnt lgkmcnt(1)\n"
    "v_lshlrev_b32 v140, 16, v96\n"
    "v_and_b32 v141, 0xffff0000, v96\n"
    "v_lshlrev_b32 v142, 16, v97\n"
    "v_and_b32 v143, 0xffff0000, v97\n"
    "s_waitcnt lgkmcnt(0)\n"
    "v_lshlrev_b32 v144, 16, v98\n"
    "v_and_b32 v145, 0xffff0000, v98\n"
    "v_lshlrev_b32 v146, 16, v99\n"
    "v_and_b32 v147, 0xffff0000, v99\n"
    "v_pk_mul_f32 v[132:133], v[132:133], v[140:141]\n"
    "v_pk_mul_f32 v[134:135], v[134:135], v[142:143]\n"
    "v_pk_mul_f32 v[136:137], v[136:137], v[144:145]\n"
    "v_pk_mul_f32 v[138:139], v[138:139], v[146:147]\n"
    "v_cvt_pk_bf16_f32 v200, v139, v138\n"
    "v_cvt_pk_bf16_f32 v202, v135, v134\n"
    "v_cvt_pk_bf16_f32 v204, v137, v136\n"
    "v_cvt_pk_bf16_f32 v206, v133, v132\n"
    "buffer_store_dwordx4 v[200:203], v25, s[8:11], s63 offen offset:0 sc1\n"
    "buffer_store_dwordx4 v[204:207], v25, s[8:11], s63 offen offset:128 sc1\n"
    "s_waitcnt vmcnt(8)\n"
    "ds_read_b64 v[80:81], v14 offset:16\n"
    "ds_read_b128 v[82:85], v14 offset:24\n"
    "ds_read_b64 v[86:87], v14 offset:1040\n"
    "ds_read_b128 v[88:91], v14 offset:1048\n"
    "ds_read_b64 v[92:93], v14 offset:2064\n"
    "ds_read_b128 v[94:97], v14 offset:2072\n"
    "ds_read_b64 v[98:99], v14 offset:3088\n"
    "ds_read_b128 v[100:103], v14 offset:3096\n"
    "v_mov_b32 v252, s50\n"
    "v_mov_b32 v253, s51\n"
    "v_add_u32 v32, 0, v1\n"
    "v_mul_u32_u24 v32, 171, v32\n"
    "v_lshrrev_b32 v32, 10, v32\n"
    "v_mul_u32_u24 v33, 2976, v32\n"
    "v_add_u32 v169, v33, v2\n"
    "v_and_b32 v32, 4, v32\n"
    "v_mul_u32_u24 v32, 6144, v32\n"
    "v_sub_u32 v32, 12288, v32\n"
    "v_add_u32 v172, v169, v32\n"
    "v_add_u32 v32, 64, v1\n"
    "v_mul_u32_u24 v32, 171, v32\n"
    "v_lshrrev_b32 v32, 10, v32\n"
    "v_mul_u32_u24 v33, 2976, v32\n"
    "v_add_u32 v170, v33, v2\n"
    "v_and_b32 v32, 4, v32\n"
    "v_mul_u32_u24 v32, 6144, v32\n"
    "v_sub_u32 v32, 12288, v32\n"
    "v_add_u32 v173, v170, v32\n"
    "v_add_u32 v32, 128, v1\n"
    "v_mul_u32_u24 v32, 171, v32\n"
    "v_lshrrev_b32 v32, 10, v32\n"
    "v_mul_u32_u24 v33, 2976, v32\n"
    "v_add_u32 v171, v33, v2\n"
    "v_and_b32 v32, 4, v32\n"
    "v_mul_u32_u24 v32, 6144, v32\n"
    "v_sub_u32 v32, 12288, v32\n"
    "v_add_u32 v174, v171, v32\n"
    "v_lshlrev_b32 v175, 7, v1\n"
    "v_cmp_gt_u32 vcc, 12, v1\n"
    "v_mov_b32 v0, 0x70000000\n"
    "s_nop 0\n"
    "v_cndmask_b32 v175, v0, v175, vcc\n"
    "s_lshr_b32 s58, s64, 16\n"
    "s_bitcmp1_b32 s4, 0\n"
    "s_cselect_b32 s64, s58, s64\n"
    "s_and_b32 s64, s64, 0xffff\n"
    "v_sub_u32 v32, 15, v1\n"
    "v_lshrrev_b32 v32, 1, v32\n"
    "v_lshlrev_b32 v33, v9, s64\n"
    "v_cmp_eq_u32 vcc, 0, v32\n"
    "s_nop 1\n"
    "v_cndmask_b32 v144, 0, v33, vcc\n"
    "v_cmp_eq_u32 vcc, 1, v32\n"
    "s_nop 1\n"
    "v_cndmask_b32 v145, 0, v33, vcc\n"
    "v_cmp_eq_u32 vcc, 2, v32\n"
    "s_nop 1\n"
    "v_cndmask_b32 v146, 0, v33, vcc\n"
    "v_cmp_eq_u32 vcc, 3, v32\n"
    "s_nop 1\n"
    "v_cndmask_b32 v147, 0, v33, vcc\n"
    "v_cmp_eq_u32 vcc, 4, v32\n"
    "s_nop 1\n"
    "v_cndmask_b32 v148, 0, v33, vcc\n"
    "v_cmp_eq_u32 vcc, 5, v32\n"
    "s_nop 1\n"
    "v_cndmask_b32 v149, 0, v33, vcc\n"
    "v_cmp_eq_u32 vcc, 6, v32\n"
    "s_nop 1\n"
    "v_cndmask_b32 v150, 0, v33, vcc\n"
    "v_cmp_eq_u32 vcc, 7, v32\n"
    "s_nop 1\n"
    "v_cndmask_b32 v151, 0, v33, vcc\n"
    "s_waitcnt lgkmcnt(7)\n"
    "v_alignbit_b32 v48, v81, v80, 16\n"
    "s_waitcnt lgkmcnt(6)\n"
    "v_alignbit_b32 v49, v82, v81, 16\n"
    "v_alignbit_b32 v50, v83, v82, 16\n"
    "v_alignbit_b32 v51, v84, v83, 16\n"
    "v_alignbit_b32 v52, v82, v81, 16\n"
    "v_alignbit_b32 v53, v83, v82, 16\n"
    "v_alignbit_b32 v54, v84, v83, 16\n"
    "v_alignbit_b32 v55, v85, v84, 16\n"
    "ds_write_b128 v14, v[48:51] offset:8280\n"
    "ds_write_b128 v14, v[52:55] offset:12408\n"
    "s_waitcnt lgkmcnt(7)\n"
    "v_alignbit_b32 v56, v87, v86, 16\n"
    "s_waitcnt lgkmcnt(6)\n"
    "v_alignbit_b32 v57, v88, v87, 16\n"
    "v_alignbit_b32 v58, v89, v88, 16\n"
    "v_alignbit_b32 v59, v90, v89, 16\n"
    "v_alignbit_b32 v60, v88, v87, 16\n"
    "v_alignbit_b32 v61, v89, v88, 16\n"
    "v_alignbit_b32 v62, v90, v89, 16\n"
    "v_alignbit_b32 v63, v91, v90, 16\n"
    "ds_write_b128 v14, v[56:59] offset:9304\n"
    "ds_write_b128 v14, v[60:63] offset:13432\n"
    "s_waitcnt lgkmcnt(7)\n"
    "v_alignbit_b32 v64, v93, v92, 16\n"
    "s_waitcnt lgkmcnt(6)\n"
    "v_alignbit_b32 v65, v94, v93, 16\n"
    "v_alignbit_b32 v66, v95, v94, 16\n"
    "v_alignbit_b32 v67, v96, v95, 16\n"
    "v_alignbit_b32 v68, v94, v93, 16\n"
    "v_alignbit_b32 v69, v95, v94, 16\n"
    "v_alignbit_b32 v70, v96, v95, 16\n"
    "v_alignbit_b32 v71, v97, v96, 16\n"
    "ds_write_b128 v14, v[64:67] offset:10328\n"
    "ds_write_b128 v14, v[68:71] offset:14456\n"
    "s_waitcnt lgkmcnt(7)\n"
    "v_alignbit_b32 v72, v99, v98, 16\n"
    "s_waitcnt lgkmcnt(6)\n"
    "v_alignbit_b32 v73, v100, v99, 16\n"
    "v_alignbit_b32 v74, v101, v100, 16\n"
    "v_alignbit_b32 v75, v102, v101, 16\n"
    "v_alignbit_b32 v76, v100, v99, 16\n"
    "v_alignbit_b32 v77, v101, v100, 16\n"
    "v_alignbit_b32 v78, v102, v101, 16\n"
    "v_alignbit_b32 v79, v103, v102, 16\n"
    "ds_write_b128 v14, v[72:75] offset:11352\n"
    "ds_write_b128 v14, v[76:79] offset:15480\n"
    "ds_read2_b64 v[16:19], v8 offset0:0 offset1:1\n"
    "ds_read2_b64 v[48:51], v8 offset0:64 offset1:65\n"
    "ds_read2_b64 v[80:83], v8 offset0:128 offset1:129\n"
    "ds_read2_b64 v[112:115], v8 offset0:192 offset1:193\n"
    "ds_read2_b64 v[20:23], v8 offset0:2 offset1:3\n"
    "ds_read2_b64 v[52:55], v8 offset0:66 offset1:67\n"
    "ds_read2_b64 v[84:87], v8 offset0:130 offset1:131\n"
    "ds_read2_b64 v[116:119], v8 offset0:194 offset1:195\n"
    "ds_read2_b64 v[24:27], v8 offset0:4 offset1:5\n"
    "ds_read2_b64 v[56:59], v8 offset0:68 offset1:69\n"
    "ds_read2_b64 v[88:91], v8 offset0:132 offset1:133\n"
    "ds_read2_b64 v[120:123], v8 offset0:196 offset1:197\n"
    "ds_read2_b64 v[28:31], v8 offset0:6 offset1:7\n"
    "ds_read2_b64 v[60:63], v8 offset0:70 offset1:71\n"
    "ds_read2_b64 v[92:95], v8 offset0:134 offset1:135\n"
    "ds_read2_b64 v[124:127], v8 offset0:198 offset1:199\n"
    "ds_read2_b64 v[32:35], v8 offset0:8 offset1:9\n"
    "ds_read2_b64 v[64:67], v8 offset0:72 offset1:73\n"
    "ds_read2_b64 v[96:99], v8 offset0:136 offset1:137\n"
    "ds_read2_b64 v[128:131], v8 offset0:200 offset1:201\n"
    "ds_read2_b64 v[36:39], v8 offset0:10 offset1:11\n"
    "ds_read2_b64 v[68:71], v8 offset0:74 offset1:75\n"
    "ds_read2_b64 v[100:103], v8 offset0:138 offset1:139\n"
    "ds_read2_b64 v[132:135], v8 offset0:202 offset1:203\n"
    "ds_read2_b64 v[40:43], v8 offset0:12 offset1:13\n"
    "ds_read2_b64 v[72:75], v8 offset0:76 offset1:77\n"
    "ds_read2_b64 v[104:107], v8 offset0:140 offset1:141\n"
    "ds_read2_b64 v[136:139], v8 offset0:204 offset1:205\n"
    "ds_read2_b64 v[44:47], v8 offset0:14 offset1:15\n"
    "ds_read2_b64 v[76:79], v8 offset0:78 offset1:79\n"
    "ds_read2_b64 v[108:111], v8 offset0:142 offset1:143\n"
    "ds_read2_b64 v[140:143], v8 offset0:206 offset1:207\n"
    "s_sleep 8\n"
    "buffer_load_dwordx4 v[208:211], v254, s[8:11], s66 offen offset:0 sc1\n"
    "buffer_load_dwordx4 v[212:215], v254, s[8:11], s66 offen offset:128 sc1\n"
    "buffer_load_dwordx4 v[216:219], v254, s[8:11], s66 offen offset:2048 sc1\n"
    "buffer_load_dwordx4 v[220:223], v254, s[8:11], s66 offen offset:2176 sc1\n"
    "ds_read2_b64 a[96:99], v5 offset0:0 offset1:1\n"
    "ds_read2_b64 a[100:103], v5 offset0:64 offset1:65\n"
    "ds_read2_b64 a[104:107], v5 offset0:128 offset1:129\n"
    "ds_read2_b64 a[108:111], v5 offset0:192 offset1:193\n"
    "ds_read2_b64 a[112:115], v5 offset0:2 offset1:3\n"
    "ds_read2_b64 a[116:119], v5 offset0:66 offset1:67\n"
    "ds_read2_b64 a[120:123], v5 offset0:130 offset1:131\n"
    "ds_read2_b64 a[124:127], v5 offset0:194 offset1:195\n"
    "ds_read2_b64 a[128:131], v5 offset0:4 offset1:5\n"
    "ds_read2_b64 a[132:135], v5 offset0:68 offset1:69\n"
    "ds_read2_b64 a[136:139], v5 offset0:132 offset1:133\n"
    "ds_read2_b64 a[140:143], v5 offset0:196 offset1:197\n"
    "ds_read2_b64 a[144:147], v5 offset0:6 offset1:7\n"
    "ds_read2_b64 a[148:151], v5 offset0:70 offset1:71\n"
    "ds_read2_b64 a[152:155], v5 offset0:134 offset1:135\n"
    "ds_read2_b64 a[156:159], v5 offset0:198 offset1:199\n"
    "ds_read2_b64 a[160:163], v5 offset0:8 offset1:9\n"
    "ds_read2_b64 a[164:167], v5 offset0:72 offset1:73\n"
    "ds_read2_b64 a[168:171], v5 offset0:136 offset1:137\n"
    "ds_read2_b64 a[172:175], v5 offset0:200 offset1:201\n"
    "ds_read2_b64 a[176:179], v5 offset0:10 offset1:11\n"
    "ds_read2_b64 a[180:183], v5 offset0:74 offset1:75\n"
    "ds_read2_b64 a[184:187], v5 offset0:138 offset1:139\n"
    "ds_read2_b64 a[188:191], v5 offset0:202 offset1:203\n"
    "ds_read2_b64 a[192:195], v5 offset0:12 offset1:13\n"
    "ds_read2_b64 a[196:199], v5 offset0:76 offset1:77\n"
    "ds_read2_b64 a[200:203], v5 offset0:140 offset1:141\n"
    "ds_read2_b64 a[204:207], v5 offset0:204 offset1:205\n"
    "ds_read2_b64 a[208:211], v5 offset0:14 offset1:15\n"
    "ds_read2_b64 a[212:215], v5 offset0:78 offset1:79\n"
    "ds_read2_b64 a[216:219], v5 offset0:142 offset1:143\n"
    "ds_read2_b64 a[220:223], v5 offset0:206 offset1:207\n"
    "s_lshl_b32 s58, s97, 2\n"
    "s_add_u32 s58, s58, s54\n"
    "s_lshr_b32 s59, s4, 1\n"
    "s_and_b32 s60, s4, 1\n"
    "s_lshl_b32 s28, s58, 2\n"
    "s_add_u32 s28, s28, s4\n"
    "s_mul_i32 s29, s28, 24576\n"
    "s_lshl_b32 s28, s28, 11\n"
    "s_add_u32 s28, s28, 315392\n"
    "s_lshl_b32 s61, s59, 10\n"
    "s_add_u32 s29, s29, s61\n"
    "s_add_u32 s29, s29, 20140032\n"
    "s_lshl_b32 s61, s59, 7\n"
    "s_mul_i32 s30, s58, 40960\n"
    "s_mul_i32 s62, s55, 5120\n"
    "s_add_u32 s30, s30, s62\n"
    "s_add_u32 s30, s30, s61\n"
    "s_add_u32 s30, s30, 13815808\n"
    "s_lshl_b32 s62, s60, 9\n"
    "s_add_u32 s31, s30, s62\n"
    "s_add_u32 s31, s31, 4096\n"
    "s_lshl_b32 s63, s60, 11\n"
    "s_add_u32 s30, s30, s63\n"
    "s_mul_i32 s32, s55, 655360\n"
    "s_lshl_b32 s63, s58, 9\n"
    "s_and_b32 s63, s63, -1\n"
    "s_add_u32 s32, s32, s63\n"
    "s_add_u32 s32, s32, s61\n"
    "s_add_u32 s32, s32, 6606848\n"
    "s_lshl_b32 s63, s60, 16\n"
    "s_add_u32 s33, s32, s63\n"
    "s_add_u32 s33, s33, 524288\n"
    "s_lshl_b32 s63, s60, 18\n"
    "s_add_u32 s32, s32, s63\n"
    "s_lshl_b32 s35, s58, 13\n"
    "s_add_u32 s35, s35, s61\n"
    "s_add_u32 s35, s35, s61\n"
    "s_add_u32 s35, s35, 19582976\n"
    "s_lshl_b32 s63, s55, 10\n"
    "s_add_u32 s34, s35, s63\n"
    "s_add_u32 s34, s34, s62\n"
    "s_lshl_b32 s36, s58, 12\n"
    "s_lshl_b32 s63, s55, 9\n"
    "s_add_u32 s36, s36, s63\n"
    "s_add_u32 s36, s36, s61\n"
    "s_lshl_b32 s63, s60, 8\n"
    "s_add_u32 s36, s36, s63\n"
    "s_add_u32 s36, s36, 19320832\n"
    "s_lshl_b32 s37, s58, 3\n"
    "s_add_u32 s37, s37, s55\n"
    "s_lshl_b32 s37, s37, 8\n"
    "s_add_u32 s37, s37, 184320\n"
    "s_lshl_b32 s38, s58, 2\n"
    "s_add_u32 s38, s38, s59\n"
    "s_mul_i32 s38, s38, 10240\n"
    "s_mul_i32 s63, s55, 160\n"
    "s_add_u32 s38, s38, s63\n"
    "s_lshl_b32 s63, s60, 4\n"
    "s_add_u32 s39, s38, s63\n"
    "s_add_u32 s39, s39, 128\n"
    "s_lshl_b32 s63, s60, 6\n"
    "s_add_u32 s38, s38, s63\n"
    "s_lshl_b32 s96, s58, 9\n"
    "s_add_u32 s96, s96, s61\n"
    "s_add_u32 s96, s96, 20107264\n"
    "v_and_b32 v159, 0x70, v2\n"
    "v_and_b32 v155, 8, v1\n"
    "v_lshl_add_u32 v158, v155, 5, v159\n"
    "v_add_u32 v155, v155, v159\n"
    "v_lshrrev_b32 v159, 4, v1\n"
    "v_lshl_add_u32 v152, v159, 9, v158\n"
    "v_lshl_add_u32 v153, v159, 16, v158\n"
    "v_mul_u32_u24 v157, 80, v158\n"
    "v_lshl_add_u32 v156, v159, 4, v157\n"
    "v_lshl_add_u32 v157, v159, 2, v157\n"
    "v_and_b32 v154, 1, v159\n"
    "v_lshl_add_u32 v154, v154, 3, v158\n"
    "v_lshrrev_b32 v159, 1, v159\n"
    "v_lshl_add_u32 v154, v159, 15, v154\n"
    "s_add_u32 s64, s96, 0\n"
    "s_add_u32 s68, s32, 0\n"
    "s_add_u32 s69, s32, 32768\n"
    "s_add_u32 s70, s33, 0\n"
    "s_lshr_b32 s57, s4, 1\n"
    "s_lshl_b32 s57, s57, 8\n"
    "s_and_b32 s58, s4, 1\n"
    "s_lshl_b32 s58, s58, 3\n"
    "s_add_u32 s57, s57, s58\n"
    "s_add_u32 s57, s57, 123008\n"
    "s_movk_i32 s59, 384\n"
    "v_bfe_u32 v160, v1, 4, 1\n"
    "v_and_b32 v168, 0xf0, v2\n"
    "v_lshl_add_u32 v168, v160, 10, v168\n"
    "v_lshrrev_b32 v162, 5, v1\n"
    "v_lshl_add_u32 v164, v162, 9, v168\n"
    "v_add_u32 v164, s57, v164\n"
    "v_lshl_add_u32 v168, v162, 8, v168\n"
    "v_add_u32 v168, s92, v168\n"
    "v_bfe_u32 v161, v1, 2, 2\n"
    "v_lshl_add_u32 v162, v162, 3, v161\n"
    "v_mul_u32_u24 v162, 96, v162\n"
    "v_and_b32 v163, 3, v1\n"
    "v_lshlrev_b32 v163, 3, v163\n"
    "v_add3_u32 v162, v162, v163, s92\n"
    "v_mul_u32_u24 v166, 3456, v160\n"
    "v_mul_u32_u24 v167, 2688, v160\n"
    "v_add_u32 v166, v166, v162\n"
    "v_add3_u32 v167, v167, v162, s59\n"
    "v_xor_b32 v160, v160, v161\n"
    "v_and_b32 v160, 1, v160\n"
    "v_add_u32 v160, -1, v160\n"
    "v_and_b32 v160, 0x3f803f80, v160\n"
    "v_mov_b32 v161, v160\n"
    "v_mov_b32 v162, v160\n"
    "v_mov_b32 v163, v160\n"
    "v_lshrrev_b32 v247, 3, v1\n"
    "v_mul_u32_u24 v247, 24, v247\n"
    "v_and_b32 v248, 7, v1\n"
    "v_min_u32 v248, 5, v248\n"
    "v_add_lshl_u32 v247, v247, v248, 7\n"
    "s_lshl_b32 s79, s6, 2\n"
    "s_add_u32 s79, s79, s4\n"
    "s_lshl_b32 s79, s79, 5\n"
    "s_lshr_b32 s71, s5, 1\n"
    "s_add_u32 s79, s79, s71\n"
    "s_mulk_i32 s79, 768\n"
    "s_waitcnt vmcnt(3)\n"
    "v_xor_b32 v0, s50, v209\n"
    "v_bitop3_b32 v0, v0, v211, s50 bitop3:0xf6\n"
    "s_waitcnt vmcnt(2)\n"
    "v_bitop3_b32 v0, v0, v213, s50 bitop3:0xf6\n"
    "v_bitop3_b32 v0, v0, v215, s50 bitop3:0xf6\n"
    "s_waitcnt vmcnt(1)\n"
    "v_bitop3_b32 v0, v0, v217, s50 bitop3:0xf6\n"
    "v_bitop3_b32 v0, v0, v219, s50 bitop3:0xf6\n"
    "s_waitcnt vmcnt(0)\n"
    "v_bitop3_b32 v0, v0, v221, s50 bitop3:0xf6\n"
    "v_bitop3_b32 v0, v0, v223, s50 bitop3:0xf6\n"
    "v_cmp_ne_u32 vcc, 0, v0\n"
    "s_nop 0\n"
    "s_cmp_lg_u64 vcc, 0\n"
    "s_cbranch_scc1 .Lf_gslow3\n"
    ".Lf_gback4:\n"
    "v_mov_b32 v209, v212\n"
    "v_mov_b32 v211, v214\n"
    "ds_write_b128 v13, v[208:211] offset:0\n"
    "v_mov_b32 v217, v220\n"
    "v_mov_b32 v219, v222\n"
    "ds_write_b128 v13, v[216:219] offset:128\n"
    "buffer_load_dword v248, v247, s[12:15], s79 offen\n"
    "buffer_load_dword v249, v247, s[16:19], s79 offen\n"
    "s_waitcnt lgkmcnt(0)\n"
    "s_barrier\n"
    "ds_read_b128 a[252:255], v10 offset:3136\n"
    "ds_read_b128 a[248:251], v10 offset:2880\n"
    "ds_read_b128 a[244:247], v10 offset:2624\n"
    "ds_read_b128 a[240:243], v10 offset:2368\n"
    "s_waitcnt lgkmcnt(3)\n"
    "v_mfma_f32_16x16x32_bf16 v[188:191], v[16:19], a[252:255], 0\n"
    "ds_read_b128 a[236:239], v10 offset:816\n"
    "s_waitcnt lgkmcnt(3)\n"
    "v_mfma_f32_16x16x32_bf16 v[184:187], v[16:19], a[248:251], 0\n"
    "ds_read_b128 a[232:235], v10 offset:560\n"
    "v_mfma_f32_16x16x32_bf16 v[188:191], v[144:147], a[252:255], v[188:191]\n"
    "ds_read_b128 a[228:231], v10 offset:304\n"
    "v_mfma_f32_16x16x32_bf16 v[184:187], v[144:147], a[248:251], v[184:187]\n"
    "ds_read_b128 a[224:227], v10 offset:48\n"
    "v_mfma_f32_16x16x32_bf16 v[188:191], v[48:51], a[248:251], v[188:191]\n"
    "s_waitcnt lgkmcnt(5)\n"
    "v_mfma_f32_16x16x32_bf16 v[180:183], v[16:19], a[244:247], 0\n"
    "v_mfma_f32_16x16x32_bf16 v[184:187], v[48:51], a[244:247], v[184:187]\n"
    "v_mfma_f32_16x16x32_bf16 v[180:183], v[144:147], a[244:247], v[180:183]\n"
    "v_mfma_f32_16x16x32_bf16 v[188:191], v[80:83], a[244:247], v[188:191]\n"
    "s_waitcnt lgkmcnt(4)\n"
    "v_mfma_f32_16x16x32_bf16 v[176:179], v[16:19], a[240:243], 0\n"
    "v_mfma_f32_16x16x32_bf16 v[180:183], v[48:51], a[240:243], v[180:183]\n"
    "v_mfma_f32_16x16x32_bf16 v[184:187], v[80:83], a[240:243], v[184:187]\n"
    "v_mfma_f32_16x16x32_bf16 v[176:179], v[144:147], a[240:243], v[176:179]\n"
    "v_mfma_f32_16x16x32_bf16 v[188:191], v[112:115], a[240:243], v[188:191]\n"
    "s_waitcnt lgkmcnt(3)\n"
    "v_mfma_f32_16x16x32_bf16 v[188:191], v[20:23], a[236:239], v[188:191]\n"
    "ds_read_b128 a[252:255], v10 offset:3120\n"
    "s_waitcnt lgkmcnt(3)\n"
    "v_mfma_f32_16x16x32_bf16 v[184:187], v[20:23], a[232:235], v[184:187]\n"
    "ds_read_b128 a[248:251], v10 offset:2864\n"
    "v_mfma_f32_16x16x32_bf16 v[188:191], v[148:151], a[236:239], v[188:191]\n"
    "ds_read_b128 a[244:247], v10 offset:2608\n"
    "v_mfma_f32_16x16x32_bf16 v[184:187], v[148:151], a[232:235], v[184:187]\n"
    "ds_read_b128 a[240:243], v10 offset:2352\n"
    "v_mfma_f32_16x16x32_bf16 v[188:191], v[52:55], a[232:235], v[188:191]\n"
    "s_waitcnt lgkmcnt(5)\n"
    "v_mfma_f32_16x16x32_bf16 v[180:183], v[20:23], a[228:231], v[180:183]\n"
    "v_mfma_f32_16x16x32_bf16 v[184:187], v[52:55], a[228:231], v[184:187]\n"
    "v_mfma_f32_16x16x32_bf16 v[180:183], v[148:151], a[228:231], v[180:183]\n"
    "v_mfma_f32_16x16x32_bf16 v[188:191], v[84:87], a[228:231], v[188:191]\n"
    "s_waitcnt lgkmcnt(4)\n"
    "v_mfma_f32_16x16x32_bf16 v[176:179], v[20:23], a[224:227], v[176:179]\n"
    "v_mfma_f32_16x16x32_bf16 v[180:183], v[52:55], a[224:227], v[180:183]\n"
    "v_mfma_f32_16x16x32_bf16 v[184:187], v[84:87], a[224:227], v[184:187]\n"
    "v_mfma_f32_16x16x32_bf16 v[176:179], v[148:151], a[224:227], v[176:179]\n"
    "v_mfma_f32_16x16x32_bf16 v[188:191], v[116:119], a[224:227], v[188:191]\n"
    "s_waitcnt lgkmcnt(3)\n"
    "v_mfma_f32_16x16x32_bf16 v[188:191], v[24:27], a[252:255], v[188:191]\n"
    "ds_read_b128 a[236:239], v10 offset:800\n"
    "s_waitcnt lgkmcnt(3)\n"
    "v_mfma_f32_16x16x32_bf16 v[184:187], v[24:27], a[248:251], v[184:187]\n"
    "ds_read_b128 a[232:235], v10 offset:544\n"
    "v_mfma_f32_16x16x32_bf16 v[188:191], v[56:59], a[248:251], v[188:191]\n"
    "ds_read_b128 a[228:231], v10 offset:288\n"
    "buffer_load_dwordx4 v[224:227], v254, s[8:11], s67 offen offset:0 sc1\n"
    "s_waitcnt lgkmcnt(4)\n"
    "v_mfma_f32_16x16x32_bf16 v[180:183], v[24:27], a[244:247], v[180:183]\n"
    "ds_read_b128 a[224:227], v10 offset:32\n"
    "buffer_load_dwordx4 v[228:231], v254, s[8:11], s67 offen offset:128 sc1\n"
    "v_mfma_f32_16x16x32_bf16 v[184:187], v[56:59], a[244:247], v[184:187]\n"
    "buffer_load_dwordx4 v[232:235], v254, s[8:11], s67 offen offset:2048 sc1\n"
    "v_mfma_f32_16x16x32_bf16 v[188:191], v[88:91], a[244:247], v[188:191]\n"
    "buffer_load_dwordx4 v[236:239], v254, s[8:11], s67 offen offset:2176 sc1\n"
    "s_waitcnt lgkmcnt(4)\n"
    "v_mfma_f32_16x16x32_bf16 v[176:179], v[24:27], a[240:243], v[176:179]\n"
    "v_mfma_f32_16x16x32_bf16 v[180:183], v[56:59], a[240:243], v[180:183]\n"
    "v_mfma_f32_16x16x32_bf16 v[184:187], v[88:91], a[240:243], v[184:187]\n"
    "v_mfma_f32_16x16x32_bf16 v[188:191], v[120:123], a[240:243], v[188:191]\n"
    "s_waitcnt lgkmcnt(3)\n"
    "v_mfma_f32_16x16x32_bf16 v[188:191], v[28:31], a[236:239], v[188:191]\n"
    "ds_read_b128 a[252:255], v10 offset:3104\n"
    "s_waitcnt lgkmcnt(3)\n"
    "v_mfma_f32_16x16x32_bf16 v[184:187], v[28:31], a[232:235], v[184:187]\n"
    "ds_read_b128 a[248:251], v10 offset:2848\n"
    "v_mfma_f32_16x16x32_bf16 v[188:191], v[60:63], a[232:235], v[188:191]\n"
    "ds_read_b128 a[244:247], v10 offset:2592\n"
    "s_waitcnt lgkmcnt(4)\n"
    "v_mfma_f32_16x16x32_bf16 v[180:183], v[28:31], a[228:231], v[180:183]\n"
    "ds_read_b128 a[240:243], v10 offset:2336\n"
    "v_mfma_f32_16x16x32_bf16 v[184:187], v[60:63], a[228:231], v[184:187]\n"
    "v_mfma_f32_16x16x32_bf16 v[188:191], v[92:95], a[228:231], v[188:191]\n"
    "s_waitcnt lgkmcnt(4)\n"
    "v_mfma_f32_16x16x32_bf16 v[176:179], v[28:31], a[224:227], v[176:179]\n"
    "v_mfma_f32_16x16x32_bf16 v[180:183], v[60:63], a[224:227], v[180:183]\n"
    "v_mfma_f32_16x16x32_bf16 v[184:187], v[92:95], a[224:227], v[184:187]\n"
    "v_mfma_f32_16x16x32_bf16 v[188:191], v[124:127], a[224:227], v[188:191]\n"
    "s_waitcnt lgkmcnt(3)\n"
    "v_mfma_f32_16x16x32_bf16 v[188:191], v[32:35], a[252:255], v[188:191]\n"
    "ds_read_b128 a[236:239], v10 offset:784\n"
    "s_waitcnt lgkmcnt(3)\n"
    "v_mfma_f32_16x16x32_bf16 v[184:187], v[32:35], a[248:251], v[184:187]\n"
    "ds_read_b128 a[232:235], v10 offset:528\n"
    "v_mfma_f32_16x16x32_bf16 v[188:191], v[64:67], a[248:251], v[188:191]\n"
    "ds_read_b128 a[228:231], v10 offset:272\n"
    "s_waitcnt lgkmcnt(4)\n"
    "v_mfma_f32_16x16x32_bf16 v[180:183], v[32:35], a[244:247], v[180:183]\n"
    "ds_read_b128 a[224:227], v10 offset:16\n"
    "v_mfma_f32_16x16x32_bf16 v[184:187], v[64:67], a[244:247], v[184:187]\n"
    "v_mfma_f32_16x16x32_bf16 v[188:191], v[96:99], a[244:247], v[188:191]\n"
    "s_waitcnt lgkmcnt(4)\n"
    "v_mfma_f32_16x16x32_bf16 v[176:179], v[32:35], a[240:243], v[176:179]\n"
    "v_mfma_f32_16x16x32_bf16 v[180:183], v[64:67], a[240:243], v[180:183]\n"
    "v_mfma_f32_16x16x32_bf16 v[184:187], v[96:99], a[240:243], v[184:187]\n"
    "v_mfma_f32_16x16x32_bf16 v[188:191], v[128:131], a[240:243], v[188:191]\n"
    "s_waitcnt lgkmcnt(3)\n"
    "v_mfma_f32_16x16x32_bf16 v[188:191], v[36:39], a[236:239], v[188:191]\n"
    "ds_read_b128 a[252:255], v10 offset:3088\n"
    "s_waitcnt lgkmcnt(3)\n"
    "v_mfma_f32_16x16x32_bf16 v[184:187], v[36:39], a[232:235], v[184:187]\n"
    "ds_read_b128 a[248:251], v10 offset:2832\n"
    "v_mfma_f32_16x16x32_bf16 v[188:191], v[68:71], a[232:235], v[188:191]\n"
    "ds_read_b128 a[244:247], v10 offset:2576\n"
    "s_waitcnt lgkmcnt(4)\n"
    "v_mfma_f32_16x16x32_bf16 v[180:183], v[36:39], a[228:231], v[180:183]\n"
    "ds_read_b128 a[240:243], v10 offset:2320\n"
    "v_mfma_f32_16x16x32_bf16 v[184:187], v[68:71], a[228:231], v[184:187]\n"
    "v_mfma_f32_16x16x32_bf16 v[188:191], v[100:103], a[228:231], v[188:191]\n"
    "s_waitcnt lgkmcnt(4)\n"
    "v_mfma_f32_16x16x32_bf16 v[176:179], v[36:39], a[224:227], v[176:179]\n"
    "v_mfma_f32_16x16x32_bf16 v[180:183], v[68:71], a[224:227], v[180:183]\n"
    "v_mfma_f32_16x16x32_bf16 v[184:187], v[100:103], a[224:227], v[184:187]\n"
    "v_mfma_f32_16x16x32_bf16 v[188:191], v[132:135], a[224:227], v[188:191]\n"
    "s_waitcnt lgkmcnt(3)\n"
    "v_mfma_f32_16x16x32_bf16 v[188:191], v[40:43], a[252:255], v[188:191]\n"
    "ds_read_b128 a[236:239], v10 offset:768\n"
    "s_waitcnt lgkmcnt(3)\n"
    "v_mfma_f32_16x16x32_bf16 v[184:187], v[40:43], a[248:251], v[184:187]\n"
    "ds_read_b128 a[232:235], v10 offset:512\n"
    "v_mfma_f32_16x16x32_bf16 v[188:191], v[72:75], a[248:251], v[188:191]\n"
    "ds_read_b128 a[228:231], v10 offset:256\n"
    "s_waitcnt lgkmcnt(4)\n"
    "v_mfma_f32_16x16x32_bf16 v[180:183], v[40:43], a[244:247], v[180:183]\n"
    "ds_read_b128 a[224:227], v10 offset:0\n"
    "v_mfma_f32_16x16x32_bf16 v[184:187], v[72:75], a[244:247], v[184:187]\n"
    "v_mfma_f32_16x16x32_bf16 v[188:191], v[104:107], a[244:247], v[188:191]\n"
    "s_waitcnt lgkmcnt(4)\n"
    "v_mfma_f32_16x16x32_bf16 v[176:179], v[40:43], a[240:243], v[176:179]\n"
    "v_mfma_f32_16x16x32_bf16 v[180:183], v[72:75], a[240:243], v[180:183]\n"
    "v_mfma_f32_16x16x32_bf16 v[184:187], v[104:107], a[240:243], v[184:187]\n"
    "v_mfma_f32_16x16x32_bf16 v[188:191], v[136:139], a[240:243], v[188:191]\n"
    "s_waitcnt lgkmcnt(3)\n"
    "v_mfma_f32_16x16x32_bf16 v[188:191], v[44:47], a[236:239], v[188:191]\n"
    "ds_read_b128 a[252:255], v10 offset:3136\n"
    "s_waitcnt lgkmcnt(3)\n"
    "v_mfma_f32_16x16x32_bf16 v[184:187], v[44:47], a[232:235], v[184:187]\n"
    "ds_read_b128 a[248:251], v10 offset:2880\n"
    "v_mfma_f32_16x16x32_bf16 v[188:191], v[76:79], a[232:235], v[188:191]\n"
    "ds_read_b128 a[244:247], v10 offset:2624\n"
    "s_waitcnt lgkmcnt(4)\n"
    "v_mfma_f32_16x16x32_bf16 v[180:183], v[44:47], a[228:231], v[180:183]\n"
    "ds_read_b128 a[240:243], v10 offset:2368\n"
    "v_mfma_f32_16x16x32_bf16 v[184:187], v[76:79], a[228:231], v[184:187]\n"
    "v_mfma_f32_16x16x32_bf16 v[188:191], v[108:111], a[228:231], v[188:191]\n"
    "s_waitcnt lgkmcnt(4)\n"
    "v_mfma_f32_16x16x32_bf16 v[176:179], v[44:47], a[224:227], v[176:179]\n"
    "v_mfma_f32_16x16x32_bf16 v[180:183], v[76:79], a[224:227], v[180:183]\n"
    "v_mfma_f32_16x16x32_bf16 v[184:187], v[108:111], a[224:227], v[184:187]\n"
    "v_cvt_pk_bf16_f32 v176, v176, v177\n"
    "v_cvt_pk_bf16_f32 v177, v178, v179\n"
    "v_mfma_f32_16x16x32_bf16 v[188:191], v[140:143], a[224:227], v[188:191]\n"
    "ds_write_b64 v11, v[176:177] offset:0\n"
    "v_cvt_pk_bf16_f32 v180, v180, v181\n"
    "v_cvt_pk_bf16_f32 v181, v182, v183\n"
    "s_waitcnt lgkmcnt(4)\n"
    "v_mfma_f32_16x16x32_bf16 v[192:195], v[48:51], a[252:255], 0\n"
    "ds_read_b128 a[236:239], v10 offset:816\n"
    "ds_write_b64 v11, v[180:181] offset:576\n"
    "v_mov_b32 v178, s50\n"
    "v_mov_b32 v179, s50\n"
    "v_cvt_pk_bf16_f32 v184, v184, v185\n"
    "v_cvt_pk_bf16_f32 v185, v186, v187\n"
    "v_mfma_f32_16x16x32_bf16 v[196:199], v[80:83], a[252:255], 0\n"
    "ds_read_b128 a[232:235], v10 offset:560\n"
    "ds_write_b64 v11, v[184:185] offset:1152\n"
    "v_mov_b32 v182, s50\n"
    "v_mov_b32 v183, s50\n"
    "v_cvt_pk_bf16_f32 v188, v188, v189\n"
    "v_cvt_pk_bf16_f32 v189, v190, v191\n"
    "v_mfma_f32_16x16x32_bf16 v[200:203], v[112:115], a[252:255], 0\n"
    "ds_read_b128 a[228:231], v10 offset:304\n"
    "ds_write_b64 v11, v[188:189] offset:1728\n"
    "v_mov_b32 v186, s50\n"
    "v_mov_b32 v187, s50\n"
    "v_mfma_f32_16x16x32_bf16 v[204:207], a[96:99], a[252:255], 0\n"
    "ds_read_b128 a[224:227], v10 offset:48\n"
    "v_mov_b32 v190, s50\n"
    "v_mov_b32 v191, s50\n"
    "s_waitcnt lgkmcnt(10)\n"
    "v_mfma_f32_16x16x32_bf16 v[192:195], v[80:83], a[248:251], v[192:195]\n"
    "s_waitcnt lgkmcnt(0)\n"
    "s_barrier\n"
    "ds_read_b64_tr_b16 v[176:177], v12 offset:0\n"
    "ds_read_b64_tr_b16 v[180:181], v12 offset:32\n"
    "ds_read_b64_tr_b16 v[184:185], v12 offset:64\n"
    "ds_read_b64_tr_b16 v[188:189], v12 offset:96\n"
    "v_mfma_f32_16x16x32_bf16 v[196:199], v[112:115], a[248:251], v[196:199]\n"
    "v_mfma_f32_16x16x32_bf16 v[200:203], a[96:99], a[248:251], v[200:203]\n"
    "v_mfma_f32_16x16x32_bf16 v[204:207], a[100:103], a[248:251], v[204:207]\n"
    "s_waitcnt lgkmcnt(3)\n"
    "v_swap_b32 v177, v178\n"
    "v_mfma_f32_16x16x32_bf16 v[192:195], v[112:115], a[244:247], v[192:195]\n"
    "buffer_store_dwordx4 v[176:179], v2, s[8:11], s65 offen offset:0 sc1\n"
    "s_waitcnt lgkmcnt(2)\n"
    "v_swap_b32 v181, v182\n"
    "v_mfma_f32_16x16x32_bf16 v[196:199], a[96:99], a[244:247], v[196:199]\n"
    "buffer_store_dwordx4 v[180:183], v2, s[8:11], s65 offen offset:1024 sc1\n"
    "s_waitcnt lgkmcnt(1)\n"
    "v_swap_b32 v185, v186\n"
    "v_mfma_f32_16x16x32_bf16 v[200:203], a[100:103], a[244:247], v[200:203]\n"
    "buffer_store_dwordx4 v[184:187], v2, s[8:11], s65 offen offset:2048 sc1\n"
    "s_waitcnt lgkmcnt(0)\n"
    "v_swap_b32 v189, v190\n"
    "v_mfma_f32_16x16x32_bf16 v[204:207], a[104:107], a[244:247], v[204:207]\n"
    "buffer_store_dwordx4 v[188:191], v2, s[8:11], s65 offen offset:3072 sc1\n"
    "s_add_u32 s65, s65, 16384\n"
    "v_mfma_f32_16x16x32_bf16 v[192:195], a[96:99], a[240:243], v[192:195]\n"
    "v_mfma_f32_16x16x32_bf16 v[196:199], a[100:103], a[240:243], v[196:199]\n"
    "v_mfma_f32_16x16x32_bf16 v[200:203], a[104:107], a[240:243], v[200:203]\n"
    "v_mfma_f32_16x16x32_bf16 v[204:207], a[108:111], a[240:243], v[204:207]\n"
    "v_mfma_f32_16x16x32_bf16 v[192:195], v[52:55], a[236:239], v[192:195]\n"
    "ds_read_b128 a[252:255], v10 offset:3120\n"
    "v_mfma_f32_16x16x32_bf16 v[196:199], v[84:87], a[236:239], v[196:199]\n"
    "ds_read_b128 a[248:251], v10 offset:2864\n"
    "v_mfma_f32_16x16x32_bf16 v[200:203], v[116:119], a[236:239], v[200:203]\n"
    "ds_read_b128 a[244:247], v10 offset:2608\n"
    "v_mfma_f32_16x16x32_bf16 v[204:207], a[112:115], a[236:239], v[204:207]\n"
    "ds_read_b128 a[240:243], v10 offset:2352\n"
    "s_add_u32 m0, s92, 0\n"
    "s_nop 0\n"
    "buffer_load_dwordx4 v169, s[12:15], s77 offen offset:0 lds\n"
    "v_mfma_f32_16x16x32_bf16 v[192:195], v[84:87], a[232:235], v[192:195]\n"
    "v_mfma_f32_16x16x32_bf16 v[196:199], v[116:119], a[232:235], v[196:199]\n"
    "v_mfma_f32_16x16x32_bf16 v[200:203], a[112:115], a[232:235], v[200:203]\n"
    "v_mfma_f32_16x16x32_bf16 v[204:207], a[116:119], a[232:235], v[204:207]\n"
    "v_mfma_f32_16x16x32_bf16 v[192:195], v[116:119], a[228:231], v[192:195]\n"
    "buffer_load_dwordx4 v170, s[12:15], s77 offen offset:1024 lds\n"
    "v_mfma_f32_16x16x32_bf16 v[196:199], a[112:115], a[228:231], v[196:199]\n"
    "s_waitcnt vmcnt(9)\n"
    "v_xor_b32 v0, s50, v225\n"
    "v_bitop3_b32 v0, v0, v227, s50 bitop3:0xf6\n"
    "s_waitcnt vmcnt(8)\n"
    "v_bitop3_b32 v0, v0, v229, s50 bitop3:0xf6\n"
    "v_mfma_f32_16x16x32_bf16 v[200:203], a[116:119], a[228:231], v[200:203]\n"
    "v_bitop3_b32 v0, v0, v231, s50 bitop3:0xf6\n"
    "s_waitcnt vmcnt(7)\n"
    "v_bitop3_b32 v0, v0, v233, s50 bitop3:0xf6\n"
    "v_bitop3_b32 v0, v0, v235, s50 bitop3:0xf6\n"
    "v_mfma_f32_16x16x32_bf16 v[204:207], a[120:123], a[228:231], v[204:207]\n"
    "s_waitcnt vmcnt(6)\n"
    "v_bitop3_b32 v0, v0, v237, s50 bitop3:0xf6\n"
    "v_bitop3_b32 v0, v0, v239, s50 bitop3:0xf6\n"
    "v_cmp_ne_u32 vcc, 0, v0\n"
    "v_mfma_f32_16x16x32_bf16 v[192:195], a[112:115], a[224:227], v[192:195]\n"
    "s_cmp_lg_u64 vcc, 0\n"
    "s_cbranch_scc1 .Lf_gslow5\n"
    ".Lf_gback6:\n"
    "v_mfma_f32_16x16x32_bf16 v[196:199], a[116:119], a[224:227], v[196:199]\n"
    "buffer_load_dwordx4 v171, s[12:15], s77 offen offset:2048 lds\n"
    "v_mov_b32 v225, v228\n"
    "v_mov_b32 v227, v230\n"
    "v_mfma_f32_16x16x32_bf16 v[200:203], a[120:123], a[224:227], v[200:203]\n"
    "ds_write_b128 v13, v[224:227] offset:1024\n"
    "v_mfma_f32_16x16x32_bf16 v[204:207], a[124:127], a[224:227], v[204:207]\n"
    "v_mov_b32 v233, v236\n"
    "v_mov_b32 v235, v238\n"
    "s_waitcnt lgkmcnt(4)\n"
    "v_mfma_f32_16x16x32_bf16 v[192:195], v[56:59], a[252:255], v[192:195]\n"
    "ds_read_b128 a[236:239], v10 offset:800\n"
    "ds_write_b128 v13, v[232:235] offset:1152\n"
    "v_mfma_f32_16x16x32_bf16 v[196:199], v[88:91], a[252:255], v[196:199]\n"
    "ds_read_b128 a[232:235], v10 offset:544\n"
    "v_mfma_f32_16x16x32_bf16 v[200:203], v[120:123], a[252:255], v[200:203]\n"
    "ds_read_b128 a[228:231], v10 offset:288\n"
    "s_add_u32 m0, s92, 3072\n"
    "s_nop 0\n"
    "buffer_load_dwordx4 v172, s[16:19], s77 offen offset:0 lds\n"
    "v_mfma_f32_16x16x32_bf16 v[204:207], a[128:131], a[252:255], v[204:207]\n"
    "ds_read_b128 a[224:227], v10 offset:32\n"
    "s_waitcnt lgkmcnt(8)\n"
    "v_mfma_f32_16x16x32_bf16 v[192:195], v[88:91], a[248:251], v[192:195]\n"
    "v_mfma_f32_16x16x32_bf16 v[196:199], v[120:123], a[248:251], v[196:199]\n"
    "v_mfma_f32_16x16x32_bf16 v[200:203], a[128:131], a[248:251], v[200:203]\n"
    "v_mfma_f32_16x16x32_bf16 v[204:207], a[132:135], a[248:251], v[204:207]\n"
    "buffer_load_dwordx4 v173, s[16:19], s77 offen offset:1024 lds\n"
    "s_waitcnt lgkmcnt(7)\n"
    "v_mfma_f32_16x16x32_bf16 v[192:195], v[120:123], a[244:247], v[192:195]\n"
    "v_mfma_f32_16x16x32_bf16 v[196:199], a[128:131], a[244:247], v[196:199]\n"
    "v_mfma_f32_16x16x32_bf16 v[200:203], a[132:135], a[244:247], v[200:203]\n"
    "v_mfma_f32_16x16x32_bf16 v[204:207], a[136:139], a[244:247], v[204:207]\n"
    "s_waitcnt lgkmcnt(6)\n"
    "v_mfma_f32_16x16x32_bf16 v[192:195], a[128:131], a[240:243], v[192:195]\n"
    "buffer_load_dwordx4 v174, s[16:19], s77 offen offset:2048 lds\n"
    "v_mfma_f32_16x16x32_bf16 v[196:199], a[132:135], a[240:243], v[196:199]\n"
    "v_mfma_f32_16x16x32_bf16 v[200:203], a[136:139], a[240:243], v[200:203]\n"
    "v_mfma_f32_16x16x32_bf16 v[204:207], a[140:143], a[240:243], v[204:207]\n"
    "s_waitcnt lgkmcnt(4)\n"
    "v_mfma_f32_16x16x32_bf16 v[192:195], v[60:63], a[236:239], v[192:195]\n"
    "ds_read_b128 a[252:255], v10 offset:3104\n"
    "v_mfma_f32_16x16x32_bf16 v[196:199], v[92:95], a[236:239], v[196:199]\n"
    "ds_read_b128 a[248:251], v10 offset:2848\n"
    "s_add_u32 s92, s92, 6144\n"
    "s_add_u32 s77, s77, 98304\n"
    "s_cmp_eq_u32 s77, s94\n"
    "s_cselect_b32 s92, s95, s92\n"
    "s_mov_b32 m0, s92\n"
    "s_cselect_b32 s77, s93, s77\n"
    "buffer_load_dwordx4 v169, s[12:15], s77 offen offset:0 lds\n"
    "v_mfma_f32_16x16x32_bf16 v[200:203], v[124:127], a[236:239], v[200:203]\n"
    "ds_read_b128 a[244:247], v10 offset:2592\n"
    "v_mfma_f32_16x16x32_bf16 v[204:207], a[144:147], a[236:239], v[204:207]\n"
    "ds_read_b128 a[240:243], v10 offset:2336\n"
    "s_waitcnt lgkmcnt(6)\n"
    "v_mfma_f32_16x16x32_bf16 v[192:195], v[92:95], a[232:235], v[192:195]\n"
    "v_mfma_f32_16x16x32_bf16 v[196:199], v[124:127], a[232:235], v[196:199]\n"
    "v_mfma_f32_16x16x32_bf16 v[200:203], a[144:147], a[232:235], v[200:203]\n"
    "buffer_load_dwordx4 v170, s[12:15], s77 offen offset:1024 lds\n"
    "v_mfma_f32_16x16x32_bf16 v[204:207], a[148:151], a[232:235], v[204:207]\n"
    "s_waitcnt lgkmcnt(5)\n"
    "v_mfma_f32_16x16x32_bf16 v[192:195], v[124:127], a[228:231], v[192:195]\n"
    "v_mfma_f32_16x16x32_bf16 v[196:199], a[144:147], a[228:231], v[196:199]\n"
    "v_mfma_f32_16x16x32_bf16 v[200:203], a[148:151], a[228:231], v[200:203]\n"
    "v_mfma_f32_16x16x32_bf16 v[204:207], a[152:155], a[228:231], v[204:207]\n"
    "buffer_load_dwordx4 v171, s[12:15], s77 offen offset:2048 lds\n"
    "s_waitcnt lgkmcnt(4)\n"
    "v_mfma_f32_16x16x32_bf16 v[192:195], a[144:147], a[224:227], v[192:195]\n"
    "v_mfma_f32_16x16x32_bf16 v[196:199], a[148:151], a[224:227], v[196:199]\n"
    "v_mfma_f32_16x16x32_bf16 v[200:203], a[152:155], a[224:227], v[200:203]\n"
    "v_mfma_f32_16x16x32_bf16 v[204:207], a[156:159], a[224:227], v[204:207]\n"
    "s_waitcnt lgkmcnt(3)\n"
    "v_mfma_f32_16x16x32_bf16 v[192:195], v[64:67], a[252:255], v[192:195]\n"
    "ds_read_b128 a[236:239], v10 offset:784\n"
    "s_add_u32 m0, s92, 3072\n"
    "s_nop 0\n"
    "buffer_load_dwordx4 v172, s[16:19], s77 offen offset:0 lds\n"
    "v_mfma_f32_16x16x32_bf16 v[196:199], v[96:99], a[252:255], v[196:199]\n"
    "ds_read_b128 a[232:235], v10 offset:528\n"
    "v_mfma_f32_16x16x32_bf16 v[200:203], v[128:131], a[252:255], v[200:203]\n"
    "ds_read_b128 a[228:231], v10 offset:272\n"
    "v_mfma_f32_16x16x32_bf16 v[204:207], a[160:163], a[252:255], v[204:207]\n"
    "ds_read_b128 a[224:227], v10 offset:16\n"
    "s_waitcnt lgkmcnt(6)\n"
    "v_mfma_f32_16x16x32_bf16 v[192:195], v[96:99], a[248:251], v[192:195]\n"
    "v_mfma_f32_16x16x32_bf16 v[196:199], v[128:131], a[248:251], v[196:199]\n"
    "buffer_load_dwordx4 v173, s[16:19], s77 offen offset:1024 lds\n"
    "v_mfma_f32_16x16x32_bf16 v[200:203], a[160:163], a[248:251], v[200:203]\n"
    "v_mfma_f32_16x16x32_bf16 v[204:207], a[164:167], a[248:251], v[204:207]\n"
    "s_waitcnt lgkmcnt(5)\n"
    "v_mfma_f32_16x16x32_bf16 v[192:195], v[128:131], a[244:247], v[192:195]\n"
    "v_mfma_f32_16x16x32_bf16 v[196:199], a[160:163], a[244:247], v[196:199]\n"
    "v_mfma_f32_16x16x32_bf16 v[200:203], a[164:167], a[244:247], v[200:203]\n"
    "buffer_load_dwordx4 v174, s[16:19], s77 offen offset:2048 lds\n"
    "v_mfma_f32_16x16x32_bf16 v[204:207], a[168:171], a[244:247], v[204:207]\n"
    "s_waitcnt lgkmcnt(4)\n"
    "v_mfma_f32_16x16x32_bf16 v[192:195], a[160:163], a[240:243], v[192:195]\n"
    "v_mfma_f32_16x16x32_bf16 v[196:199], a[164:167], a[240:243], v[196:199]\n"
    "v_mfma_f32_16x16x32_bf16 v[200:203], a[168:171], a[240:243], v[200:203]\n"
    "v_mfma_f32_16x16x32_bf16 v[204:207], a[172:175], a[240:243], v[204:207]\n"
    "s_add_u32 s92, s92, 6144\n"
    "s_add_u32 s77, s77, 98304\n"
    "s_cmp_eq_u32 s77, s94\n"
    "s_cselect_b32 s92, s95, s92\n"
    "s_mov_b32 m0, s92\n"
    "s_cselect_b32 s77, s93, s77\n"
    "buffer_load_dwordx4 v169, s[12:15], s77 offen offset:0 lds\n"
    "s_waitcnt lgkmcnt(3)\n"
    "v_mfma_f32_16x16x32_bf16 v[192:195], v[68:71], a[236:239], v[192:195]\n"
    "ds_read_b128 a[252:255], v10 offset:3088\n"
    "v_mfma_f32_16x16x32_bf16 v[196:199], v[100:103], a[236:239], v[196:199]\n"
    "ds_read_b128 a[248:251], v10 offset:2832\n"
    "v_mfma_f32_16x16x32_bf16 v[200:203], v[132:135], a[236:239], v[200:203]\n"
    "ds_read_b128 a[244:247], v10 offset:2576\n"
    "v_mfma_f32_16x16x32_bf16 v[204:207], a[176:179], a[236:239], v[204:207]\n"
    "ds_read_b128 a[240:243], v10 offset:2320\n"
    "s_waitcnt lgkmcnt(6)\n"
    "v_mfma_f32_16x16x32_bf16 v[192:195], v[100:103], a[232:235], v[192:195]\n"
    "buffer_load_dwordx4 v170, s[12:15], s77 offen offset:1024 lds\n"
    "v_mfma_f32_16x16x32_bf16 v[196:199], v[132:135], a[232:235], v[196:199]\n"
    "v_mfma_f32_16x16x32_bf16 v[200:203], a[176:179], a[232:235], v[200:203]\n"
    "v_mfma_f32_16x16x32_bf16 v[204:207], a[180:183], a[232:235], v[204:207]\n"
    "s_waitcnt lgkmcnt(5)\n"
    "v_mfma_f32_16x16x32_bf16 v[192:195], v[132:135], a[228:231], v[192:195]\n"
    "buffer_load_dwordx4 v[208:211], v156, s[20:23], s38 offen\n"
    "v_mfma_f32_16x16x32_bf16 v[196:199], a[176:179], a[228:231], v[196:199]\n"
    "buffer_load_dwordx4 v171, s[12:15], s77 offen offset:2048 lds\n"
    "v_mfma_f32_16x16x32_bf16 v[200:203], a[180:183], a[228:231], v[200:203]\n"
    "v_mfma_f32_16x16x32_bf16 v[204:207], a[184:187], a[228:231], v[204:207]\n"
    "buffer_load_dword v231, v157, s[20:23], s39 offen\n"
    "s_waitcnt lgkmcnt(4)\n"
    "v_mfma_f32_16x16x32_bf16 v[192:195], a[176:179], a[224:227], v[192:195]\n"
    "s_waitcnt lgkmcnt(0)\n"
    "s_barrier\n"
    "v_mfma_f32_16x16x32_bf16 v[196:199], a[180:183], a[224:227], v[196:199]\n"
    "v_mfma_f32_16x16x32_bf16 v[200:203], a[184:187], a[224:227], v[200:203]\n"
    "s_add_u32 m0, s92, 3072\n"
    "s_nop 0\n"
    "buffer_load_dwordx4 v172, s[16:19], s77 offen offset:0 lds\n"
    "v_mfma_f32_16x16x32_bf16 v[204:207], a[188:191], a[224:227], v[204:207]\n"
    "v_mfma_f32_16x16x32_bf16 v[192:195], v[72:75], a[252:255], v[192:195]\n"
    "ds_read_b128 a[236:239], v10 offset:768\n"
    "v_mfma_f32_16x16x32_bf16 v[196:199], v[104:107], a[252:255], v[196:199]\n"
    "ds_read_b128 a[232:235], v10 offset:512\n"
    "v_mfma_f32_16x16x32_bf16 v[200:203], v[136:139], a[252:255], v[200:203]\n"
    "ds_read_b128 a[228:231], v10 offset:256\n"
    "buffer_load_dwordx2 v[250:251], v175, s[8:11], s78 offen sc1\n"
    "v_mfma_f32_16x16x32_bf16 v[204:207], a[192:195], a[252:255], v[204:207]\n"
    "ds_read_b128 a[224:227], v10 offset:0\n"
    "buffer_load_dwordx4 v173, s[16:19], s77 offen offset:1024 lds\n"
    "v_mfma_f32_16x16x32_bf16 v[192:195], v[104:107], a[248:251], v[192:195]\n"
    "v_mfma_f32_16x16x32_bf16 v[196:199], v[136:139], a[248:251], v[196:199]\n"
    "v_mfma_f32_16x16x32_bf16 v[200:203], a[192:195], a[248:251], v[200:203]\n"
    "v_mfma_f32_16x16x32_bf16 v[204:207], a[196:199], a[248:251], v[204:207]\n"
    "v_mfma_f32_16x16x32_bf16 v[192:195], v[136:139], a[244:247], v[192:195]\n"
    "buffer_load_dwordx4 v174, s[16:19], s77 offen offset:2048 lds\n"
    "v_mfma_f32_16x16x32_bf16 v[196:199], a[192:195], a[244:247], v[196:199]\n"
    "v_mfma_f32_16x16x32_bf16 v[200:203], a[196:199], a[244:247], v[200:203]\n"
    "v_mfma_f32_16x16x32_bf16 v[204:207], a[200:203], a[244:247], v[204:207]\n"
    "v_mfma_f32_16x16x32_bf16 v[192:195], a[192:195], a[240:243], v[192:195]\n"
    "v_mfma_f32_16x16x32_bf16 v[196:199], a[196:199], a[240:243], v[196:199]\n"
    "s_add_u32 s92, s92, 6144\n"
    "s_add_u32 s77, s77, 98304\n"
    "s_cmp_eq_u32 s77, s94\n"
    "s_cselect_b32 s92, s95, s92\n"
    "s_mov_b32 m0, s92\n"
    "s_cselect_b32 s77, s93, s77\n"
    "buffer_load_dwordx4 v169, s[12:15], s77 offen offset:0 lds\n"
    "v_mfma_f32_16x16x32_bf16 v[200:203], a[200:203], a[240:243], v[200:203]\n"
    "v_mfma_f32_16x16x32_bf16 v[204:207], a[204:207], a[240:243], v[204:207]\n"
    "s_waitcnt lgkmcnt(3)\n"
    "v_mfma_f32_16x16x32_bf16 v[192:195], v[76:79], a[236:239], v[192:195]\n"
    "ds_read_b128 a[252:255], v10 offset:4160\n"
    "v_mfma_f32_16x16x32_bf16 v[196:199], v[108:111], a[236:239], v[196:199]\n"
    "ds_read_b128 a[248:251], v10 offset:3904\n"
    "v_mfma_f32_16x16x32_bf16 v[200:203], v[140:143], a[236:239], v[200:203]\n"
    "ds_read_b128 a[244:247], v10 offset:3648\n"
    "buffer_load_dwordx4 v170, s[12:15], s77 offen offset:1024 lds\n"
    "s_waitcnt vmcnt(8)\n"
    "v_lshlrev_b32 v222, 16, v208\n"
    "v_mfma_f32_16x16x32_bf16 v[204:207], a[208:211], a[236:239], v[204:207]\n"
    "ds_read_b128 a[240:243], v10 offset:3392\n"
    "v_and_b32 v223, 0xffff0000, v208\n"
    "v_lshlrev_b32 v224, 16, v209\n"
    "v_and_b32 v225, 0xffff0000, v209\n"
    "s_waitcnt lgkmcnt(6)\n"
    "v_mfma_f32_16x16x32_bf16 v[192:195], v[108:111], a[232:235], v[192:195]\n"
    "v_lshlrev_b32 v226, 16, v210\n"
    "v_and_b32 v227, 0xffff0000, v210\n"
    "v_lshlrev_b32 v228, 16, v211\n"
    "v_and_b32 v229, 0xffff0000, v211\n"
    "v_mfma_f32_16x16x32_bf16 v[196:199], v[140:143], a[232:235], v[196:199]\n"
    "s_waitcnt vmcnt(6)\n"
    "v_lshlrev_b32 v230, 16, v231\n"
    "v_and_b32 v231, 0xffff0000, v231\n"
    "v_mfma_f32_16x16x32_bf16 v[200:203], a[208:211], a[232:235], v[200:203]\n"
    "v_mfma_f32_16x16x32_bf16 v[204:207], a[212:215], a[232:235], v[204:207]\n"
    "buffer_load_dwordx4 v171, s[12:15], s77 offen offset:2048 lds\n"
    "s_waitcnt lgkmcnt(5)\n"
    "v_mfma_f32_16x16x32_bf16 v[192:195], v[140:143], a[228:231], v[192:195]\n"
    "v_mfma_f32_16x16x32_bf16 v[196:199], a[208:211], a[228:231], v[196:199]\n"
    "v_mfma_f32_16x16x32_bf16 v[200:203], a[212:215], a[228:231], v[200:203]\n"
    "v_mfma_f32_16x16x32_bf16 v[204:207], a[216:219], a[228:231], v[204:207]\n"
    "s_waitcnt lgkmcnt(4)\n"
    "v_mfma_f32_16x16x32_bf16 v[192:195], a[208:211], a[224:227], v[192:195]\n"
    "v_mfma_f32_16x16x32_bf16 v[196:199], a[212:215], a[224:227], v[196:199]\n"
    "v_mfma_f32_16x16x32_bf16 v[200:203], a[216:219], a[224:227], v[200:203]\n"
    "v_mfma_f32_16x16x32_bf16 v[204:207], a[220:223], a[224:227], v[204:207]\n"
    "s_waitcnt lgkmcnt(3)\n"
    "v_mfma_f32_16x16x32_bf16 v[204:207], v[16:19], a[252:255], v[204:207]\n"
    "ds_read_b128 a[236:239], v10 offset:1840\n"
    "buffer_load_dwordx4 v[232:235], v158, s[8:11], s64 offen sc1\n"
    "s_waitcnt lgkmcnt(3)\n"
    "v_mfma_f32_16x16x32_bf16 v[200:203], v[16:19], a[248:251], v[200:203]\n"
    "ds_read_b128 a[232:235], v10 offset:1584\n"
    "v_mfma_f32_16x16x32_bf16 v[204:207], v[144:147], a[252:255], v[204:207]\n"
    "ds_read_b128 a[228:231], v10 offset:1328\n"
    "buffer_load_dwordx4 v[212:215], v153, s[8:11], s68 offen sc1\n"
    "v_mfma_f32_16x16x32_bf16 v[200:203], v[144:147], a[248:251], v[200:203]\n"
    "ds_read_b128 a[224:227], v10 offset:1072\n"
    "v_mfma_f32_16x16x32_bf16 v[204:207], v[48:51], a[248:251], v[204:207]\n"
    "buffer_load_dwordx4 v[216:219], v153, s[8:11], s69 offen sc1\n"
    "s_waitcnt lgkmcnt(5)\n"
    "v_mfma_f32_16x16x32_bf16 v[196:199], v[16:19], a[244:247], v[196:199]\n"
    "v_mfma_f32_16x16x32_bf16 v[200:203], v[48:51], a[244:247], v[200:203]\n"
    "buffer_load_dwordx2 v[220:221], v154, s[8:11], s70 offen sc1\n"
    "v_mfma_f32_16x16x32_bf16 v[196:199], v[144:147], a[244:247], v[196:199]\n"
    "v_mfma_f32_16x16x32_bf16 v[204:207], v[80:83], a[244:247], v[204:207]\n"
    "s_add_u32 m0, s92, 3072\n"
    "s_nop 0\n"
    "buffer_load_dwordx4 v172, s[16:19], s77 offen offset:0 lds\n"
    "s_waitcnt lgkmcnt(4)\n"
    "v_mfma_f32_16x16x32_bf16 v[192:195], v[16:19], a[240:243], v[192:195]\n"
    "v_mfma_f32_16x16x32_bf16 v[196:199], v[48:51], a[240:243], v[196:199]\n"
    "v_mfma_f32_16x16x32_bf16 v[200:203], v[80:83], a[240:243], v[200:203]\n"
    "v_mfma_f32_16x16x32_bf16 v[192:195], v[144:147], a[240:243], v[192:195]\n"
    "v_mfma_f32_16x16x32_bf16 v[204:207], v[112:115], a[240:243], v[204:207]\n"
    "buffer_load_dwordx4 v173, s[16:19], s77 offen offset:1024 lds\n"
    "s_waitcnt lgkmcnt(3)\n"
    "v_mfma_f32_16x16x32_bf16 v[204:207], v[20:23], a[236:239], v[204:207]\n"
    "ds_read_b128 a[252:255], v10 offset:4144\n"
    "s_waitcnt lgkmcnt(3)\n"
    "v_mfma_f32_16x16x32_bf16 v[200:203], v[20:23], a[232:235], v[200:203]\n"
    "ds_read_b128 a[248:251], v10 offset:3888\n"
    "v_mfma_f32_16x16x32_bf16 v[204:207], v[148:151], a[236:239], v[204:207]\n"
    "ds_read_b128 a[244:247], v10 offset:3632\n"
    "v_mfma_f32_16x16x32_bf16 v[200:203], v[148:151], a[232:235], v[200:203]\n"
    "ds_read_b128 a[240:243], v10 offset:3376\n"
    "v_mfma_f32_16x16x32_bf16 v[204:207], v[52:55], a[232:235], v[204:207]\n"
    "buffer_load_dwordx4 v174, s[16:19], s77 offen offset:2048 lds\n"
    "s_waitcnt lgkmcnt(5)\n"
    "v_mfma_f32_16x16x32_bf16 v[196:199], v[20:23], a[228:231], v[196:199]\n"
    "v_mfma_f32_16x16x32_bf16 v[200:203], v[52:55], a[228:231], v[200:203]\n"
    "v_mfma_f32_16x16x32_bf16 v[196:199], v[148:151], a[228:231], v[196:199]\n"
    "v_mfma_f32_16x16x32_bf16 v[204:207], v[84:87], a[228:231], v[204:207]\n"
    "s_waitcnt lgkmcnt(4)\n"
    "v_mfma_f32_16x16x32_bf16 v[192:195], v[20:23], a[224:227], v[192:195]\n"
    "s_add_u32 s92, s92, 6144\n"
    "s_add_u32 s77, s77, 98304\n"
    "s_cmp_eq_u32 s77, s94\n"
    "s_cselect_b32 s92, s95, s92\n"
    "s_mov_b32 m0, s92\n"
    "s_cselect_b32 s77, s93, s77\n"
    "buffer_load_dwordx4 v169, s[12:15], s77 offen offset:0 lds\n"
    "v_mfma_f32_16x16x32_bf16 v[196:199], v[52:55], a[224:227], v[196:199]\n"
    "v_mfma_f32_16x16x32_bf16 v[200:203], v[84:87], a[224:227], v[200:203]\n"
    "v_mfma_f32_16x16x32_bf16 v[192:195], v[148:151], a[224:227], v[192:195]\n"
    "v_mfma_f32_16x16x32_bf16 v[204:207], v[116:119], a[224:227], v[204:207]\n"
    "s_waitcnt lgkmcnt(3)\n"
    "v_mfma_f32_16x16x32_bf16 v[204:207], v[24:27], a[252:255], v[204:207]\n"
    "ds_read_b128 a[236:239], v10 offset:1824\n"
    "buffer_load_dwordx4 v170, s[12:15], s77 offen offset:1024 lds\n"
    "s_waitcnt lgkmcnt(3)\n"
    "v_mfma_f32_16x16x32_bf16 v[200:203], v[24:27], a[248:251], v[200:203]\n"
    "ds_read_b128 a[232:235], v10 offset:1568\n"
    "v_mfma_f32_16x16x32_bf16 v[204:207], v[56:59], a[248:251], v[204:207]\n"
    "ds_read_b128 a[228:231], v10 offset:1312\n"
    "s_waitcnt lgkmcnt(4)\n"
    "v_mfma_f32_16x16x32_bf16 v[196:199], v[24:27], a[244:247], v[196:199]\n"
    "ds_read_b128 a[224:227], v10 offset:1056\n"
    "v_mfma_f32_16x16x32_bf16 v[200:203], v[56:59], a[244:247], v[200:203]\n"
    "v_mfma_f32_16x16x32_bf16 v[204:207], v[88:91], a[244:247], v[204:207]\n"
    "buffer_load_dwordx4 v171, s[12:15], s77 offen offset:2048 lds\n"
    "s_waitcnt lgkmcnt(4)\n"
    "v_mfma_f32_16x16x32_bf16 v[192:195], v[24:27], a[240:243], v[192:195]\n"
    "v_mfma_f32_16x16x32_bf16 v[196:199], v[56:59], a[240:243], v[196:199]\n"
    "v_mfma_f32_16x16x32_bf16 v[200:203], v[88:91], a[240:243], v[200:203]\n"
    "v_mfma_f32_16x16x32_bf16 v[204:207], v[120:123], a[240:243], v[204:207]\n"
    "s_waitcnt lgkmcnt(3)\n"
    "v_mfma_f32_16x16x32_bf16 v[204:207], v[28:31], a[236:239], v[204:207]\n"
    "ds_read_b128 a[252:255], v10 offset:4128\n"
    "s_add_u32 m0, s92, 3072\n"
    "s_nop 0\n"
    "buffer_load_dwordx4 v172, s[16:19], s77 offen offset:0 lds\n"
    "s_waitcnt lgkmcnt(3)\n"
    "v_mfma_f32_16x16x32_bf16 v[200:203], v[28:31], a[232:235], v[200:203]\n"
    "ds_read_b128 a[248:251], v10 offset:3872\n"
    "v_mfma_f32_16x16x32_bf16 v[204:207], v[60:63], a[232:235], v[204:207]\n"
    "ds_read_b128 a[244:247], v10 offset:3616\n"
    "s_waitcnt lgkmcnt(4)\n"
    "v_mfma_f32_16x16x32_bf16 v[196:199], v[28:31], a[228:231], v[196:199]\n"
    "ds_read_b128 a[240:243], v10 offset:3360\n"
    "v_mfma_f32_16x16x32_bf16 v[200:203], v[60:63], a[228:231], v[200:203]\n"
    "v_mfma_f32_16x16x32_bf16 v[204:207], v[92:95], a[228:231], v[204:207]\n"
    "buffer_load_dwordx4 v173, s[16:19], s77 offen offset:1024 lds\n"
    "s_waitcnt lgkmcnt(4)\n"
    "v_mfma_f32_16x16x32_bf16 v[192:195], v[28:31], a[224:227], v[192:195]\n"
    "v_mfma_f32_16x16x32_bf16 v[196:199], v[60:63], a[224:227], v[196:199]\n"
    "v_mfma_f32_16x16x32_bf16 v[200:203], v[92:95], a[224:227], v[200:203]\n"
    "v_mfma_f32_16x16x32_bf16 v[204:207], v[124:127], a[224:227], v[204:207]\n"
    "s_waitcnt lgkmcnt(3)\n"
    "v_mfma_f32_16x16x32_bf16 v[204:207], v[32:35], a[252:255], v[204:207]\n"
    "ds_read_b128 a[236:239], v10 offset:1808\n"
    "s_waitcnt vmcnt(11)\n"
    "v_xor_b32 v246, s50, v233\n"
    "v_bitop3_b32 v246, v246, v235, s50 bitop3:0xf6\n"
    "s_waitcnt vmcnt(10)\n"
    "v_bitop3_b32 v246, v246, v213, s50 bitop3:0xf6\n"
    "v_bitop3_b32 v246, v246, v215, s50 bitop3:0xf6\n"
    "s_waitcnt vmcnt(9)\n"
    "v_bitop3_b32 v246, v246, v217, s50 bitop3:0xf6\n"
    "v_bitop3_b32 v246, v246, v219, s50 bitop3:0xf6\n"
    "s_waitcnt vmcnt(8)\n"
    "v_bitop3_b32 v246, v246, v221, s50 bitop3:0xf6\n"
    "v_cmp_ne_u32 vcc, 0, v246\n"
    "s_nop 0\n"
    "s_cmp_lg_u64 vcc, 0\n"
    "s_cbranch_scc1 .Lf_gslow7\n"
    ".Lf_gback8:\n"
    "v_and_b32 v213, 0xffff0000, v212\n"
    "v_lshlrev_b32 v212, 16, v212\n"
    "v_and_b32 v215, 0xffff0000, v214\n"
    "v_lshlrev_b32 v214, 16, v214\n"
    "v_and_b32 v217, 0xffff0000, v216\n"
    "v_lshlrev_b32 v216, 16, v216\n"
    "v_and_b32 v219, 0xffff0000, v218\n"
    "v_lshlrev_b32 v218, 16, v218\n"
    "v_and_b32 v221, 0xffff0000, v220\n"
    "v_lshlrev_b32 v220, 16, v220\n"
    "v_pk_fma_f32 v[236:237], v[222:223], v[234:235], v[232:233] op_sel_hi:[1,0,0] neg_lo:[0,0,1] neg_hi:[0,0,1]\n"
    "v_pk_fma_f32 v[238:239], v[224:225], v[234:235], v[232:233] op_sel_hi:[1,0,0] neg_lo:[0,0,1] neg_hi:[0,0,1]\n"
    "v_pk_fma_f32 v[240:241], v[226:227], v[234:235], v[232:233] op_sel_hi:[1,0,0] neg_lo:[0,0,1] neg_hi:[0,0,1]\n"
    "v_pk_fma_f32 v[242:243], v[228:229], v[234:235], v[232:233] op_sel_hi:[1,0,0] neg_lo:[0,0,1] neg_hi:[0,0,1]\n"
    "v_pk_fma_f32 v[244:245], v[230:231], v[234:235], v[232:233] op_sel_hi:[1,0,0] neg_lo:[0,0,1] neg_hi:[0,0,1]\n"
    "v_pk_fma_f32 v[222:223], v[236:237], v[212:213], v[222:223]\n"
    "v_pk_fma_f32 v[224:225], v[238:239], v[214:215], v[224:225]\n"
    "v_pk_fma_f32 v[226:227], v[240:241], v[216:217], v[226:227]\n"
    "v_pk_fma_f32 v[228:229], v[242:243], v[218:219], v[228:229]\n"
    "v_pk_fma_f32 v[230:231], v[244:245], v[220:221], v[230:231]\n"
    "v_cvt_pk_bf16_f32 v240, v230, v231\n"
    "v_cvt_pk_bf16_f32 v236, v222, v223\n"
    "v_mov_b32 v241, v240\n"
    "v_cvt_pk_bf16_f32 v237, v224, v225\n"
    "v_cvt_pk_bf16_f32 v238, v226, v227\n"
    "v_cvt_pk_bf16_f32 v239, v228, v229\n"
    "v_permlane16_swap_b32 v240, v241\n"
    "v_mov_b32 v242, v240\n"
    "v_mov_b32 v243, v241\n"
    "v_pk_add_f32 v[232:233], v[222:223], v[224:225]\n"
    "v_pk_mul_f32 v[234:235], v[222:223], v[222:223]\n"
    "buffer_store_dwordx4 v[236:239], v152, s[8:11], s30 offen sc1\n"
    "v_permlane32_swap_b32 v240, v242\n"
    "v_permlane32_swap_b32 v241, v243\n"
    "v_pk_fma_f32 v[234:235], v[224:225], v[224:225], v[234:235]\n"
    "v_pk_add_f32 v[232:233], v[232:233], v[226:227]\n"
    "v_pk_fma_f32 v[234:235], v[226:227], v[226:227], v[234:235]\n"
    "v_pk_add_f32 v[232:233], v[232:233], v[228:229]\n"
    "v_pk_fma_f32 v[234:235], v[228:229], v[228:229], v[234:235]\n"
    "v_pk_add_f32 v[232:233], v[232:233], v[230:231]\n"
    "v_pk_fma_f32 v[234:235], v[230:231], v[230:231], v[234:235]\n"
    "v_add_f32 v232, v232, v233\n"
    "v_add_f32 v234, v234, v235\n"
    "v_mov_b32 v233, v234\n"
    "s_nop 1\n"
    "v_permlane16_swap_b32 v232, v233\n"
    "v_add_f32 v232, v232, v233\n"
    "v_mov_b32 v233, v232\n"
    "s_nop 1\n"
    "v_permlane32_swap_b32 v232, v233\n"
    "v_add_f32 v232, v232, v233\n"
    "v_mov_b32 v233, v232\n"
    "s_nop 1\n"
    "v_permlane16_swap_b32 v232, v233\n"
    "s_add_u32 s88, s36, -262144\n"
    "s_mov_b64 exec, 0xffff\n"
    "buffer_store_dwordx4 v[240:243], v152, s[8:11], s31 offen sc1\n"
    "buffer_store_dwordx2 v[232:233], v155, s[8:11], s88 offen sc1\n"
    "s_mov_b64 exec, -1\n"
    "s_waitcnt lgkmcnt(3)\n"
    "v_mfma_f32_16x16x32_bf16 v[200:203], v[32:35], a[248:251], v[200:203]\n"
    "ds_read_b128 a[232:235], v10 offset:1552\n"
    "v_mfma_f32_16x16x32_bf16 v[204:207], v[64:67], a[248:251], v[204:207]\n"
    "ds_read_b128 a[228:231], v10 offset:1296\n"
    "buffer_load_dwordx4 v174, s[16:19], s77 offen offset:2048 lds\n"
    "s_waitcnt lgkmcnt(4)\n"
    "v_mfma_f32_16x16x32_bf16 v[196:199], v[32:35], a[244:247], v[196:199]\n"
    "ds_read_b128 a[224:227], v10 offset:1040\n"
    "v_mfma_f32_16x16x32_bf16 v[200:203], v[64:67], a[244:247], v[200:203]\n"
    "v_mfma_f32_16x16x32_bf16 v[204:207], v[96:99], a[244:247], v[204:207]\n"
    "s_waitcnt lgkmcnt(4)\n"
    "v_mfma_f32_16x16x32_bf16 v[192:195], v[32:35], a[240:243], v[192:195]\n"
    "v_mfma_f32_16x16x32_bf16 v[196:199], v[64:67], a[240:243], v[196:199]\n"
    "v_mfma_f32_16x16x32_bf16 v[200:203], v[96:99], a[240:243], v[200:203]\n"
    "v_mfma_f32_16x16x32_bf16 v[204:207], v[128:131], a[240:243], v[204:207]\n"
    "s_waitcnt lgkmcnt(3)\n"
    "v_mfma_f32_16x16x32_bf16 v[204:207], v[36:39], a[236:239], v[204:207]\n"
    "ds_read_b128 a[252:255], v10 offset:4112\n"
    "s_add_u32 s89, s38, 1310720\n"
    "buffer_load_dwordx4 v[176:179], v156, s[20:23], s89 offen\n"
    "s_waitcnt lgkmcnt(3)\n"
    "v_mfma_f32_16x16x32_bf16 v[200:203], v[36:39], a[232:235], v[200:203]\n"
    "ds_read_b128 a[248:251], v10 offset:3856\n"
    "v_mfma_f32_16x16x32_bf16 v[204:207], v[68:71], a[232:235], v[204:207]\n"
    "ds_read_b128 a[244:247], v10 offset:3600\n"
    "s_waitcnt lgkmcnt(4)\n"
    "v_mfma_f32_16x16x32_bf16 v[196:199], v[36:39], a[228:231], v[196:199]\n"
    "ds_read_b128 a[240:243], v10 offset:3344\n"
    "s_add_u32 s90, s39, 1310720\n"
    "buffer_load_dword v180, v157, s[20:23], s90 offen\n"
    "v_mfma_f32_16x16x32_bf16 v[200:203], v[68:71], a[228:231], v[200:203]\n"
    "v_mfma_f32_16x16x32_bf16 v[204:207], v[100:103], a[228:231], v[204:207]\n"
    "s_waitcnt lgkmcnt(4)\n"
    "v_mfma_f32_16x16x32_bf16 v[192:195], v[36:39], a[224:227], v[192:195]\n"
    "v_mfma_f32_16x16x32_bf16 v[196:199], v[68:71], a[224:227], v[196:199]\n"
    "v_mfma_f32_16x16x32_bf16 v[200:203], v[100:103], a[224:227], v[200:203]\n"
    "v_mfma_f32_16x16x32_bf16 v[204:207], v[132:135], a[224:227], v[204:207]\n"
    "s_waitcnt lgkmcnt(3)\n"
    "v_mfma_f32_16x16x32_bf16 v[204:207], v[40:43], a[252:255], v[204:207]\n"
    "ds_read_b128 a[236:239], v10 offset:1792\n"
    "s_waitcnt lgkmcnt(3)\n"
    "v_mfma_f32_16x16x32_bf16 v[200:203], v[40:43], a[248:251], v[200:203]\n"
    "ds_read_b128 a[232:235], v10 offset:1536\n"
    "v_mfma_f32_16x16x32_bf16 v[204:207], v[72:75], a[248:251], v[204:207]\n"
    "ds_read_b128 a[228:231], v10 offset:1280\n"
    "s_waitcnt lgkmcnt(4)\n"
    "v_mfma_f32_16x16x32_bf16 v[196:199], v[40:43], a[244:247], v[196:199]\n"
    "ds_read_b128 a[224:227], v10 offset:1024\n"
    "v_mfma_f32_16x16x32_bf16 v[200:203], v[72:75], a[244:247], v[200:203]\n"
    "v_mfma_f32_16x16x32_bf16 v[204:207], v[104:107], a[244:247], v[204:207]\n"
    "s_waitcnt lgkmcnt(4)\n"
    "v_mfma_f32_16x16x32_bf16 v[192:195], v[40:43], a[240:243], v[192:195]\n"
    "v_mfma_f32_16x16x32_bf16 v[196:199], v[72:75], a[240:243], v[196:199]\n"
    "v_mfma_f32_16x16x32_bf16 v[200:203], v[104:107], a[240:243], v[200:203]\n"
    "v_mfma_f32_16x16x32_bf16 v[204:207], v[136:139], a[240:243], v[204:207]\n"
    "s_waitcnt lgkmcnt(3)\n"
    "v_mfma_f32_16x16x32_bf16 v[204:207], v[44:47], a[236:239], v[204:207]\n"
    "s_waitcnt lgkmcnt(2)\n"
    "v_mfma_f32_16x16x32_bf16 v[200:203], v[44:47], a[232:235], v[200:203]\n"
    "v_mfma_f32_16x16x32_bf16 v[204:207], v[76:79], a[232:235], v[204:207]\n"
    "s_waitcnt vmcnt(3)\n"
    "s_mov_b64 exec, 1\n"
    "ds_add_rtn_u32 v165, v3, v3 offset:57344\n"
    "s_mov_b64 exec, -1\n"
    "s_waitcnt lgkmcnt(2)\n"
    "v_mfma_f32_16x16x32_bf16 v[196:199], v[44:47], a[228:231], v[196:199]\n"
    "v_mfma_f32_16x16x32_bf16 v[200:203], v[76:79], a[228:231], v[200:203]\n"
    "v_mfma_f32_16x16x32_bf16 v[204:207], v[108:111], a[228:231], v[204:207]\n"
    "s_waitcnt lgkmcnt(0)\n"
    "v_readfirstlane_b32 s79, v165\n"
    "v_mfma_f32_16x16x32_bf16 v[192:195], v[44:47], a[224:227], v[192:195]\n"
    "s_add_u32 s91, s37, -131072\n"
    "s_cmp_eq_u32 s79, 0x30000\n"
    "s_cselect_b64 exec, 0xff, 0\n"
    "buffer_store_dwordx4 v[252:255], v2, s[8:11], s91 offen sc1\n"
    "s_mov_b64 exec, -1\n"
    "v_mfma_f32_16x16x32_bf16 v[196:199], v[76:79], a[224:227], v[196:199]\n"
    "v_mfma_f32_16x16x32_bf16 v[200:203], v[108:111], a[224:227], v[200:203]\n"
    "v_mfma_f32_16x16x32_bf16 v[204:207], v[140:143], a[224:227], v[204:207]\n"
    "v_cvt_pk_bf16_f32 v192, v192, v193\n"
    "v_cvt_pk_bf16_f32 v193, v194, v195\n"
    "ds_write_b64 v11, v[192:193] offset:9472\n"
    "v_mov_b32 v194, s50\n"
    "v_mov_b32 v195, s50\n"
    "v_cvt_pk_bf16_f32 v196, v196, v197\n"
    "v_cvt_pk_bf16_f32 v197, v198, v199\n"
    "ds_write_b64 v11, v[196:197] offset:10048\n"
    "v_mov_b32 v198, s50\n"
    "v_mov_b32 v199, s50\n"
    "v_cvt_pk_bf16_f32 v200, v200, v201\n"
    "v_cvt_pk_bf16_f32 v201, v202, v203\n"
    "ds_write_b64 v11, v[200:201] offset:10624\n"
    "v_mov_b32 v202, s50\n"
    "v_mov_b32 v203, s50\n"
    "v_cvt_pk_bf16_f32 v204, v204, v205\n"
    "v_cvt_pk_bf16_f32 v205, v206, v207\n"
    "ds_write_b64 v11, v[204:205] offset:11200\n"
    "v_mov_b32 v206, s50\n"
    "v_mov_b32 v207, s50\n"
    "s_waitcnt lgkmcnt(0)\n"
    "s_barrier\n"
    "ds_read_b64_tr_b16 v[192:193], v12 offset:9472\n"
    "ds_read_b64_tr_b16 v[196:197], v12 offset:9504\n"
    "ds_read_b64_tr_b16 v[200:201], v12 offset:9536\n"
    "ds_read_b64_tr_b16 v[204:205], v12 offset:9568\n"
    "s_waitcnt lgkmcnt(3)\n"
    "v_swap_b32 v193, v194\n"
    "buffer_store_dwordx4 v[192:195], v2, s[8:11], s65 offen offset:0 sc1\n"
    "s_waitcnt lgkmcnt(2)\n"
    "v_swap_b32 v197, v198\n"
    "buffer_store_dwordx4 v[196:199], v2, s[8:11], s65 offen offset:1024 sc1\n"
    "s_waitcnt lgkmcnt(1)\n"
    "v_swap_b32 v201, v202\n"
    "buffer_store_dwordx4 v[200:203], v2, s[8:11], s65 offen offset:2048 sc1\n"
    "s_waitcnt lgkmcnt(0)\n"
    "v_swap_b32 v205, v206\n"
    "buffer_store_dwordx4 v[204:207], v2, s[8:11], s65 offen offset:3072 sc1\n"
    "ds_read_b64_tr_b16 a[144:145], v166 offset:0\n"
    "ds_read_b64_tr_b16 a[146:147], v167 offset:0\n"
    "ds_read_b64_tr_b16 a[148:149], v166 offset:32\n"
    "ds_read_b64_tr_b16 a[150:151], v167 offset:32\n"
    "ds_read_b64_tr_b16 a[152:153], v166 offset:64\n"
    "ds_read_b64_tr_b16 a[154:155], v167 offset:64\n"
    "ds_read_b64_tr_b16 a[156:157], v166 offset:1536\n"
    "ds_read_b64_tr_b16 a[158:159], v167 offset:1536\n"
    "ds_read_b64_tr_b16 a[160:161], v166 offset:1568\n"
    "ds_read_b64_tr_b16 a[162:163], v167 offset:1568\n"
    "ds_read_b64_tr_b16 a[164:165], v166 offset:1600\n"
    "ds_read_b64_tr_b16 a[166:167], v167 offset:1600\n"
    "s_cmp_eq_u32 s98, 4\n"
    "s_cselect_b32 s67, -4, 1\n"
    "s_mulk_i32 s67, 6144\n"
    "v_add_u32 v166, s67, v166\n"
    "v_add_u32 v167, s67, v167\n"
    "ds_read_b64_tr_b16 a[168:169], v166 offset:0\n"
    "ds_read_b64_tr_b16 a[170:171], v167 offset:0\n"
    "ds_read_b64_tr_b16 a[172:173], v166 offset:32\n"
    "ds_read_b64_tr_b16 a[174:175], v167 offset:32\n"
    "ds_read_b64_tr_b16 a[176:177], v166 offset:64\n"
    "ds_read_b64_tr_b16 a[178:179], v167 offset:64\n"
    "ds_read_b64_tr_b16 a[180:181], v166 offset:1536\n"
    "ds_read_b64_tr_b16 a[182:183], v167 offset:1536\n"
    "ds_read_b64_tr_b16 a[184:185], v166 offset:1568\n"
    "ds_read_b64_tr_b16 a[186:187], v167 offset:1568\n"
    "ds_read_b64_tr_b16 a[188:189], v166 offset:1600\n"
    "ds_read_b64_tr_b16 a[190:191], v167 offset:1600\n"
    "s_waitcnt lgkmcnt(15)\n"
    "ds_write_b128 v168, a[144:147] offset:0\n"
    "v_mfma_f32_16x16x32_bf16 a[192:195], v[160:163], a[144:147], 0\n"
    "v_cmp_eq_u64 vcc, s[50:51], v[250:251]\n"
    "s_nop 0\n"
    "s_andn2_b32 s79, 0xfff, vcc_lo\n"
    "s_cbranch_scc1 .Lf_slow9\n"
    ".Lf_back10:\n"
    "buffer_load_dwordx4 a[0:3], v6, s[8:11], s76 offen offset:0 sc1\n"
    "s_lshl_b32 s57, s52, 2\n"
    "s_add_u32 s57, s57, s4\n"
    "s_lshl_b32 s23, s57, 11\n"
    "buffer_load_dwordx4 a[4:7], v6, s[8:11], s76 offen offset:1024 sc1\n"
    "s_add_u32 s23, s23, 53248\n"
    "s_mov_b32 s12, 0\n"
    "s_mov_b32 s13, 4096\n"
    "ds_write_b128 v168, a[148:151] offset:2048\n"
    "v_mfma_f32_16x16x32_bf16 a[196:199], v[160:163], a[148:151], 0\n"
    "buffer_load_dwordx4 a[8:11], v7, s[8:11], s76 offen offset:0 sc1\n"
    "s_mov_b32 s14, 8192\n"
    "s_mov_b32 s15, 12288\n"
    "s_mov_b32 s16, 16384\n"
    "ds_write_b128 v168, a[152:155] offset:4096\n"
    "v_mfma_f32_16x16x32_bf16 a[200:203], v[160:163], a[152:155], 0\n"
    "buffer_load_dwordx4 a[12:15], v7, s[8:11], s76 offen offset:1024 sc1\n"
    "s_mov_b32 s17, 20480\n"
    "s_mov_b32 s18, 24576\n"
    "s_mov_b32 s19, 28672\n"
    "buffer_load_dwordx4 a[16:19], v15, s[8:11], s76 offen offset:0 sc1\n"
    "s_mov_b32 s20, 32768\n"
    "s_mov_b32 s40, 36864\n"
    "s_mul_i32 s21, s57, 40960\n"
    "ds_write_b128 v168, a[156:159] offset:512\n"
    "v_mfma_f32_16x16x32_bf16 a[192:195], v[160:163], a[156:159], a[192:195]\n"
    "s_add_u32 s76, s76, 5120\n"
    "buffer_load_dwordx4 a[20:23], v6, s[8:11], s76 offen offset:0 sc1\n"
    "s_add_u32 s21, s21, 13815808\n"
    "s_lshl_b32 s22, s57, 12\n"
    "s_add_u32 s22, s22, 19058688\n"
    "s_waitcnt lgkmcnt(15)\n"
    "ds_write_b128 v168, a[160:163] offset:2560\n"
    "v_mfma_f32_16x16x32_bf16 a[196:199], v[160:163], a[160:163], a[196:199]\n"
    "buffer_load_dwordx4 a[24:27], v6, s[8:11], s76 offen offset:1024 sc1\n"
    "s_lshl_b32 s27, s57, 5\n"
    "s_add_u32 s27, s27, s53\n"
    "s_lshl_b32 s27, s27, 8\n"
    "buffer_load_dwordx4 a[28:31], v7, s[8:11], s76 offen offset:0 sc1\n"
    "s_add_u32 s27, s27, 315392\n"
    "s_mul_i32 s61, s57, 98304\n"
    "s_add_u32 s61, s61, 20140032\n"
    "ds_write_b128 v168, a[164:167] offset:4608\n"
    "v_mfma_f32_16x16x32_bf16 a[200:203], v[160:163], a[164:167], a[200:203]\n"
    "buffer_load_dwordx4 a[32:35], v7, s[8:11], s76 offen offset:1024 sc1\n"
    "s_lshr_b32 s62, s53, 3\n"
    "s_lshl_b32 s62, s62, 14\n"
    "s_add_u32 s61, s61, s62\n"
    "s_cmp_eq_u32 s98, 3\n"
    "s_cselect_b32 s66, -4, 1\n"
    "s_mulk_i32 s66, 6144\n"
    "v_add_u32 v166, s66, v166\n"
    "v_add_u32 v167, s66, v167\n"
    "ds_read_b64_tr_b16 a[144:145], v166 offset:0\n"
    "ds_read_b64_tr_b16 a[146:147], v167 offset:0\n"
    "ds_read_b64_tr_b16 a[148:149], v166 offset:32\n"
    "ds_read_b64_tr_b16 a[150:151], v167 offset:32\n"
    "ds_read_b64_tr_b16 a[152:153], v166 offset:64\n"
    "ds_read_b64_tr_b16 a[154:155], v167 offset:64\n"
    "ds_read_b64_tr_b16 a[156:157], v166 offset:1536\n"
    "ds_read_b64_tr_b16 a[158:159], v167 offset:1536\n"
    "ds_read_b64_tr_b16 a[160:161], v166 offset:1568\n"
    "ds_read_b64_tr_b16 a[162:163], v167 offset:1568\n"
    "ds_read_b64_tr_b16 a[164:165], v166 offset:1600\n"
    "ds_read_b64_tr_b16 a[166:167], v167 offset:1600\n"
    "v_add_u32 v168, s67, v168\n"
    "s_waitcnt lgkmcnt(15)\n"
    "ds_write_b128 v168, a[168:171] offset:0\n"
    "v_mfma_f32_16x16x32_bf16 a[192:195], v[160:163], a[168:171], a[192:195]\n"
    "buffer_load_dwordx4 a[36:39], v15, s[8:11], s76 offen offset:0 sc1\n"
    "s_lshr_b32 s62, s53, 1\n"
    "s_lshl_b32 s62, s62, 11\n"
    "s_add_u32 s61, s61, s62\n"
    "s_add_u32 s76, s76, 5120\n"
    "buffer_load_dwordx4 a[40:43], v6, s[8:11], s76 offen offset:0 sc1\n"
    "s_and_b32 s62, s53, 1\n"
    "s_lshl_b32 s62, s62, 9\n"
    "s_add_u32 s24, s61, s62\n"
    "ds_write_b128 v168, a[172:175] offset:2048\n"
    "v_mfma_f32_16x16x32_bf16 a[196:199], v[160:163], a[172:175], a[196:199]\n"
    "buffer_load_dwordx4 a[44:47], v6, s[8:11], s76 offen offset:1024 sc1\n"
    "s_add_u32 s25, s24, 8192\n"
    "s_add_u32 s26, s24, 16384\n"
    "v_and_b32 v0, 0xf0, v2\n"
    "ds_write_b128 v168, a[176:179] offset:4096\n"
    "v_mfma_f32_16x16x32_bf16 a[200:203], v[160:163], a[176:179], a[200:203]\n"
    "buffer_load_dwordx4 a[48:51], v7, s[8:11], s76 offen offset:0 sc1\n"
    "v_lshrrev_b32 v232, 4, v1\n"
    "v_lshrrev_b32 v233, 5, v1\n"
    "v_and_b32 v234, 1, v232\n"
    "buffer_load_dwordx4 a[52:55], v7, s[8:11], s76 offen offset:1024 sc1\n"
    "v_lshl_add_u32 v8, v232, 11, v0\n"
    "v_and_b32 v0, 0x70, v2\n"
    "v_and_b32 v11, 8, v1\n"
    "ds_write_b128 v168, a[180:183] offset:512\n"
    "v_mfma_f32_16x16x32_bf16 a[192:195], v[160:163], a[180:183], a[192:195]\n"
    "buffer_load_dwordx4 a[56:59], v15, s[8:11], s76 offen offset:0 sc1\n"
    "v_lshl_add_u32 v10, v11, 7, v0\n"
    "v_lshl_add_u32 v10, v234, 7, v10\n"
    "v_lshl_add_u32 v10, v233, 8, v10\n"
    "ds_write_b128 v168, a[184:187] offset:2560\n"
    "v_mfma_f32_16x16x32_bf16 a[196:199], v[160:163], a[184:187], a[196:199]\n"
    "s_add_u32 s76, s76, 5120\n"
    "buffer_load_dwordx4 a[60:63], v6, s[8:11], s76 offen offset:0 sc1\n"
    "v_lshl_add_u32 v13, v11, 5, v0\n"
    "v_add_u32 v11, v11, v0\n"
    "v_lshl_add_u32 v12, v232, 9, v13\n"
    "buffer_load_dwordx4 a[64:67], v6, s[8:11], s76 offen offset:1024 sc1\n"
    "v_lshl_add_u32 v14, v232, 16, v13\n"
    "v_lshl_add_u32 v5, v233, 15, v13\n"
    "v_lshl_add_u32 v5, v234, 3, v5\n"
    "ds_write_b128 v168, a[188:191] offset:4608\n"
    "v_mfma_f32_16x16x32_bf16 a[200:203], v[160:163], a[188:191], a[200:203]\n"
    "buffer_load_dwordx4 a[68:71], v7, s[8:11], s76 offen offset:0 sc1\n"
    "v_lshl_add_u32 v13, v232, 2, v13\n"
    "v_add_u32 v235, s21, v2\n"
    "v_and_b32 v62, 0x1ff, v12\n"
    "s_cmp_eq_u32 s98, 2\n"
    "s_cselect_b32 s67, -4, 1\n"
    "s_mulk_i32 s67, 6144\n"
    "v_add_u32 v166, s67, v166\n"
    "v_add_u32 v167, s67, v167\n"
    "ds_read_b64_tr_b16 a[168:169], v166 offset:0\n"
    "ds_read_b64_tr_b16 a[170:171], v167 offset:0\n"
    "ds_read_b64_tr_b16 a[172:173], v166 offset:32\n"
    "ds_read_b64_tr_b16 a[174:175], v167 offset:32\n"
    "ds_read_b64_tr_b16 a[176:177], v166 offset:64\n"
    "ds_read_b64_tr_b16 a[178:179], v167 offset:64\n"
    "ds_read_b64_tr_b16 a[180:181], v166 offset:1536\n"
    "ds_read_b64_tr_b16 a[182:183], v167 offset:1536\n"
    "ds_read_b64_tr_b16 a[184:185], v166 offset:1568\n"
    "ds_read_b64_tr_b16 a[186:187], v167 offset:1568\n"
    "ds_read_b64_tr_b16 a[188:189], v166 offset:1600\n"
    "ds_read_b64_tr_b16 a[190:191], v167 offset:1600\n"
    "v_add_u32 v168, s66, v168\n"
    "s_waitcnt lgkmcnt(15)\n"
    "ds_write_b128 v168, a[144:147] offset:0\n"
    "v_mfma_f32_16x16x32_bf16 a[192:195], v[160:163], a[144:147], a[192:195]\n"
    "buffer_load_dwordx4 a[72:75], v7, s[8:11], s76 offen offset:1024 sc1\n"
    "v_lshl_add_u32 v235, v232, 8, v235\n"
    "s_add_u32 s84, s96, 16384\n"
    "s_add_u32 s74, s32, 16384\n"
    "buffer_load_dwordx4 a[76:79], v15, s[8:11], s76 offen offset:0 sc1\n"
    "s_add_u32 s75, s32, 49152\n"
    "s_add_u32 s77, s33, 16384\n"
    "s_movk_i32 s41, 0\n"
    "ds_write_b128 v168, a[148:151] offset:2048\n"
    "v_mfma_f32_16x16x32_bf16 a[196:199], v[160:163], a[148:151], a[196:199]\n"
    "s_add_u32 s76, s76, 5120\n"
    "buffer_load_dwordx4 a[80:83], v6, s[8:11], s76 offen offset:0 sc1\n"
    "s_movk_i32 s42, 4096\n"
    "s_movk_i32 s43, 8192\n"
    "s_movk_i32 s48, 12288\n"
    "ds_write_b128 v168, a[152:155] offset:4096\n"
    "v_mfma_f32_16x16x32_bf16 a[200:203], v[160:163], a[152:155], a[200:203]\n"
    "buffer_load_dwordx4 a[84:87], v6, s[8:11], s76 offen offset:1024 sc1\n"
    "s_movk_i32 s49, 16384\n"
    "s_movk_i32 s85, 20480\n"
    "s_lshl_b32 s63, s4, 11\n"
    "buffer_load_dwordx4 a[88:91], v7, s[8:11], s76 offen offset:0 sc1\n"
    "s_add_u32 s63, s63, 131072\n"
    "v_add_u32 v254, s63, v2\n"
    "v_xor_b32 v232, 0x800, v254\n"
    "ds_write_b128 v168, a[156:159] offset:512\n"
    "v_mfma_f32_16x16x32_bf16 a[192:195], v[160:163], a[156:159], a[192:195]\n"
    "buffer_load_dwordx4 a[92:95], v7, s[8:11], s76 offen offset:1024 sc1\n"
    "v_xor_b32 v233, 0x1000, v254\n"
    "v_xor_b32 v234, 0x1800, v254\n"
    "ds_write_b128 v168, a[160:163] offset:2560\n"
    "v_mfma_f32_16x16x32_bf16 a[196:199], v[160:163], a[160:163], a[196:199]\n"
    "buffer_load_dwordx4 a[96:99], v15, s[8:11], s76 offen offset:0 sc1\n"
    "s_waitcnt vmcnt(31)\n"
    "v_lshlrev_b32 v198, 16, v176\n"
    "v_and_b32 v199, 0xffff0000, v176\n"
    "v_lshlrev_b32 v200, 16, v177\n"
    "v_and_b32 v201, 0xffff0000, v177\n"
    "v_lshlrev_b32 v202, 16, v178\n"
    "v_and_b32 v203, 0xffff0000, v178\n"
    "v_lshlrev_b32 v204, 16, v179\n"
    "v_and_b32 v205, 0xffff0000, v179\n"
    "s_waitcnt vmcnt(30)\n"
    "v_lshlrev_b32 v206, 16, v180\n"
    "v_and_b32 v207, 0xffff0000, v180\n"
    "s_add_u32 s76, s76, 5120\n"
    "buffer_load_dwordx4 a[100:103], v6, s[8:11], s76 offen offset:0 sc1\n"
    "ds_write_b128 v168, a[164:167] offset:4608\n"
    "v_mfma_f32_16x16x32_bf16 a[200:203], v[160:163], a[164:167], a[200:203]\n"
    "buffer_load_dwordx4 a[104:107], v6, s[8:11], s76 offen offset:1024 sc1\n"
    "s_cmp_eq_u32 s98, 1\n"
    "s_cselect_b32 s66, -4, 1\n"
    "s_mulk_i32 s66, 6144\n"
    "v_add_u32 v166, s66, v166\n"
    "v_add_u32 v167, s66, v167\n"
    "ds_read_b64_tr_b16 a[144:145], v166 offset:0\n"
    "ds_read_b64_tr_b16 a[146:147], v167 offset:0\n"
    "ds_read_b64_tr_b16 a[148:149], v166 offset:32\n"
    "ds_read_b64_tr_b16 a[150:151], v167 offset:32\n"
    "ds_read_b64_tr_b16 a[152:153], v166 offset:64\n"
    "ds_read_b64_tr_b16 a[154:155], v167 offset:64\n"
    "ds_read_b64_tr_b16 a[156:157], v166 offset:1536\n"
    "ds_read_b64_tr_b16 a[158:159], v167 offset:1536\n"
    "ds_read_b64_tr_b16 a[160:161], v166 offset:1568\n"
    "ds_read_b64_tr_b16 a[162:163], v167 offset:1568\n"
    "ds_read_b64_tr_b16 a[164:165], v166 offset:1600\n"
    "ds_read_b64_tr_b16 a[166:167], v167 offset:1600\n"
    "v_add_u32 v168, s67, v168\n"
    "s_waitcnt lgkmcnt(15)\n"
    "ds_write_b128 v168, a[168:171] offset:0\n"
    "v_mfma_f32_16x16x32_bf16 a[192:195], v[160:163], a[168:171], a[192:195]\n"
    "buffer_load_dwordx4 a[108:111], v7, s[8:11], s76 offen offset:0 sc1\n"
    "buffer_load_dwordx2 v[238:239], v4, s[8:11], s23 offen sc1\n"
    "ds_write_b128 v168, a[172:175] offset:2048\n"
    "v_mfma_f32_16x16x32_bf16 a[196:199], v[160:163], a[172:175], a[196:199]\n"
    "buffer_load_dwordx4 a[112:115], v7, s[8:11], s76 offen offset:1024 sc1\n"
    "ds_write_b128 v168, a[176:179] offset:4096\n"
    "v_mfma_f32_16x16x32_bf16 a[200:203], v[160:163], a[176:179], a[200:203]\n"
    "buffer_load_dwordx4 a[116:119], v15, s[8:11], s76 offen offset:0 sc1\n"
    "s_add_u32 s76, s76, 5120\n"
    "buffer_load_dwordx4 a[120:123], v6, s[8:11], s76 offen offset:0 sc1\n"
    "ds_write_b128 v168, a[180:183] offset:512\n"
    "v_mfma_f32_16x16x32_bf16 a[192:195], v[160:163], a[180:183], a[192:195]\n"
    "buffer_load_dwordx4 a[124:127], v6, s[8:11], s76 offen offset:1024 sc1\n"
    "ds_write_b128 v168, a[184:187] offset:2560\n"
    "v_mfma_f32_16x16x32_bf16 a[196:199], v[160:163], a[184:187], a[196:199]\n"
    "buffer_load_dwordx4 a[128:131], v7, s[8:11], s76 offen offset:0 sc1\n"
    "buffer_load_dwordx4 a[132:135], v7, s[8:11], s76 offen offset:1024 sc1\n"
    "ds_write_b128 v168, a[188:191] offset:4608\n"
    "v_mfma_f32_16x16x32_bf16 a[200:203], v[160:163], a[188:191], a[200:203]\n"
    "buffer_load_dwordx4 a[136:139], v15, s[8:11], s76 offen offset:0 sc1\n"
    "v_add_u32 v168, s66, v168\n"
    "s_waitcnt lgkmcnt(15)\n"
    "ds_write_b128 v168, a[144:147] offset:0\n"
    "v_mfma_f32_16x16x32_bf16 a[192:195], v[160:163], a[144:147], a[192:195]\n"
    "s_add_u32 s76, s76, 5120\n"
    "buffer_load_dwordx4 a[140:143], v6, s[8:11], s76 offen offset:0 sc1\n"
    "s_waitcnt lgkmcnt(15)\n"
    "ds_write_b128 v168, a[148:151] offset:2048\n"
    "v_mfma_f32_16x16x32_bf16 a[196:199], v[160:163], a[148:151], a[196:199]\n"
    "s_waitcnt lgkmcnt(14)\n"
    "ds_write_b128 v168, a[152:155] offset:4096\n"
    "v_mfma_f32_16x16x32_bf16 a[200:203], v[160:163], a[152:155], a[200:203]\n"
    "s_waitcnt lgkmcnt(13)\n"
    "ds_write_b128 v168, a[156:159] offset:512\n"
    "v_mfma_f32_16x16x32_bf16 a[192:195], v[160:163], a[156:159], a[192:195]\n"
    "s_waitcnt lgkmcnt(12)\n"
    "ds_write_b128 v168, a[160:163] offset:2560\n"
    "v_mfma_f32_16x16x32_bf16 a[196:199], v[160:163], a[160:163], a[196:199]\n"
    "s_waitcnt lgkmcnt(11)\n"
    "ds_write_b128 v168, a[164:167] offset:4608\n"
    "v_mfma_f32_16x16x32_bf16 a[200:203], v[160:163], a[164:167], a[200:203]\n"
    "v_accvgpr_read_b32 v16, a192\n"
    "v_and_b32 v18, 0xffff0000, v16\n"
    "v_sub_f32 v17, v16, v18\n"
    "v_and_b32 v19, 0xffff0000, v17\n"
    "v_sub_f32 v17, v17, v19\n"
    "v_lshrrev_b32 v16, 16, v16\n"
    "v_or_b32 v16, v16, v19\n"
    "v_lshrrev_b32 v17, 16, v17\n"
    "ds_write_b64 v164, v[16:17] offset:0\n"
    "v_accvgpr_read_b32 v20, a196\n"
    "v_and_b32 v22, 0xffff0000, v20\n"
    "v_sub_f32 v21, v20, v22\n"
    "v_and_b32 v23, 0xffff0000, v21\n"
    "v_sub_f32 v21, v21, v23\n"
    "v_lshrrev_b32 v20, 16, v20\n"
    "v_or_b32 v20, v20, v23\n"
    "v_lshrrev_b32 v21, 16, v21\n"
    "ds_write_b64 v164, v[20:21] offset:2048\n"
    "v_accvgpr_read_b32 v24, a200\n"
    "v_and_b32 v26, 0xffff0000, v24\n"
    "v_sub_f32 v25, v24, v26\n"
    "v_and_b32 v27, 0xffff0000, v25\n"
    "v_sub_f32 v25, v25, v27\n"
    "v_lshrrev_b32 v24, 16, v24\n"
    "v_or_b32 v24, v24, v27\n"
    "v_lshrrev_b32 v25, 16, v25\n"
    "ds_write_b64 v164, v[24:25] offset:4096\n"
    "s_branch .Lf_ffn2\n"
    ".Lf_idle1:\n"
    "s_waitcnt lgkmcnt(0)\n"
    "buffer_load_dword v253, off, s[8:11], 0 sc1\n"
    "s_lshr_b32 s54, s6, 3\n"
    "s_and_b32 s55, s6, 7\n"
    "s_and_b32 s52, s5, 1\n"
    "s_lshl_b32 s52, s52, 2\n"
    "s_lshr_b32 s53, s5, 1\n"
    "s_add_u32 s97, s52, s53\n"
    "s_and_b32 s57, s6, 3\n"
    "s_add_u32 s52, s52, s57\n"
    "s_and_b32 s98, s6, 4\n"
    "s_add_u32 s98, s98, s54\n"
    "s_lshl_b32 s53, s53, 3\n"
    "s_add_u32 s53, s53, s98\n"
    "s_mul_i32 s98, s98, 5\n"
    "s_lshr_b32 s98, s98, 2\n"
    "s_add_u32 s98, s98, s57\n"
    "s_sub_u32 s57, s98, 10\n"
    "s_cselect_b32 s98, s98, s57\n"
    "s_mul_i32 s76, s55, 48\n"
    "s_mul_i32 s57, s4, 12\n"
    "s_add_u32 s76, s76, s57\n"
    "s_lshl_b32 s78, s76, 7\n"
    "s_add_u32 s78, s78, 4096\n"
    "s_mul_i32 s76, s76, 5120\n"
    "s_add_u32 s76, s76, 11849728\n"
    "s_lshr_b32 s93, s53, 2\n"
    "s_mul_i32 s93, s93, 384\n"
    "s_and_b32 s57, s53, 3\n"
    "s_lshl_b32 s58, s57, 7\n"
    "s_cmp_eq_u32 s57, 3\n"
    "s_cselect_b32 s58, 96, s58\n"
    "s_cselect_b32 s99, 96, 0\n"
    "s_add_u32 s93, s93, s58\n"
    "s_mul_i32 s57, s6, 13\n"
    "s_lshr_b32 s57, s57, 6\n"
    "s_mul_i32 s57, s57, 5\n"
    "s_sub_u32 s98, s6, s57\n"
    "s_mul_i32 s57, s4, 491520\n"
    "s_add_u32 s93, s93, s57\n"
    "s_add_u32 s94, s93, 491520\n"
    "s_mul_i32 s57, s98, 98304\n"
    "s_add_u32 s77, s93, s57\n"
    "s_mul_i32 s95, s4, 30720\n"
    "s_mul_i32 s92, s98, 6144\n"
    "s_add_u32 s92, s92, s95\n"
    "s_cmp_ge_u32 s6, 24\n"
    "s_cbranch_scc1 .Lf_heavy11\n"
    "s_sub_u32 s71, s6, 20\n"
    "v_lshrrev_b32 v8, 4, v1\n"
    "v_bfe_u32 v9, v1, 2, 2\n"
    "v_and_b32 v10, 3, v1\n"
    "v_add_u32 v11, 0, v1\n"
    "v_mul_u32_u24 v11, 205, v11\n"
    "v_lshrrev_b32 v11, 13, v11\n"
    "v_mul_u32_u24 v11, 640, v11\n"
    "v_add_u32 v240, v11, v2\n"
    "v_add_u32 v11, 64, v1\n"
    "v_mul_u32_u24 v11, 205, v11\n"
    "v_lshrrev_b32 v11, 13, v11\n"
    "v_mul_u32_u24 v11, 640, v11\n"
    "v_add_u32 v241, v11, v2\n"
    "v_add_u32 v11, 128, v1\n"
    "v_mul_u32_u24 v11, 205, v11\n"
    "v_lshrrev_b32 v11, 13, v11\n"
    "v_mul_u32_u24 v11, 640, v11\n"
    "v_add_u32 v242, v11, v2\n"
    "v_add_u32 v11, 192, v1\n"
    "v_mul_u32_u24 v11, 205, v11\n"
    "v_lshrrev_b32 v11, 13, v11\n"
    "v_mul_u32_u24 v11, 640, v11\n"
    "v_add_u32 v243, v11, v2\n"
    "v_add_u32 v11, 256, v1\n"
    "v_mul_u32_u24 v11, 205, v11\n"
    "v_lshrrev_b32 v11, 13, v11\n"
    "v_mul_u32_u24 v11, 640, v11\n"
    "v_add_u32 v244, v11, v2\n"
    "v_add_u32 v244, 0x1000, v244\n"
    "s_lshl_b32 s66, s71, 2\n"
    "s_lshr_b32 s58, s5, 1\n"
    "s_add_u32 s66, s66, s58\n"
    "s_lshl_b32 s57, s66, 1\n"
    "s_lshr_b32 s58, s4, 1\n"
    "s_add_u32 s57, s57, s58\n"
    "s_and_b32 s58, s57, 3\n"
    "s_lshl_b32 s59, s58, 2\n"
    "s_cmp_eq_u32 s58, 3\n"
    "s_cselect_b32 s58, 3, s59\n"
    "s_cselect_b32 s67, 4, 1\n"
    "s_mul_i32 s67, s67, 20480\n"
    "s_lshr_b32 s57, s57, 2\n"
    "s_mul_i32 s57, s57, 12\n"
    "s_add_u32 s57, s57, s58\n"
    "s_lshl_b32 s57, s57, 1\n"
    "s_and_b32 s58, s4, 1\n"
    "s_add_u32 s57, s57, s58\n"
    "s_mul_i32 s57, s57, 10240\n"
    "s_and_b32 s58, s5, 1\n"
    "s_mul_i32 s58, s58, 640\n"
    "s_add_u32 s73, s57, s58\n"
    "s_mul_i32 m0, s4, 5184\n"
    "s_sleep 10\n"
    "buffer_load_dwordx4 v240, s[40:43], s73 offen offset:0 lds\n"
    "buffer_load_dwordx4 v241, s[40:43], s73 offen offset:1024 lds\n"
    "buffer_load_dwordx4 v242, s[40:43], s73 offen offset:2048 lds\n"
    "buffer_load_dwordx4 v243, s[40:43], s73 offen offset:3072 lds\n"
    "s_add_u32 m0, m0, 4096\n"
    "s_nop 0\n"
    "buffer_load_dwordx4 v244, s[40:43], s73 offen lds\n"
    "s_add_u32 s73, s73, s67\n"
    "s_add_u32 m0, m0, 16640\n"
    "s_nop 0\n"
    "buffer_load_dwordx4 v240, s[40:43], s73 offen offset:0 lds\n"
    "buffer_load_dwordx4 v241, s[40:43], s73 offen offset:1024 lds\n"
    "buffer_load_dwordx4 v242, s[40:43], s73 offen offset:2048 lds\n"
    "buffer_load_dwordx4 v243, s[40:43], s73 offen offset:3072 lds\n"
    "s_add_u32 m0, m0, 4096\n"
    "s_nop 0\n"
    "buffer_load_dwordx4 v244, s[40:43], s73 offen lds\n"
    "s_add_u32 s73, s73, s67\n"
    "s_add_u32 m0, m0, 16640\n"
    "s_nop 0\n"
    "buffer_load_dwordx4 v240, s[40:43], s73 offen offset:0 lds\n"
    "buffer_load_dwordx4 v241, s[40:43], s73 offen offset:1024 lds\n"
    "buffer_load_dwordx4 v242, s[40:43], s73 offen offset:2048 lds\n"
    "buffer_load_dwordx4 v243, s[40:43], s73 offen offset:3072 lds\n"
    "s_add_u32 m0, m0, 4096\n"
    "s_nop 0\n"
    "buffer_load_dwordx4 v244, s[40:43], s73 offen lds\n"
    "v_add_u32 v11, 0, v1\n"
    "v_mul_u32_u24 v11, 171, v11\n"
    "v_lshrrev_b32 v11, 10, v11\n"
    "v_mul_u32_u24 v12, 2976, v11\n"
    "v_add_u32 v169, v12, v2\n"
    "v_and_b32 v11, 4, v11\n"
    "v_mul_u32_u24 v11, 6144, v11\n"
    "v_sub_u32 v11, 12288, v11\n"
    "v_add_u32 v172, v169, v11\n"
    "v_add_u32 v11, 64, v1\n"
    "v_mul_u32_u24 v11, 171, v11\n"
    "v_lshrrev_b32 v11, 10, v11\n"
    "v_mul_u32_u24 v12, 2976, v11\n"
    "v_add_u32 v170, v12, v2\n"
    "v_and_b32 v11, 4, v11\n"
    "v_mul_u32_u24 v11, 6144, v11\n"
    "v_sub_u32 v11, 12288, v11\n"
    "v_add_u32 v173, v170, v11\n"
    "v_add_u32 v11, 128, v1\n"
    "v_mul_u32_u24 v11, 171, v11\n"
    "v_lshrrev_b32 v11, 10, v11\n"
    "v_mul_u32_u24 v12, 2976, v11\n"
    "v_add_u32 v171, v12, v2\n"
    "v_and_b32 v11, 4, v11\n"
    "v_mul_u32_u24 v11, 6144, v11\n"
    "v_sub_u32 v11, 12288, v11\n"
    "v_add_u32 v174, v171, v11\n"
    "v_lshlrev_b32 v175, 7, v1\n"
    "v_cmp_gt_u32 vcc, 12, v1\n"
    "v_mov_b32 v0, 0x70000000\n"
    "s_nop 0\n"
    "v_cndmask_b32 v175, v0, v175, vcc\n"
    "v_add_u32 v3, 0x10000, v2\n"
    "v_lshlrev_b32 v4, 8, v1\n"
    "s_and_b32 s57, s4, 1\n"
    "s_lshl_b32 s57, s57, 11\n"
    "v_add_u32 v6, s57, v2\n"
    "v_xor_b32 v7, 0x800, v6\n"
    "s_lshr_b32 s57, s57, 6\n"
    "s_or_b32 s57, s57, 0x1000\n"
    "v_xor_b32 v15, s57, v2\n"
    "v_cmp_gt_u32 vcc, 8, v1\n"
    "v_mov_b32 v0, 0x70000000\n"
    "s_nop 0\n"
    "v_cndmask_b32 v4, v0, v4, vcc\n"
    "v_mov_b32 v60, 0\n"
    "v_mov_b32 v61, 0\n"
    "v_lshlrev_b32 v62, 3, v1\n"
    "v_and_b32 v62, 8, v62\n"
    "v_add_u32 v62, 122880, v62\n"
    "ds_write_b64 v62, v[60:61]\n"
    "v_and_b32 v159, 0x70, v2\n"
    "v_and_b32 v155, 8, v1\n"
    "v_lshl_add_u32 v158, v155, 5, v159\n"
    "v_add_u32 v155, v155, v159\n"
    "v_lshrrev_b32 v159, 4, v1\n"
    "v_lshl_add_u32 v152, v159, 9, v158\n"
    "v_lshl_add_u32 v153, v159, 16, v158\n"
    "v_mul_u32_u24 v157, 80, v158\n"
    "v_lshl_add_u32 v156, v159, 4, v157\n"
    "v_lshl_add_u32 v157, v159, 2, v157\n"
    "v_and_b32 v154, 1, v159\n"
    "v_lshl_add_u32 v154, v154, 3, v158\n"
    "v_lshrrev_b32 v159, 1, v159\n"
    "v_lshl_add_u32 v154, v159, 15, v154\n"
    "s_lshl_b32 s58, s97, 2\n"
    "s_add_u32 s58, s58, s54\n"
    "s_lshr_b32 s59, s4, 1\n"
    "s_and_b32 s60, s4, 1\n"
    "s_lshl_b32 s28, s58, 2\n"
    "s_add_u32 s28, s28, s4\n"
    "s_mul_i32 s29, s28, 24576\n"
    "s_lshl_b32 s28, s28, 11\n"
    "s_add_u32 s28, s28, 315392\n"
    "s_lshl_b32 s61, s59, 10\n"
    "s_add_u32 s29, s29, s61\n"
    "s_add_u32 s29, s29, 20140032\n"
    "s_lshl_b32 s61, s59, 7\n"
    "s_mul_i32 s30, s58, 40960\n"
    "s_mul_i32 s62, s55, 5120\n"
    "s_add_u32 s30, s30, s62\n"
    "s_add_u32 s30, s30, s61\n"
    "s_add_u32 s30, s30, 13815808\n"
    "s_lshl_b32 s62, s60, 9\n"
    "s_add_u32 s31, s30, s62\n"
    "s_add_u32 s31, s31, 4096\n"
    "s_lshl_b32 s63, s60, 11\n"
    "s_add_u32 s30, s30, s63\n"
    "s_mul_i32 s32, s55, 655360\n"
    "s_lshl_b32 s63, s58, 9\n"
    "s_and_b32 s63, s63, -1\n"
    "s_add_u32 s32, s32, s63\n"
    "s_add_u32 s32, s32, s61\n"
    "s_add_u32 s32, s32, 6606848\n"
    "s_lshl_b32 s63, s60, 16\n"
    "s_add_u32 s33, s32, s63\n"
    "s_add_u32 s33, s33, 524288\n"
    "s_lshl_b32 s63, s60, 18\n"
    "s_add_u32 s32, s32, s63\n"
    "s_lshl_b32 s35, s58, 13\n"
    "s_add_u32 s35, s35, s61\n"
    "s_add_u32 s35, s35, s61\n"
    "s_add_u32 s35, s35, 19582976\n"
    "s_lshl_b32 s63, s55, 10\n"
    "s_add_u32 s34, s35, s63\n"
    "s_add_u32 s34, s34, s62\n"
    "s_lshl_b32 s36, s58, 12\n"
    "s_lshl_b32 s63, s55, 9\n"
    "s_add_u32 s36, s36, s63\n"
    "s_add_u32 s36, s36, s61\n"
    "s_lshl_b32 s63, s60, 8\n"
    "s_add_u32 s36, s36, s63\n"
    "s_add_u32 s36, s36, 19320832\n"
    "s_lshl_b32 s37, s58, 3\n"
    "s_add_u32 s37, s37, s55\n"
    "s_lshl_b32 s37, s37, 8\n"
    "s_add_u32 s37, s37, 184320\n"
    "s_lshl_b32 s38, s58, 2\n"
    "s_add_u32 s38, s38, s59\n"
    "s_mul_i32 s38, s38, 10240\n"
    "s_mul_i32 s63, s55, 160\n"
    "s_add_u32 s38, s38, s63\n"
    "s_lshl_b32 s63, s60, 4\n"
    "s_add_u32 s39, s38, s63\n"
    "s_add_u32 s39, s39, 128\n"
    "s_lshl_b32 s63, s60, 6\n"
    "s_add_u32 s38, s38, s63\n"
    "s_lshl_b32 s96, s58, 9\n"
    "s_add_u32 s96, s96, s61\n"
    "s_add_u32 s96, s96, 20107264\n"
    "s_lshr_b32 s57, s4, 1\n"
    "s_lshl_b32 s57, s57, 8\n"
    "s_and_b32 s58, s4, 1\n"
    "s_lshl_b32 s58, s58, 3\n"
    "s_add_u32 s57, s57, s58\n"
    "s_add_u32 s57, s57, 123008\n"
    "s_movk_i32 s59, 384\n"
    "v_bfe_u32 v160, v1, 4, 1\n"
    "v_and_b32 v168, 0xf0, v2\n"
    "v_lshl_add_u32 v168, v160, 10, v168\n"
    "v_lshrrev_b32 v162, 5, v1\n"
    "v_lshl_add_u32 v164, v162, 9, v168\n"
    "v_add_u32 v164, s57, v164\n"
    "v_lshl_add_u32 v168, v162, 8, v168\n"
    "v_add_u32 v168, s92, v168\n"
    "v_bfe_u32 v161, v1, 2, 2\n"
    "v_lshl_add_u32 v162, v162, 3, v161\n"
    "v_mul_u32_u24 v162, 96, v162\n"
    "v_and_b32 v163, 3, v1\n"
    "v_lshlrev_b32 v163, 3, v163\n"
    "v_add3_u32 v162, v162, v163, s92\n"
    "v_mul_u32_u24 v166, 3456, v160\n"
    "v_mul_u32_u24 v167, 2688, v160\n"
    "v_add_u32 v166, v166, v162\n"
    "v_add3_u32 v167, v167, v162, s59\n"
    "v_xor_b32 v160, v160, v161\n"
    "v_and_b32 v160, 1, v160\n"
    "v_add_u32 v160, -1, v160\n"
    "v_and_b32 v160, 0x3f803f80, v160\n"
    "v_mov_b32 v161, v160\n"
    "v_mov_b32 v162, v160\n"
    "v_mov_b32 v163, v160\n"
    "v_lshrrev_b32 v11, 3, v1\n"
    "v_mul_u32_u24 v232, 1152, v11\n"
    "v_add_u32 v232, v232, v2\n"
    "v_lshlrev_b32 v233, 4, v11\n"
    "v_and_b32 v11, 7, v1\n"
    "v_cmp_eq_u32 vcc, 0, v11\n"
    "v_mov_b32 v11, 0x70000000\n"
    "s_nop 0\n"
    "v_cndmask_b32 v233, v11, v233, vcc\n"
    "s_lshl_b32 s64, s71, 2\n"
    "s_add_u32 s64, s64, s4\n"
    "s_lshl_b32 s57, s97, 4\n"
    "s_add_u32 s64, s64, s57\n"
    "s_lshl_b32 s65, s64, 7\n"
    "s_add_u32 s65, s65, 20107264\n"
    "s_mul_i32 s64, s64, 10240\n"
    "v_mul_u32_u24 v11, 5184, v8\n"
    "v_mul_u32_u24 v12, 640, v9\n"
    "s_mul_i32 s57, s4, 160\n"
    "v_add3_u32 v11, v11, v12, s57\n"
    "v_lshl_add_u32 v12, v10, 3, v11\n"
    "v_lshl_add_u32 v11, v10, 4, v11\n"
    "v_and_b32 v8, 49, v1\n"
    "v_bfe_u32 v9, v1, 1, 2\n"
    "v_lshl_or_b32 v8, v9, 2, v8\n"
    "v_bfe_u32 v9, v1, 3, 1\n"
    "v_lshl_or_b32 v8, v9, 1, v8\n"
    "v_lshlrev_b32 v8, 4, v8\n"
    "v_add_u32 v8, 0x1000, v8\n"
    "s_and_b32 s58, s5, 1\n"
    "s_lshl_b32 s58, s58, 2\n"
    "s_add_u32 s58, s58, s4\n"
    "s_mul_i32 s58, s58, 48\n"
    "s_lshl_b32 s57, s71, 3\n"
    "s_add_u32 s57, s57, s66\n"
    "s_add_u32 s57, s57, s58\n"
    "s_mul_i32 s74, s57, 5120\n"
    "s_add_u32 s74, s74, 11849728\n"
    "s_lshl_b32 s75, s57, 7\n"
    "s_add_u32 s75, s75, 4096\n"
    "s_waitcnt vmcnt(15)\n"
    "v_readfirstlane_b32 s50, v253\n"
    "s_nop 0\n"
    "s_mul_i32 s50, s50, 0x19660d\n"
    "s_add_u32 s50, s50, 0x3c6ef35f\n"
    "s_not_b32 s51, s50\n"
    "v_mov_b32 v252, s50\n"
    "v_mov_b32 v253, s51\n"
    "s_waitcnt vmcnt(10)\n"
    "s_waitcnt lgkmcnt(0)\n"
    "s_barrier\n"
    "ds_read_b64_tr_b16 v[128:129], v11 offset:0\n"
    "ds_read_b64_tr_b16 v[130:131], v11 offset:2560\n"
    "ds_read_b64_tr_b16 v[132:133], v11 offset:8\n"
    "ds_read_b64_tr_b16 v[134:135], v11 offset:2568\n"
    "ds_read_b64_tr_b16 v[136:137], v11 offset:64\n"
    "ds_read_b64_tr_b16 v[138:139], v11 offset:2624\n"
    "ds_read_b64_tr_b16 v[140:141], v11 offset:72\n"
    "ds_read_b64_tr_b16 v[142:143], v11 offset:2632\n"
    "ds_read_b64_tr_b16 v[144:145], v12 offset:128\n"
    "ds_read_b64_tr_b16 v[146:147], v12 offset:2688\n"
    "s_waitcnt lgkmcnt(8)\n"
    "buffer_store_dwordx4 v[128:131], v2, s[8:11], s74 offen offset:0 sc1\n"
    "s_waitcnt lgkmcnt(6)\n"
    "buffer_store_dwordx4 v[132:135], v2, s[8:11], s74 offen offset:1024 sc1\n"
    "s_waitcnt lgkmcnt(4)\n"
    "buffer_store_dwordx4 v[136:139], v2, s[8:11], s74 offen offset:2048 sc1\n"
    "s_waitcnt lgkmcnt(2)\n"
    "buffer_store_dwordx4 v[140:143], v2, s[8:11], s74 offen offset:3072 sc1\n"
    "s_waitcnt lgkmcnt(0)\n"
    "buffer_store_dwordx4 v[144:147], v8, s[8:11], s74 offen sc1\n"
    "s_waitcnt vmcnt(10)\n"
    "s_waitcnt lgkmcnt(0)\n"
    "s_barrier\n"
    "ds_read_b64_tr_b16 v[128:129], v11 offset:20736\n"
    "ds_read_b64_tr_b16 v[130:131], v11 offset:23296\n"
    "ds_read_b64_tr_b16 v[132:133], v11 offset:20744\n"
    "ds_read_b64_tr_b16 v[134:135], v11 offset:23304\n"
    "ds_read_b64_tr_b16 v[136:137], v11 offset:20800\n"
    "ds_read_b64_tr_b16 v[138:139], v11 offset:23360\n"
    "ds_read_b64_tr_b16 v[140:141], v11 offset:20808\n"
    "ds_read_b64_tr_b16 v[142:143], v11 offset:23368\n"
    "ds_read_b64_tr_b16 v[144:145], v12 offset:20864\n"
    "ds_read_b64_tr_b16 v[146:147], v12 offset:23424\n"
    "s_add_u32 s74, s74, 20480\n"
    "s_waitcnt lgkmcnt(8)\n"
    "buffer_store_dwordx4 v[128:131], v2, s[8:11], s74 offen offset:0 sc1\n"
    "s_waitcnt lgkmcnt(6)\n"
    "buffer_store_dwordx4 v[132:135], v2, s[8:11], s74 offen offset:1024 sc1\n"
    "s_waitcnt lgkmcnt(4)\n"
    "buffer_store_dwordx4 v[136:139], v2, s[8:11], s74 offen offset:2048 sc1\n"
    "s_waitcnt lgkmcnt(2)\n"
    "buffer_store_dwordx4 v[140:143], v2, s[8:11], s74 offen offset:3072 sc1\n"
    "s_waitcnt lgkmcnt(0)\n"
    "buffer_store_dwordx4 v[144:147], v8, s[8:11], s74 offen sc1\n"
    "s_waitcnt vmcnt(10)\n"
    "s_waitcnt lgkmcnt(0)\n"
    "s_barrier\n"
    "ds_read_b64_tr_b16 v[128:129], v11 offset:41472\n"
    "ds_read_b64_tr_b16 v[130:131], v11 offset:44032\n"
    "ds_read_b64_tr_b16 v[132:133], v11 offset:41480\n"
    "ds_read_b64_tr_b16 v[134:135], v11 offset:44040\n"
    "ds_read_b64_tr_b16 v[136:137], v11 offset:41536\n"
    "ds_read_b64_tr_b16 v[138:139], v11 offset:44096\n"
    "ds_read_b64_tr_b16 v[140:141], v11 offset:41544\n"
    "ds_read_b64_tr_b16 v[142:143], v11 offset:44104\n"
    "ds_read_b64_tr_b16 v[144:145], v12 offset:41600\n"
    "ds_read_b64_tr_b16 v[146:147], v12 offset:44160\n"
    "s_add_u32 s74, s74, 20480\n"
    "s_waitcnt lgkmcnt(8)\n"
    "buffer_store_dwordx4 v[128:131], v2, s[8:11], s74 offen offset:0 sc1\n"
    "s_waitcnt lgkmcnt(6)\n"
    "buffer_store_dwordx4 v[132:135], v2, s[8:11], s74 offen offset:1024 sc1\n"
    "s_waitcnt lgkmcnt(4)\n"
    "buffer_store_dwordx4 v[136:139], v2, s[8:11], s74 offen offset:2048 sc1\n"
    "s_waitcnt lgkmcnt(2)\n"
    "buffer_store_dwordx4 v[140:143], v2, s[8:11], s74 offen offset:3072 sc1\n"
    "s_waitcnt lgkmcnt(0)\n"
    "buffer_store_dwordx4 v[144:147], v8, s[8:11], s74 offen sc1\n"
    "buffer_load_dwordx4 v[16:19], v232, s[20:23], s64 offen offset:0\n"
    "buffer_load_dwordx4 v[20:23], v232, s[20:23], s64 offen offset:128\n"
    "buffer_load_dwordx4 v[24:27], v232, s[20:23], s64 offen offset:256\n"
    "buffer_load_dwordx4 v[28:31], v232, s[20:23], s64 offen offset:384\n"
    "buffer_load_dwordx4 v[32:35], v232, s[20:23], s64 offen offset:512\n"
    "buffer_load_dwordx4 v[36:39], v232, s[20:23], s64 offen offset:640\n"
    "buffer_load_dwordx4 v[40:43], v232, s[20:23], s64 offen offset:768\n"
    "buffer_load_dwordx4 v[44:47], v232, s[20:23], s64 offen offset:896\n"
    "buffer_load_dwordx4 v[48:51], v232, s[20:23], s64 offen offset:1024\n"
    "buffer_load_dwordx4 v[52:55], v232, s[20:23], s64 offen offset:1152\n"
    "s_waitcnt vmcnt(20)\n"
    "s_mov_b64 exec, 0xff\n"
    "buffer_store_dwordx4 v[252:255], v2, s[8:11], s75 offen sc1\n"
    "s_mov_b64 exec, -1\n"
    "s_add_u32 s75, s75, 512\n"
    "s_waitcnt vmcnt(16)\n"
    "s_mov_b64 exec, 0xff\n"
    "buffer_store_dwordx4 v[252:255], v2, s[8:11], s75 offen sc1\n"
    "s_mov_b64 exec, -1\n"
    "s_add_u32 s75, s75, 512\n"
    "s_waitcnt vmcnt(12)\n"
    "s_mov_b64 exec, 0xff\n"
    "buffer_store_dwordx4 v[252:255], v2, s[8:11], s75 offen sc1\n"
    "s_mov_b64 exec, -1\n"
    "s_add_u32 s64, s64, 1310720\n"
    "buffer_load_dwordx4 v[64:67], v232, s[20:23], s64 offen offset:0\n"
    "buffer_load_dwordx4 v[68:71], v232, s[20:23], s64 offen offset:128\n"
    "buffer_load_dwordx4 v[72:75], v232, s[20:23], s64 offen offset:256\n"
    "buffer_load_dwordx4 v[76:79], v232, s[20:23], s64 offen offset:384\n"
    "buffer_load_dwordx4 v[80:83], v232, s[20:23], s64 offen offset:512\n"
    "buffer_load_dwordx4 v[84:87], v232, s[20:23], s64 offen offset:640\n"
    "buffer_load_dwordx4 v[88:91], v232, s[20:23], s64 offen offset:768\n"
    "buffer_load_dwordx4 v[92:95], v232, s[20:23], s64 offen offset:896\n"
    "buffer_load_dwordx4 v[96:99], v232, s[20:23], s64 offen offset:1024\n"
    "buffer_load_dwordx4 v[100:103], v232, s[20:23], s64 offen offset:1152\n"
    "s_waitcnt lgkmcnt(0)\n"
    "s_barrier\n"
    "v_mov_b32 v60, 0x3f803f80\n"
    "s_waitcnt vmcnt(22)\n"
    "v_dot2_f32_bf16 v56, v16, v60, 0\n"
    "v_dot2_f32_bf16 v58, v16, v16, 0\n"
    "v_dot2_f32_bf16 v57, v17, v60, 0\n"
    "v_dot2_f32_bf16 v59, v17, v17, 0\n"
    "v_dot2_f32_bf16 v56, v18, v60, v56\n"
    "v_dot2_f32_bf16 v58, v18, v18, v58\n"
    "v_dot2_f32_bf16 v57, v19, v60, v57\n"
    "v_dot2_f32_bf16 v59, v19, v19, v59\n"
    "s_waitcnt vmcnt(21)\n"
    "v_dot2_f32_bf16 v56, v20, v60, v56\n"
    "v_dot2_f32_bf16 v58, v20, v20, v58\n"
    "v_dot2_f32_bf16 v57, v21, v60, v57\n"
    "v_dot2_f32_bf16 v59, v21, v21, v59\n"
    "v_dot2_f32_bf16 v56, v22, v60, v56\n"
    "v_dot2_f32_bf16 v58, v22, v22, v58\n"
    "v_dot2_f32_bf16 v57, v23, v60, v57\n"
    "v_dot2_f32_bf16 v59, v23, v23, v59\n"
    "s_waitcnt vmcnt(20)\n"
    "v_dot2_f32_bf16 v56, v24, v60, v56\n"
    "v_dot2_f32_bf16 v58, v24, v24, v58\n"
    "v_dot2_f32_bf16 v57, v25, v60, v57\n"
    "v_dot2_f32_bf16 v59, v25, v25, v59\n"
    "v_dot2_f32_bf16 v56, v26, v60, v56\n"
    "v_dot2_f32_bf16 v58, v26, v26, v58\n"
    "v_dot2_f32_bf16 v57, v27, v60, v57\n"
    "v_dot2_f32_bf16 v59, v27, v27, v59\n"
    "s_waitcnt vmcnt(19)\n"
    "v_dot2_f32_bf16 v56, v28, v60, v56\n"
    "v_dot2_f32_bf16 v58, v28, v28, v58\n"
    "v_dot2_f32_bf16 v57, v29, v60, v57\n"
    "v_dot2_f32_bf16 v59, v29, v29, v59\n"
    "v_dot2_f32_bf16 v56, v30, v60, v56\n"
    "v_dot2_f32_bf16 v58, v30, v30, v58\n"
    "v_dot2_f32_bf16 v57, v31, v60, v57\n"
    "v_dot2_f32_bf16 v59, v31, v31, v59\n"
    "s_waitcnt vmcnt(18)\n"
    "v_dot2_f32_bf16 v56, v32, v60, v56\n"
    "v_dot2_f32_bf16 v58, v32, v32, v58\n"
    "v_dot2_f32_bf16 v57, v33, v60, v57\n"
    "v_dot2_f32_bf16 v59, v33, v33, v59\n"
    "v_dot2_f32_bf16 v56, v34, v60, v56\n"
    "v_dot2_f32_bf16 v58, v34, v34, v58\n"
    "v_dot2_f32_bf16 v57, v35, v60, v57\n"
    "v_dot2_f32_bf16 v59, v35, v35, v59\n"
    "s_waitcnt vmcnt(17)\n"
    "v_dot2_f32_bf16 v56, v36, v60, v56\n"
    "v_dot2_f32_bf16 v58, v36, v36, v58\n"
    "v_dot2_f32_bf16 v57, v37, v60, v57\n"
    "v_dot2_f32_bf16 v59, v37, v37, v59\n"
    "v_dot2_f32_bf16 v56, v38, v60, v56\n"
    "v_dot2_f32_bf16 v58, v38, v38, v58\n"
    "v_dot2_f32_bf16 v57, v39, v60, v57\n"
    "v_dot2_f32_bf16 v59, v39, v39, v59\n"
    "s_waitcnt vmcnt(16)\n"
    "v_dot2_f32_bf16 v56, v40, v60, v56\n"
    "v_dot2_f32_bf16 v58, v40, v40, v58\n"
    "v_dot2_f32_bf16 v57, v41, v60, v57\n"
    "v_dot2_f32_bf16 v59, v41, v41, v59\n"
    "v_dot2_f32_bf16 v56, v42, v60, v56\n"
    "v_dot2_f32_bf16 v58, v42, v42, v58\n"
    "v_dot2_f32_bf16 v57, v43, v60, v57\n"
    "v_dot2_f32_bf16 v59, v43, v43, v59\n"
    "s_waitcnt vmcnt(15)\n"
    "v_dot2_f32_bf16 v56, v44, v60, v56\n"
    "v_dot2_f32_bf16 v58, v44, v44, v58\n"
    "v_dot2_f32_bf16 v57, v45, v60, v57\n"
    "v_dot2_f32_bf16 v59, v45, v45, v59\n"
    "v_dot2_f32_bf16 v56, v46, v60, v56\n"
    "v_dot2_f32_bf16 v58, v46, v46, v58\n"
    "v_dot2_f32_bf16 v57, v47, v60, v57\n"
    "v_dot2_f32_bf16 v59, v47, v47, v59\n"
    "s_waitcnt vmcnt(14)\n"
    "v_dot2_f32_bf16 v56, v48, v60, v56\n"
    "v_dot2_f32_bf16 v58, v48, v48, v58\n"
    "v_dot2_f32_bf16 v57, v49, v60, v57\n"
    "v_dot2_f32_bf16 v59, v49, v49, v59\n"
    "v_dot2_f32_bf16 v56, v50, v60, v56\n"
    "v_dot2_f32_bf16 v58, v50, v50, v58\n"
    "v_dot2_f32_bf16 v57, v51, v60, v57\n"
    "v_dot2_f32_bf16 v59, v51, v51, v59\n"
    "s_waitcnt vmcnt(13)\n"
    "v_dot2_f32_bf16 v56, v52, v60, v56\n"
    "v_dot2_f32_bf16 v58, v52, v52, v58\n"
    "v_dot2_f32_bf16 v57, v53, v60, v57\n"
    "v_dot2_f32_bf16 v59, v53, v53, v59\n"
    "v_dot2_f32_bf16 v56, v54, v60, v56\n"
    "v_dot2_f32_bf16 v58, v54, v54, v58\n"
    "v_dot2_f32_bf16 v57, v55, v60, v57\n"
    "v_dot2_f32_bf16 v59, v55, v55, v59\n"
    "s_nop 1\n"
    "v_add_f32 v56, v56, v57\n"
    "v_add_f32 v58, v58, v59\n"
    "s_nop 0\n"
    "v_add_f32_dpp v56, v56, v56 quad_perm:[1,0,3,2] row_mask:0xf bank_mask:0xf\n"
    "v_add_f32_dpp v58, v58, v58 quad_perm:[1,0,3,2] row_mask:0xf bank_mask:0xf\n"
    "s_nop 0\n"
    "v_add_f32_dpp v56, v56, v56 quad_perm:[2,3,0,1] row_mask:0xf bank_mask:0xf\n"
    "v_add_f32_dpp v58, v58, v58 quad_perm:[2,3,0,1] row_mask:0xf bank_mask:0xf\n"
    "s_nop 0\n"
    "v_add_f32_dpp v56, v56, v56 row_half_mirror row_mask:0xf bank_mask:0xf\n"
    "v_add_f32_dpp v58, v58, v58 row_half_mirror row_mask:0xf bank_mask:0xf\n"
    "v_mul_f32 v56, 0x3acccccd, v56\n"
    "v_mul_f32 v58, 0x3acccccd, v58\n"
    "v_fma_f32 v58, -v56, v56, v58\n"
    "v_max_f32 v58, 0, v58\n"
    "v_cvt_pk_bf16_f32 v58, v58, v58\n"
    "v_lshlrev_b32 v58, 16, v58\n"
    "v_add_f32 v58, 0x3727c5ac, v58\n"
    "v_cvt_pk_bf16_f32 v58, v58, v58\n"
    "v_lshlrev_b32 v58, 16, v58\n"
    "v_sqrt_f32 v58, v58\n"
    "s_nop 0\n"
    "v_cvt_pk_bf16_f32 v58, v58, v58\n"
    "v_lshlrev_b32 v58, 16, v58\n"
    "v_cvt_pk_bf16_f32 v56, v56, v56\n"
    "v_rcp_f32 v58, v58\n"
    "v_lshlrev_b32 v56, 16, v56\n"
    "v_mov_b32 v57, s50\n"
    "v_mov_b32 v59, s50\n"
    "v_mul_f32 v56, v56, v58\n"
    "buffer_store_dwordx4 v[56:59], v233, s[8:11], s65 offen sc1\n"
    "s_add_u32 m0, s92, 0\n"
    "s_nop 0\n"
    "buffer_load_dwordx4 v169, s[12:15], s77 offen offset:0 lds\n"
    "buffer_load_dwordx4 v170, s[12:15], s77 offen offset:1024 lds\n"
    "buffer_load_dwordx4 v171, s[12:15], s77 offen offset:2048 lds\n"
    "s_add_u32 m0, s92, 3072\n"
    "s_nop 0\n"
    "buffer_load_dwordx4 v172, s[16:19], s77 offen offset:0 lds\n"
    "buffer_load_dwordx4 v173, s[16:19], s77 offen offset:1024 lds\n"
    "buffer_load_dwordx4 v174, s[16:19], s77 offen offset:2048 lds\n"
    "s_add_u32 s65, s65, 16384\n"
    "v_mov_b32 v60, 0x3f803f80\n"
    "s_waitcnt vmcnt(16)\n"
    "v_dot2_f32_bf16 v56, v64, v60, 0\n"
    "v_dot2_f32_bf16 v58, v64, v64, 0\n"
    "v_dot2_f32_bf16 v57, v65, v60, 0\n"
    "v_dot2_f32_bf16 v59, v65, v65, 0\n"
    "v_dot2_f32_bf16 v56, v66, v60, v56\n"
    "v_dot2_f32_bf16 v58, v66, v66, v58\n"
    "v_dot2_f32_bf16 v57, v67, v60, v57\n"
    "v_dot2_f32_bf16 v59, v67, v67, v59\n"
    "s_waitcnt vmcnt(15)\n"
    "v_dot2_f32_bf16 v56, v68, v60, v56\n"
    "v_dot2_f32_bf16 v58, v68, v68, v58\n"
    "v_dot2_f32_bf16 v57, v69, v60, v57\n"
    "v_dot2_f32_bf16 v59, v69, v69, v59\n"
    "v_dot2_f32_bf16 v56, v70, v60, v56\n"
    "v_dot2_f32_bf16 v58, v70, v70, v58\n"
    "v_dot2_f32_bf16 v57, v71, v60, v57\n"
    "v_dot2_f32_bf16 v59, v71, v71, v59\n"
    "s_waitcnt vmcnt(14)\n"
    "v_dot2_f32_bf16 v56, v72, v60, v56\n"
    "v_dot2_f32_bf16 v58, v72, v72, v58\n"
    "v_dot2_f32_bf16 v57, v73, v60, v57\n"
    "v_dot2_f32_bf16 v59, v73, v73, v59\n"
    "v_dot2_f32_bf16 v56, v74, v60, v56\n"
    "v_dot2_f32_bf16 v58, v74, v74, v58\n"
    "v_dot2_f32_bf16 v57, v75, v60, v57\n"
    "v_dot2_f32_bf16 v59, v75, v75, v59\n"
    "s_waitcnt vmcnt(13)\n"
    "v_dot2_f32_bf16 v56, v76, v60, v56\n"
    "v_dot2_f32_bf16 v58, v76, v76, v58\n"
    "v_dot2_f32_bf16 v57, v77, v60, v57\n"
    "v_dot2_f32_bf16 v59, v77, v77, v59\n"
    "v_dot2_f32_bf16 v56, v78, v60, v56\n"
    "v_dot2_f32_bf16 v58, v78, v78, v58\n"
    "v_dot2_f32_bf16 v57, v79, v60, v57\n"
    "v_dot2_f32_bf16 v59, v79, v79, v59\n"
    "s_waitcnt vmcnt(12)\n"
    "v_dot2_f32_bf16 v56, v80, v60, v56\n"
    "v_dot2_f32_bf16 v58, v80, v80, v58\n"
    "v_dot2_f32_bf16 v57, v81, v60, v57\n"
    "v_dot2_f32_bf16 v59, v81, v81, v59\n"
    "v_dot2_f32_bf16 v56, v82, v60, v56\n"
    "v_dot2_f32_bf16 v58, v82, v82, v58\n"
    "v_dot2_f32_bf16 v57, v83, v60, v57\n"
    "v_dot2_f32_bf16 v59, v83, v83, v59\n"
    "s_waitcnt vmcnt(11)\n"
    "v_dot2_f32_bf16 v56, v84, v60, v56\n"
    "v_dot2_f32_bf16 v58, v84, v84, v58\n"
    "v_dot2_f32_bf16 v57, v85, v60, v57\n"
    "v_dot2_f32_bf16 v59, v85, v85, v59\n"
    "v_dot2_f32_bf16 v56, v86, v60, v56\n"
    "v_dot2_f32_bf16 v58, v86, v86, v58\n"
    "v_dot2_f32_bf16 v57, v87, v60, v57\n"
    "v_dot2_f32_bf16 v59, v87, v87, v59\n"
    "s_waitcnt vmcnt(10)\n"
    "v_dot2_f32_bf16 v56, v88, v60, v56\n"
    "v_dot2_f32_bf16 v58, v88, v88, v58\n"
    "v_dot2_f32_bf16 v57, v89, v60, v57\n"
    "v_dot2_f32_bf16 v59, v89, v89, v59\n"
    "v_dot2_f32_bf16 v56, v90, v60, v56\n"
    "v_dot2_f32_bf16 v58, v90, v90, v58\n"
    "v_dot2_f32_bf16 v57, v91, v60, v57\n"
    "v_dot2_f32_bf16 v59, v91, v91, v59\n"
    "s_waitcnt vmcnt(9)\n"
    "v_dot2_f32_bf16 v56, v92, v60, v56\n"
    "v_dot2_f32_bf16 v58, v92, v92, v58\n"
    "v_dot2_f32_bf16 v57, v93, v60, v57\n"
    "v_dot2_f32_bf16 v59, v93, v93, v59\n"
    "v_dot2_f32_bf16 v56, v94, v60, v56\n"
    "v_dot2_f32_bf16 v58, v94, v94, v58\n"
    "v_dot2_f32_bf16 v57, v95, v60, v57\n"
    "v_dot2_f32_bf16 v59, v95, v95, v59\n"
    "s_waitcnt vmcnt(8)\n"
    "v_dot2_f32_bf16 v56, v96, v60, v56\n"
    "v_dot2_f32_bf16 v58, v96, v96, v58\n"
    "v_dot2_f32_bf16 v57, v97, v60, v57\n"
    "v_dot2_f32_bf16 v59, v97, v97, v59\n"
    "v_dot2_f32_bf16 v56, v98, v60, v56\n"
    "v_dot2_f32_bf16 v58, v98, v98, v58\n"
    "v_dot2_f32_bf16 v57, v99, v60, v57\n"
    "v_dot2_f32_bf16 v59, v99, v99, v59\n"
    "s_waitcnt vmcnt(7)\n"
    "v_dot2_f32_bf16 v56, v100, v60, v56\n"
    "v_dot2_f32_bf16 v58, v100, v100, v58\n"
    "v_dot2_f32_bf16 v57, v101, v60, v57\n"
    "v_dot2_f32_bf16 v59, v101, v101, v59\n"
    "v_dot2_f32_bf16 v56, v102, v60, v56\n"
    "v_dot2_f32_bf16 v58, v102, v102, v58\n"
    "v_dot2_f32_bf16 v57, v103, v60, v57\n"
    "v_dot2_f32_bf16 v59, v103, v103, v59\n"
    "s_nop 1\n"
    "v_add_f32 v56, v56, v57\n"
    "v_add_f32 v58, v58, v59\n"
    "s_nop 0\n"
    "v_add_f32_dpp v56, v56, v56 quad_perm:[1,0,3,2] row_mask:0xf bank_mask:0xf\n"
    "v_add_f32_dpp v58, v58, v58 quad_perm:[1,0,3,2] row_mask:0xf bank_mask:0xf\n"
    "s_nop 0\n"
    "v_add_f32_dpp v56, v56, v56 quad_perm:[2,3,0,1] row_mask:0xf bank_mask:0xf\n"
    "v_add_f32_dpp v58, v58, v58 quad_perm:[2,3,0,1] row_mask:0xf bank_mask:0xf\n"
    "s_nop 0\n"
    "v_add_f32_dpp v56, v56, v56 row_half_mirror row_mask:0xf bank_mask:0xf\n"
    "v_add_f32_dpp v58, v58, v58 row_half_mirror row_mask:0xf bank_mask:0xf\n"
    "v_mul_f32 v56, 0x3acccccd, v56\n"
    "v_mul_f32 v58, 0x3acccccd, v58\n"
    "v_fma_f32 v58, -v56, v56, v58\n"
    "v_max_f32 v58, 0, v58\n"
    "v_cvt_pk_bf16_f32 v58, v58, v58\n"
    "v_lshlrev_b32 v58, 16, v58\n"
    "v_add_f32 v58, 0x3727c5ac, v58\n"
    "v_cvt_pk_bf16_f32 v58, v58, v58\n"
    "v_lshlrev_b32 v58, 16, v58\n"
    "v_sqrt_f32 v58, v58\n"
    "s_nop 0\n"
    "v_cvt_pk_bf16_f32 v58, v58, v58\n"
    "v_lshlrev_b32 v58, 16, v58\n"
    "v_cvt_pk_bf16_f32 v56, v56, v56\n"
    "v_rcp_f32 v58, v58\n"
    "v_lshlrev_b32 v56, 16, v56\n"
    "v_mov_b32 v57, s50\n"
    "v_mov_b32 v59, s50\n"
    "v_mul_f32 v56, v56, v58\n"
    "buffer_store_dwordx4 v[56:59], v233, s[8:11], s65 offen sc1\n"
    "buffer_load_dwordx4 v[208:211], v156, s[20:23], s38 offen\n"
    "buffer_load_dword v231, v157, s[20:23], s39 offen\n"
    "s_add_u32 s88, s38, 1310720\n"
    "buffer_load_dwordx4 v[176:179], v156, s[20:23], s88 offen\n"
    "s_add_u32 s89, s39, 1310720\n"
    "buffer_load_dword v180, v157, s[20:23], s89 offen\n"
    "s_add_u32 s92, s92, 6144\n"
    "s_add_u32 s77, s77, 98304\n"
    "s_cmp_eq_u32 s77, s94\n"
    "s_cselect_b32 s92, s95, s92\n"
    "s_mov_b32 m0, s92\n"
    "s_cselect_b32 s77, s93, s77\n"
    "buffer_load_dwordx4 v169, s[12:15], s77 offen offset:0 lds\n"
    "buffer_load_dwordx4 v170, s[12:15], s77 offen offset:1024 lds\n"
    "buffer_load_dwordx4 v171, s[12:15], s77 offen offset:2048 lds\n"
    "s_add_u32 m0, s92, 3072\n"
    "s_nop 0\n"
    "buffer_load_dwordx4 v172, s[16:19], s77 offen offset:0 lds\n"
    "buffer_load_dwordx4 v173, s[16:19], s77 offen offset:1024 lds\n"
    "buffer_load_dwordx4 v174, s[16:19], s77 offen offset:2048 lds\n"
    "s_add_u32 s92, s92, 6144\n"
    "s_add_u32 s77, s77, 98304\n"
    "s_cmp_eq_u32 s77, s94\n"
    "s_cselect_b32 s92, s95, s92\n"
    "s_mov_b32 m0, s92\n"
    "s_cselect_b32 s77, s93, s77\n"
    "buffer_load_dwordx4 v169, s[12:15], s77 offen offset:0 lds\n"
    "buffer_load_dwordx4 v170, s[12:15], s77 offen offset:1024 lds\n"
    "buffer_load_dwordx4 v171, s[12:15], s77 offen offset:2048 lds\n"
    "s_add_u32 m0, s92, 3072\n"
    "s_nop 0\n"
    "buffer_load_dwordx4 v172, s[16:19], s77 offen offset:0 lds\n"
    "buffer_load_dwordx4 v173, s[16:19], s77 offen offset:1024 lds\n"
    "buffer_load_dwordx4 v174, s[16:19], s77 offen offset:2048 lds\n"
    "buffer_load_dwordx2 v[244:245], v175, s[8:11], s78 offen sc1\n"
    "s_add_u32 s92, s92, 6144\n"
    "s_add_u32 s77, s77, 98304\n"
    "s_cmp_eq_u32 s77, s94\n"
    "s_cselect_b32 s92, s95, s92\n"
    "s_mov_b32 m0, s92\n"
    "s_cselect_b32 s77, s93, s77\n"
    "buffer_load_dwordx4 v169, s[12:15], s77 offen offset:0 lds\n"
    "buffer_load_dwordx4 v170, s[12:15], s77 offen offset:1024 lds\n"
    "buffer_load_dwordx4 v171, s[12:15], s77 offen offset:2048 lds\n"
    "s_add_u32 s72, s96, 0\n"
    "s_add_u32 s68, s32, 0\n"
    "s_add_u32 s69, s32, 32768\n"
    "s_add_u32 s70, s33, 0\n"
    "buffer_load_dwordx4 v[188:191], v158, s[8:11], s72 offen sc1\n"
    "buffer_load_dwordx4 v[212:215], v153, s[8:11], s68 offen sc1\n"
    "buffer_load_dwordx4 v[216:219], v153, s[8:11], s69 offen sc1\n"
    "buffer_load_dwordx2 v[220:221], v154, s[8:11], s70 offen sc1\n"
    "s_add_u32 m0, s92, 3072\n"
    "s_nop 0\n"
    "buffer_load_dwordx4 v172, s[16:19], s77 offen offset:0 lds\n"
    "buffer_load_dwordx4 v173, s[16:19], s77 offen offset:1024 lds\n"
    "buffer_load_dwordx4 v174, s[16:19], s77 offen offset:2048 lds\n"
    "s_waitcnt vmcnt(26)\n"
    "v_lshlrev_b32 v222, 16, v208\n"
    "v_and_b32 v223, 0xffff0000, v208\n"
    "v_lshlrev_b32 v224, 16, v209\n"
    "v_and_b32 v225, 0xffff0000, v209\n"
    "v_lshlrev_b32 v226, 16, v210\n"
    "v_and_b32 v227, 0xffff0000, v210\n"
    "v_lshlrev_b32 v228, 16, v211\n"
    "v_and_b32 v229, 0xffff0000, v211\n"
    "s_waitcnt vmcnt(25)\n"
    "v_lshlrev_b32 v230, 16, v231\n"
    "v_and_b32 v231, 0xffff0000, v231\n"
    "s_waitcnt vmcnt(6)\n"
    "v_xor_b32 v246, s50, v189\n"
    "v_bitop3_b32 v246, v246, v191, s50 bitop3:0xf6\n"
    "s_waitcnt vmcnt(5)\n"
    "v_bitop3_b32 v246, v246, v213, s50 bitop3:0xf6\n"
    "v_bitop3_b32 v246, v246, v215, s50 bitop3:0xf6\n"
    "s_waitcnt vmcnt(4)\n"
    "v_bitop3_b32 v246, v246, v217, s50 bitop3:0xf6\n"
    "v_bitop3_b32 v246, v246, v219, s50 bitop3:0xf6\n"
    "s_waitcnt vmcnt(3)\n"
    "v_bitop3_b32 v246, v246, v221, s50 bitop3:0xf6\n"
    "v_cmp_ne_u32 vcc, 0, v246\n"
    "s_nop 0\n"
    "s_cmp_lg_u64 vcc, 0\n"
    "s_cbranch_scc1 .Lf_gslow12\n"
    ".Lf_gback13:\n"
    "v_and_b32 v213, 0xffff0000, v212\n"
    "v_lshlrev_b32 v212, 16, v212\n"
    "v_and_b32 v215, 0xffff0000, v214\n"
    "v_lshlrev_b32 v214, 16, v214\n"
    "v_and_b32 v217, 0xffff0000, v216\n"
    "v_lshlrev_b32 v216, 16, v216\n"
    "v_and_b32 v219, 0xffff0000, v218\n"
    "v_lshlrev_b32 v218, 16, v218\n"
    "v_and_b32 v221, 0xffff0000, v220\n"
    "v_lshlrev_b32 v220, 16, v220\n"
    "v_pk_fma_f32 v[192:193], v[222:223], v[190:191], v[188:189] op_sel_hi:[1,0,0] neg_lo:[0,0,1] neg_hi:[0,0,1]\n"
    "v_pk_fma_f32 v[194:195], v[224:225], v[190:191], v[188:189] op_sel_hi:[1,0,0] neg_lo:[0,0,1] neg_hi:[0,0,1]\n"
    "v_pk_fma_f32 v[196:197], v[226:227], v[190:191], v[188:189] op_sel_hi:[1,0,0] neg_lo:[0,0,1] neg_hi:[0,0,1]\n"
    "v_pk_fma_f32 v[198:199], v[228:229], v[190:191], v[188:189] op_sel_hi:[1,0,0] neg_lo:[0,0,1] neg_hi:[0,0,1]\n"
    "v_pk_fma_f32 v[200:201], v[230:231], v[190:191], v[188:189] op_sel_hi:[1,0,0] neg_lo:[0,0,1] neg_hi:[0,0,1]\n"
    "v_pk_fma_f32 v[222:223], v[192:193], v[212:213], v[222:223]\n"
    "v_pk_fma_f32 v[224:225], v[194:195], v[214:215], v[224:225]\n"
    "v_pk_fma_f32 v[226:227], v[196:197], v[216:217], v[226:227]\n"
    "v_pk_fma_f32 v[228:229], v[198:199], v[218:219], v[228:229]\n"
    "v_pk_fma_f32 v[230:231], v[200:201], v[220:221], v[230:231]\n"
    "v_cvt_pk_bf16_f32 v196, v230, v231\n"
    "v_cvt_pk_bf16_f32 v192, v222, v223\n"
    "v_mov_b32 v197, v196\n"
    "v_cvt_pk_bf16_f32 v193, v224, v225\n"
    "v_cvt_pk_bf16_f32 v194, v226, v227\n"
    "v_cvt_pk_bf16_f32 v195, v228, v229\n"
    "v_permlane16_swap_b32 v196, v197\n"
    "v_mov_b32 v198, v196\n"
    "v_mov_b32 v199, v197\n"
    "v_pk_add_f32 v[188:189], v[222:223], v[224:225]\n"
    "v_pk_mul_f32 v[190:191], v[222:223], v[222:223]\n"
    "buffer_store_dwordx4 v[192:195], v152, s[8:11], s30 offen sc1\n"
    "v_permlane32_swap_b32 v196, v198\n"
    "v_permlane32_swap_b32 v197, v199\n"
    "v_pk_fma_f32 v[190:191], v[224:225], v[224:225], v[190:191]\n"
    "v_pk_add_f32 v[188:189], v[188:189], v[226:227]\n"
    "v_pk_fma_f32 v[190:191], v[226:227], v[226:227], v[190:191]\n"
    "v_pk_add_f32 v[188:189], v[188:189], v[228:229]\n"
    "v_pk_fma_f32 v[190:191], v[228:229], v[228:229], v[190:191]\n"
    "v_pk_add_f32 v[188:189], v[188:189], v[230:231]\n"
    "v_pk_fma_f32 v[190:191], v[230:231], v[230:231], v[190:191]\n"
    "v_add_f32 v188, v188, v189\n"
    "v_add_f32 v190, v190, v191\n"
    "v_mov_b32 v189, v190\n"
    "s_nop 1\n"
    "v_permlane16_swap_b32 v188, v189\n"
    "v_add_f32 v188, v188, v189\n"
    "v_mov_b32 v189, v188\n"
    "s_nop 1\n"
    "v_permlane32_swap_b32 v188, v189\n"
    "v_add_f32 v188, v188, v189\n"
    "v_mov_b32 v189, v188\n"
    "s_nop 1\n"
    "v_permlane16_swap_b32 v188, v189\n"
    "s_add_u32 s90, s36, -262144\n"
    "s_mov_b64 exec, 0xffff\n"
    "buffer_store_dwordx4 v[196:199], v152, s[8:11], s31 offen sc1\n"
    "buffer_store_dwordx2 v[188:189], v155, s[8:11], s90 offen sc1\n"
    "s_mov_b64 exec, -1\n"
    "s_add_u32 s92, s92, 6144\n"
    "s_add_u32 s77, s77, 98304\n"
    "s_cmp_eq_u32 s77, s94\n"
    "s_cselect_b32 s92, s95, s92\n"
    "s_mov_b32 m0, s92\n"
    "s_cselect_b32 s77, s93, s77\n"
    "buffer_load_dwordx4 v169, s[12:15], s77 offen offset:0 lds\n"
    "buffer_load_dwordx4 v170, s[12:15], s77 offen offset:1024 lds\n"
    "buffer_load_dwordx4 v171, s[12:15], s77 offen offset:2048 lds\n"
    "s_add_u32 m0, s92, 3072\n"
    "s_nop 0\n"
    "buffer_load_dwordx4 v172, s[16:19], s77 offen offset:0 lds\n"
    "buffer_load_dwordx4 v173, s[16:19], s77 offen offset:1024 lds\n"
    "buffer_load_dwordx4 v174, s[16:19], s77 offen offset:2048 lds\n"
    "v_cmp_eq_u64 vcc, s[50:51], v[244:245]\n"
    "s_nop 0\n"
    "s_andn2_b32 s79, 0xfff, vcc_lo\n"
    "s_cbranch_scc1 .Lf_slow14\n"
    ".Lf_back15:\n"
    "ds_read_b64_tr_b16 a[144:145], v166 offset:0\n"
    "ds_read_b64_tr_b16 a[146:147], v167 offset:0\n"
    "ds_read_b64_tr_b16 a[148:149], v166 offset:32\n"
    "ds_read_b64_tr_b16 a[150:151], v167 offset:32\n"
    "ds_read_b64_tr_b16 a[152:153], v166 offset:64\n"
    "ds_read_b64_tr_b16 a[154:155], v167 offset:64\n"
    "ds_read_b64_tr_b16 a[156:157], v166 offset:1536\n"
    "ds_read_b64_tr_b16 a[158:159], v167 offset:1536\n"
    "ds_read_b64_tr_b16 a[160:161], v166 offset:1568\n"
    "ds_read_b64_tr_b16 a[162:163], v167 offset:1568\n"
    "ds_read_b64_tr_b16 a[164:165], v166 offset:1600\n"
    "ds_read_b64_tr_b16 a[166:167], v167 offset:1600\n"
    "s_waitcnt lgkmcnt(10)\n"
    "ds_write_b128 v168, a[144:147] offset:0\n"
    "v_mfma_f32_16x16x32_bf16 a[192:195], v[160:163], a[144:147], 0\n"
    "s_waitcnt lgkmcnt(9)\n"
    "ds_write_b128 v168, a[148:151] offset:2048\n"
    "v_mfma_f32_16x16x32_bf16 a[196:199], v[160:163], a[148:151], 0\n"
    "s_waitcnt lgkmcnt(8)\n"
    "ds_write_b128 v168, a[152:155] offset:4096\n"
    "v_mfma_f32_16x16x32_bf16 a[200:203], v[160:163], a[152:155], 0\n"
    "s_waitcnt lgkmcnt(7)\n"
    "ds_write_b128 v168, a[156:159] offset:512\n"
    "v_mfma_f32_16x16x32_bf16 a[192:195], v[160:163], a[156:159], a[192:195]\n"
    "s_waitcnt lgkmcnt(6)\n"
    "ds_write_b128 v168, a[160:163] offset:2560\n"
    "v_mfma_f32_16x16x32_bf16 a[196:199], v[160:163], a[160:163], a[196:199]\n"
    "s_waitcnt lgkmcnt(5)\n"
    "ds_write_b128 v168, a[164:167] offset:4608\n"
    "v_mfma_f32_16x16x32_bf16 a[200:203], v[160:163], a[164:167], a[200:203]\n"
    "s_waitcnt vmcnt(6)\n"
    "s_mov_b64 exec, 1\n"
    "ds_add_rtn_u32 v165, v3, v3 offset:57344\n"
    "s_mov_b64 exec, -1\n"
    "s_waitcnt lgkmcnt(0)\n"
    "v_readfirstlane_b32 s79, v165\n"
    "s_add_u32 s91, s37, -131072\n"
    "s_cmp_eq_u32 s79, 0x30000\n"
    "s_cselect_b64 exec, 0xff, 0\n"
    "buffer_store_dwordx4 v[252:255], v2, s[8:11], s91 offen sc1\n"
    "s_mov_b64 exec, -1\n"
    "buffer_load_dwordx4 a[0:3], v6, s[8:11], s76 offen offset:0 sc1\n"
    "buffer_load_dwordx4 a[4:7], v6, s[8:11], s76 offen offset:1024 sc1\n"
    "buffer_load_dwordx4 a[8:11], v7, s[8:11], s76 offen offset:0 sc1\n"
    "buffer_load_dwordx4 a[12:15], v7, s[8:11], s76 offen offset:1024 sc1\n"
    "buffer_load_dwordx4 a[16:19], v15, s[8:11], s76 offen offset:0 sc1\n"
    "s_add_u32 s76, s76, 5120\n"
    "buffer_load_dwordx4 a[20:23], v6, s[8:11], s76 offen offset:0 sc1\n"
    "s_cmp_eq_u32 s98, 4\n"
    "s_cselect_b32 s67, -4, 1\n"
    "s_mulk_i32 s67, 6144\n"
    "v_add_u32 v166, s67, v166\n"
    "v_add_u32 v167, s67, v167\n"
    "ds_read_b64_tr_b16 a[168:169], v166 offset:0\n"
    "ds_read_b64_tr_b16 a[170:171], v167 offset:0\n"
    "ds_read_b64_tr_b16 a[172:173], v166 offset:32\n"
    "ds_read_b64_tr_b16 a[174:175], v167 offset:32\n"
    "ds_read_b64_tr_b16 a[176:177], v166 offset:64\n"
    "ds_read_b64_tr_b16 a[178:179], v167 offset:64\n"
    "ds_read_b64_tr_b16 a[180:181], v166 offset:1536\n"
    "ds_read_b64_tr_b16 a[182:183], v167 offset:1536\n"
    "ds_read_b64_tr_b16 a[184:185], v166 offset:1568\n"
    "ds_read_b64_tr_b16 a[186:187], v167 offset:1568\n"
    "ds_read_b64_tr_b16 a[188:189], v166 offset:1600\n"
    "ds_read_b64_tr_b16 a[190:191], v167 offset:1600\n"
    "v_add_u32 v168, s67, v168\n"
    "s_waitcnt lgkmcnt(10)\n"
    "ds_write_b128 v168, a[168:171] offset:0\n"
    "v_mfma_f32_16x16x32_bf16 a[192:195], v[160:163], a[168:171], a[192:195]\n"
    "s_waitcnt lgkmcnt(9)\n"
    "ds_write_b128 v168, a[172:175] offset:2048\n"
    "v_mfma_f32_16x16x32_bf16 a[196:199], v[160:163], a[172:175], a[196:199]\n"
    "s_waitcnt lgkmcnt(8)\n"
    "ds_write_b128 v168, a[176:179] offset:4096\n"
    "v_mfma_f32_16x16x32_bf16 a[200:203], v[160:163], a[176:179], a[200:203]\n"
    "s_waitcnt lgkmcnt(7)\n"
    "ds_write_b128 v168, a[180:183] offset:512\n"
    "v_mfma_f32_16x16x32_bf16 a[192:195], v[160:163], a[180:183], a[192:195]\n"
    "s_waitcnt lgkmcnt(6)\n"
    "ds_write_b128 v168, a[184:187] offset:2560\n"
    "v_mfma_f32_16x16x32_bf16 a[196:199], v[160:163], a[184:187], a[196:199]\n"
    "s_waitcnt lgkmcnt(5)\n"
    "ds_write_b128 v168, a[188:191] offset:4608\n"
    "v_mfma_f32_16x16x32_bf16 a[200:203], v[160:163], a[188:191], a[200:203]\n"
    "s_cmp_eq_u32 s98, 3\n"
    "s_cselect_b32 s66, -4, 1\n"
    "s_mulk_i32 s66, 6144\n"
    "v_add_u32 v166, s66, v166\n"
    "v_add_u32 v167, s66, v167\n"
    "ds_read_b64_tr_b16 a[144:145], v166 offset:0\n"
    "ds_read_b64_tr_b16 a[146:147], v167 offset:0\n"
    "ds_read_b64_tr_b16 a[148:149], v166 offset:32\n"
    "ds_read_b64_tr_b16 a[150:151], v167 offset:32\n"
    "ds_read_b64_tr_b16 a[152:153], v166 offset:64\n"
    "ds_read_b64_tr_b16 a[154:155], v167 offset:64\n"
    "ds_read_b64_tr_b16 a[156:157], v166 offset:1536\n"
    "ds_read_b64_tr_b16 a[158:159], v167 offset:1536\n"
    "ds_read_b64_tr_b16 a[160:161], v166 offset:1568\n"
    "ds_read_b64_tr_b16 a[162:163], v167 offset:1568\n"
    "ds_read_b64_tr_b16 a[164:165], v166 offset:1600\n"
    "ds_read_b64_tr_b16 a[166:167], v167 offset:1600\n"
    "s_cmp_eq_u32 s98, 2\n"
    "s_cselect_b32 s67, -4, 1\n"
    "s_mulk_i32 s67, 6144\n"
    "v_add_u32 v166, s67, v166\n"
    "v_add_u32 v167, s67, v167\n"
    "ds_read_b64_tr_b16 a[168:169], v166 offset:0\n"
    "ds_read_b64_tr_b16 a[170:171], v167 offset:0\n"
    "ds_read_b64_tr_b16 a[172:173], v166 offset:32\n"
    "ds_read_b64_tr_b16 a[174:175], v167 offset:32\n"
    "ds_read_b64_tr_b16 a[176:177], v166 offset:64\n"
    "ds_read_b64_tr_b16 a[178:179], v167 offset:64\n"
    "ds_read_b64_tr_b16 a[180:181], v166 offset:1536\n"
    "ds_read_b64_tr_b16 a[182:183], v167 offset:1536\n"
    "ds_read_b64_tr_b16 a[184:185], v166 offset:1568\n"
    "ds_read_b64_tr_b16 a[186:187], v167 offset:1568\n"
    "ds_read_b64_tr_b16 a[188:189], v166 offset:1600\n"
    "ds_read_b64_tr_b16 a[190:191], v167 offset:1600\n"
    "v_add_u32 v168, s66, v168\n"
    "s_waitcnt lgkmcnt(15)\n"
    "ds_write_b128 v168, a[144:147] offset:0\n"
    "v_mfma_f32_16x16x32_bf16 a[192:195], v[160:163], a[144:147], a[192:195]\n"
    "buffer_load_dwordx4 a[24:27], v6, s[8:11], s76 offen offset:1024 sc1\n"
    "s_lshl_b32 s57, s52, 2\n"
    "s_add_u32 s57, s57, s4\n"
    "s_lshl_b32 s23, s57, 11\n"
    "buffer_load_dwordx4 a[28:31], v7, s[8:11], s76 offen offset:0 sc1\n"
    "s_add_u32 s23, s23, 53248\n"
    "s_mov_b32 s12, 0\n"
    "s_mov_b32 s13, 4096\n"
    "ds_write_b128 v168, a[148:151] offset:2048\n"
    "v_mfma_f32_16x16x32_bf16 a[196:199], v[160:163], a[148:151], a[196:199]\n"
    "buffer_load_dwordx4 a[32:35], v7, s[8:11], s76 offen offset:1024 sc1\n"
    "s_mov_b32 s14, 8192\n"
    "s_mov_b32 s15, 12288\n"
    "s_mov_b32 s16, 16384\n"
    "ds_write_b128 v168, a[152:155] offset:4096\n"
    "v_mfma_f32_16x16x32_bf16 a[200:203], v[160:163], a[152:155], a[200:203]\n"
    "buffer_load_dwordx4 a[36:39], v15, s[8:11], s76 offen offset:0 sc1\n"
    "s_mov_b32 s17, 20480\n"
    "s_mov_b32 s18, 24576\n"
    "s_mov_b32 s19, 28672\n"
    "s_add_u32 s76, s76, 5120\n"
    "buffer_load_dwordx4 a[40:43], v6, s[8:11], s76 offen offset:0 sc1\n"
    "s_mov_b32 s20, 32768\n"
    "s_mov_b32 s40, 36864\n"
    "s_mul_i32 s21, s57, 40960\n"
    "ds_write_b128 v168, a[156:159] offset:512\n"
    "v_mfma_f32_16x16x32_bf16 a[192:195], v[160:163], a[156:159], a[192:195]\n"
    "buffer_load_dwordx4 a[44:47], v6, s[8:11], s76 offen offset:1024 sc1\n"
    "s_add_u32 s21, s21, 13815808\n"
    "s_lshl_b32 s22, s57, 12\n"
    "s_add_u32 s22, s22, 19058688\n"
    "s_waitcnt lgkmcnt(15)\n"
    "ds_write_b128 v168, a[160:163] offset:2560\n"
    "v_mfma_f32_16x16x32_bf16 a[196:199], v[160:163], a[160:163], a[196:199]\n"
    "buffer_load_dwordx4 a[48:51], v7, s[8:11], s76 offen offset:0 sc1\n"
    "s_lshl_b32 s27, s57, 5\n"
    "s_add_u32 s27, s27, s53\n"
    "s_lshl_b32 s27, s27, 8\n"
    "buffer_load_dwordx4 a[52:55], v7, s[8:11], s76 offen offset:1024 sc1\n"
    "s_add_u32 s27, s27, 315392\n"
    "s_mul_i32 s61, s57, 98304\n"
    "s_add_u32 s61, s61, 20140032\n"
    "ds_write_b128 v168, a[164:167] offset:4608\n"
    "v_mfma_f32_16x16x32_bf16 a[200:203], v[160:163], a[164:167], a[200:203]\n"
    "buffer_load_dwordx4 a[56:59], v15, s[8:11], s76 offen offset:0 sc1\n"
    "s_lshr_b32 s62, s53, 3\n"
    "s_lshl_b32 s62, s62, 14\n"
    "s_add_u32 s61, s61, s62\n"
    "s_cmp_eq_u32 s98, 1\n"
    "s_cselect_b32 s66, -4, 1\n"
    "s_mulk_i32 s66, 6144\n"
    "v_add_u32 v166, s66, v166\n"
    "v_add_u32 v167, s66, v167\n"
    "s_waitcnt vmcnt(16)\n"
    "ds_read_b64_tr_b16 a[144:145], v166 offset:0\n"
    "ds_read_b64_tr_b16 a[146:147], v167 offset:0\n"
    "ds_read_b64_tr_b16 a[148:149], v166 offset:32\n"
    "ds_read_b64_tr_b16 a[150:151], v167 offset:32\n"
    "ds_read_b64_tr_b16 a[152:153], v166 offset:64\n"
    "ds_read_b64_tr_b16 a[154:155], v167 offset:64\n"
    "ds_read_b64_tr_b16 a[156:157], v166 offset:1536\n"
    "ds_read_b64_tr_b16 a[158:159], v167 offset:1536\n"
    "ds_read_b64_tr_b16 a[160:161], v166 offset:1568\n"
    "ds_read_b64_tr_b16 a[162:163], v167 offset:1568\n"
    "ds_read_b64_tr_b16 a[164:165], v166 offset:1600\n"
    "ds_read_b64_tr_b16 a[166:167], v167 offset:1600\n"
    "v_add_u32 v168, s67, v168\n"
    "s_waitcnt lgkmcnt(15)\n"
    "ds_write_b128 v168, a[168:171] offset:0\n"
    "v_mfma_f32_16x16x32_bf16 a[192:195], v[160:163], a[168:171], a[192:195]\n"
    "s_add_u32 s76, s76, 5120\n"
    "buffer_load_dwordx4 a[60:63], v6, s[8:11], s76 offen offset:0 sc1\n"
    "s_lshr_b32 s62, s53, 1\n"
    "s_lshl_b32 s62, s62, 11\n"
    "s_add_u32 s61, s61, s62\n"
    "buffer_load_dwordx4 a[64:67], v6, s[8:11], s76 offen offset:1024 sc1\n"
    "s_and_b32 s62, s53, 1\n"
    "s_lshl_b32 s62, s62, 9\n"
    "s_add_u32 s24, s61, s62\n"
    "ds_write_b128 v168, a[172:175] offset:2048\n"
    "v_mfma_f32_16x16x32_bf16 a[196:199], v[160:163], a[172:175], a[196:199]\n"
    "buffer_load_dwordx4 a[68:71], v7, s[8:11], s76 offen offset:0 sc1\n"
    "s_add_u32 s25, s24, 8192\n"
    "s_add_u32 s26, s24, 16384\n"
    "v_and_b32 v0, 0xf0, v2\n"
    "ds_write_b128 v168, a[176:179] offset:4096\n"
    "v_mfma_f32_16x16x32_bf16 a[200:203], v[160:163], a[176:179], a[200:203]\n"
    "buffer_load_dwordx4 a[72:75], v7, s[8:11], s76 offen offset:1024 sc1\n"
    "v_lshrrev_b32 v232, 4, v1\n"
    "v_lshrrev_b32 v233, 5, v1\n"
    "v_and_b32 v234, 1, v232\n"
    "buffer_load_dwordx4 a[76:79], v15, s[8:11], s76 offen offset:0 sc1\n"
    "v_lshl_add_u32 v8, v232, 11, v0\n"
    "v_and_b32 v0, 0x70, v2\n"
    "v_and_b32 v11, 8, v1\n"
    "ds_write_b128 v168, a[180:183] offset:512\n"
    "v_mfma_f32_16x16x32_bf16 a[192:195], v[160:163], a[180:183], a[192:195]\n"
    "s_add_u32 s76, s76, 5120\n"
    "buffer_load_dwordx4 a[80:83], v6, s[8:11], s76 offen offset:0 sc1\n"
    "v_lshl_add_u32 v10, v11, 7, v0\n"
    "v_lshl_add_u32 v10, v234, 7, v10\n"
    "v_lshl_add_u32 v10, v233, 8, v10\n"
    "ds_write_b128 v168, a[184:187] offset:2560\n"
    "v_mfma_f32_16x16x32_bf16 a[196:199], v[160:163], a[184:187], a[196:199]\n"
    "buffer_load_dwordx4 a[84:87], v6, s[8:11], s76 offen offset:1024 sc1\n"
    "v_lshl_add_u32 v13, v11, 5, v0\n"
    "v_add_u32 v11, v11, v0\n"
    "v_lshl_add_u32 v12, v232, 9, v13\n"
    "buffer_load_dwordx4 a[88:91], v7, s[8:11], s76 offen offset:0 sc1\n"
    "v_lshl_add_u32 v14, v232, 16, v13\n"
    "v_lshl_add_u32 v5, v233, 15, v13\n"
    "v_lshl_add_u32 v5, v234, 3, v5\n"
    "ds_write_b128 v168, a[188:191] offset:4608\n"
    "v_mfma_f32_16x16x32_bf16 a[200:203], v[160:163], a[188:191], a[200:203]\n"
    "buffer_load_dwordx4 a[92:95], v7, s[8:11], s76 offen offset:1024 sc1\n"
    "v_lshl_add_u32 v13, v232, 2, v13\n"
    "v_add_u32 v235, s21, v2\n"
    "v_and_b32 v62, 0x1ff, v12\n"
    "v_add_u32 v168, s66, v168\n"
    "s_waitcnt lgkmcnt(15)\n"
    "ds_write_b128 v168, a[144:147] offset:0\n"
    "v_mfma_f32_16x16x32_bf16 a[192:195], v[160:163], a[144:147], a[192:195]\n"
    "buffer_load_dwordx4 a[96:99], v15, s[8:11], s76 offen offset:0 sc1\n"
    "v_lshl_add_u32 v235, v232, 8, v235\n"
    "s_add_u32 s84, s96, 16384\n"
    "s_add_u32 s74, s32, 16384\n"
    "s_add_u32 s76, s76, 5120\n"
    "buffer_load_dwordx4 a[100:103], v6, s[8:11], s76 offen offset:0 sc1\n"
    "s_add_u32 s75, s32, 49152\n"
    "s_add_u32 s77, s33, 16384\n"
    "s_movk_i32 s41, 0\n"
    "s_waitcnt lgkmcnt(15)\n"
    "ds_write_b128 v168, a[148:151] offset:2048\n"
    "v_mfma_f32_16x16x32_bf16 a[196:199], v[160:163], a[148:151], a[196:199]\n"
    "buffer_load_dwordx4 a[104:107], v6, s[8:11], s76 offen offset:1024 sc1\n"
    "s_movk_i32 s42, 4096\n"
    "s_movk_i32 s43, 8192\n"
    "s_movk_i32 s48, 12288\n"
    "s_waitcnt lgkmcnt(14)\n"
    "ds_write_b128 v168, a[152:155] offset:4096\n"
    "v_mfma_f32_16x16x32_bf16 a[200:203], v[160:163], a[152:155], a[200:203]\n"
    "buffer_load_dwordx4 a[108:111], v7, s[8:11], s76 offen offset:0 sc1\n"
    "s_movk_i32 s49, 16384\n"
    "s_movk_i32 s85, 20480\n"
    "s_lshl_b32 s63, s4, 11\n"
    "s_add_u32 s63, s63, 131072\n"
    "v_add_u32 v254, s63, v2\n"
    "v_xor_b32 v232, 0x800, v254\n"
    "s_waitcnt lgkmcnt(13)\n"
    "ds_write_b128 v168, a[156:159] offset:512\n"
    "v_mfma_f32_16x16x32_bf16 a[192:195], v[160:163], a[156:159], a[192:195]\n"
    "v_xor_b32 v233, 0x1000, v254\n"
    "v_xor_b32 v234, 0x1800, v254\n"
    "s_waitcnt lgkmcnt(12)\n"
    "ds_write_b128 v168, a[160:163] offset:2560\n"
    "v_mfma_f32_16x16x32_bf16 a[196:199], v[160:163], a[160:163], a[196:199]\n"
    "v_lshlrev_b32 v198, 16, v176\n"
    "v_and_b32 v199, 0xffff0000, v176\n"
    "v_lshlrev_b32 v200, 16, v177\n"
    "v_and_b32 v201, 0xffff0000, v177\n"
    "v_lshlrev_b32 v202, 16, v178\n"
    "v_and_b32 v203, 0xffff0000, v178\n"
    "v_lshlrev_b32 v204, 16, v179\n"
    "v_and_b32 v205, 0xffff0000, v179\n"
    "v_lshlrev_b32 v206, 16, v180\n"
    "v_and_b32 v207, 0xffff0000, v180\n"
    "buffer_load_dwordx2 v[238:239], v4, s[8:11], s23 offen sc1\n"
    "s_waitcnt lgkmcnt(11)\n"
    "ds_write_b128 v168, a[164:167] offset:4608\n"
    "v_mfma_f32_16x16x32_bf16 a[200:203], v[160:163], a[164:167], a[200:203]\n"
    "buffer_load_dwordx4 a[112:115], v7, s[8:11], s76 offen offset:1024 sc1\n"
    "v_accvgpr_read_b32 v16, a192\n"
    "v_and_b32 v18, 0xffff0000, v16\n"
    "v_sub_f32 v17, v16, v18\n"
    "v_and_b32 v19, 0xffff0000, v17\n"
    "v_sub_f32 v17, v17, v19\n"
    "v_lshrrev_b32 v16, 16, v16\n"
    "v_or_b32 v16, v16, v19\n"
    "v_lshrrev_b32 v17, 16, v17\n"
    "ds_write_b64 v164, v[16:17] offset:0\n"
    "v_accvgpr_read_b32 v20, a196\n"
    "v_and_b32 v22, 0xffff0000, v20\n"
    "v_sub_f32 v21, v20, v22\n"
    "v_and_b32 v23, 0xffff0000, v21\n"
    "v_sub_f32 v21, v21, v23\n"
    "v_lshrrev_b32 v20, 16, v20\n"
    "v_or_b32 v20, v20, v23\n"
    "v_lshrrev_b32 v21, 16, v21\n"
    "ds_write_b64 v164, v[20:21] offset:2048\n"
    "v_accvgpr_read_b32 v24, a200\n"
    "v_and_b32 v26, 0xffff0000, v24\n"
    "v_sub_f32 v25, v24, v26\n"
    "v_and_b32 v27, 0xffff0000, v25\n"
    "v_sub_f32 v25, v25, v27\n"
    "v_lshrrev_b32 v24, 16, v24\n"
    "v_or_b32 v24, v24, v27\n"
    "v_lshrrev_b32 v25, 16, v25\n"
    "ds_write_b64 v164, v[24:25] offset:4096\n"
    "buffer_load_dwordx4 a[116:119], v15, s[8:11], s76 offen offset:0 sc1\n"
    "s_add_u32 s76, s76, 5120\n"
    "buffer_load_dwordx4 a[120:123], v6, s[8:11], s76 offen offset:0 sc1\n"
    "buffer_load_dwordx4 a[124:127], v6, s[8:11], s76 offen offset:1024 sc1\n"
    "buffer_load_dwordx4 a[128:131], v7, s[8:11], s76 offen offset:0 sc1\n"
    "buffer_load_dwordx4 a[132:135], v7, s[8:11], s76 offen offset:1024 sc1\n"
    "buffer_load_dwordx4 a[136:139], v15, s[8:11], s76 offen offset:0 sc1\n"
    "s_add_u32 s76, s76, 5120\n"
    "buffer_load_dwordx4 a[140:143], v6, s[8:11], s76 offen offset:0 sc1\n"
    "s_branch .Lf_ffn2\n"
    ".Lf_heavy11:\n"
    "v_add_u32 v11, 0, v1\n"
    "v_mul_u32_u24 v11, 171, v11\n"
    "v_lshrrev_b32 v11, 10, v11\n"
    "v_mul_u32_u24 v12, 2976, v11\n"
    "v_add_u32 v169, v12, v2\n"
    "v_mul_u32_u24 v12, 3, v11\n"
    "v_lshrrev_b32 v13, 1, v1\n"
    "v_sub_u32 v12, v13, v12\n"
    "v_add_u32 v12, 0, v12\n"
    "v_mad_u32_u24 v169, v12, s99, v169\n"
    "v_and_b32 v11, 4, v11\n"
    "v_mul_u32_u24 v11, 6144, v11\n"
    "v_sub_u32 v11, 12288, v11\n"
    "v_add_u32 v172, v169, v11\n"
    "v_add_u32 v11, 64, v1\n"
    "v_mul_u32_u24 v11, 171, v11\n"
    "v_lshrrev_b32 v11, 10, v11\n"
    "v_mul_u32_u24 v12, 2976, v11\n"
    "v_add_u32 v170, v12, v2\n"
    "v_mul_u32_u24 v12, 3, v11\n"
    "v_lshrrev_b32 v13, 1, v1\n"
    "v_sub_u32 v12, v13, v12\n"
    "v_add_u32 v12, 32, v12\n"
    "v_mad_u32_u24 v170, v12, s99, v170\n"
    "v_and_b32 v11, 4, v11\n"
    "v_mul_u32_u24 v11, 6144, v11\n"
    "v_sub_u32 v11, 12288, v11\n"
    "v_add_u32 v173, v170, v11\n"
    "v_add_u32 v11, 128, v1\n"
    "v_mul_u32_u24 v11, 171, v11\n"
    "v_lshrrev_b32 v11, 10, v11\n"
    "v_mul_u32_u24 v12, 2976, v11\n"
    "v_add_u32 v171, v12, v2\n"
    "v_mul_u32_u24 v12, 3, v11\n"
    "v_lshrrev_b32 v13, 1, v1\n"
    "v_sub_u32 v12, v13, v12\n"
    "v_add_u32 v12, 64, v12\n"
    "v_mad_u32_u24 v171, v12, s99, v171\n"
    "v_and_b32 v11, 4, v11\n"
    "v_mul_u32_u24 v11, 6144, v11\n"
    "v_sub_u32 v11, 12288, v11\n"
    "v_add_u32 v174, v171, v11\n"
    "v_lshlrev_b32 v175, 7, v1\n"
    "v_cmp_gt_u32 vcc, 12, v1\n"
    "v_mov_b32 v0, 0x70000000\n"
    "s_nop 0\n"
    "v_cndmask_b32 v175, v0, v175, vcc\n"
    "v_add_u32 v3, 0x10000, v2\n"
    "v_lshlrev_b32 v4, 8, v1\n"
    "s_and_b32 s57, s4, 1\n"
    "s_lshl_b32 s57, s57, 11\n"
    "v_add_u32 v6, s57, v2\n"
    "v_xor_b32 v7, 0x800, v6\n"
    "s_lshr_b32 s57, s57, 6\n"
    "s_or_b32 s57, s57, 0x1000\n"
    "v_xor_b32 v15, s57, v2\n"
    "v_cmp_gt_u32 vcc, 8, v1\n"
    "v_mov_b32 v0, 0x70000000\n"
    "s_nop 0\n"
    "v_cndmask_b32 v4, v0, v4, vcc\n"
    "v_mov_b32 v60, 0\n"
    "v_mov_b32 v61, 0\n"
    "v_lshlrev_b32 v62, 3, v1\n"
    "v_and_b32 v62, 8, v62\n"
    "v_add_u32 v62, 122880, v62\n"
    "ds_write_b64 v62, v[60:61]\n"
    "v_and_b32 v159, 0x70, v2\n"
    "v_and_b32 v155, 8, v1\n"
    "v_lshl_add_u32 v158, v155, 5, v159\n"
    "v_add_u32 v155, v155, v159\n"
    "v_lshrrev_b32 v159, 4, v1\n"
    "v_lshl_add_u32 v152, v159, 9, v158\n"
    "v_lshl_add_u32 v153, v159, 16, v158\n"
    "v_mul_u32_u24 v157, 80, v158\n"
    "v_lshl_add_u32 v156, v159, 4, v157\n"
    "v_lshl_add_u32 v157, v159, 2, v157\n"
    "v_and_b32 v154, 1, v159\n"
    "v_lshl_add_u32 v154, v154, 3, v158\n"
    "v_lshrrev_b32 v159, 1, v159\n"
    "v_lshl_add_u32 v154, v159, 15, v154\n"
    "s_lshl_b32 s58, s97, 2\n"
    "s_add_u32 s58, s58, s54\n"
    "s_lshr_b32 s59, s4, 1\n"
    "s_and_b32 s60, s4, 1\n"
    "s_lshl_b32 s28, s58, 2\n"
    "s_add_u32 s28, s28, s4\n"
    "s_mul_i32 s29, s28, 24576\n"
    "s_lshl_b32 s28, s28, 11\n"
    "s_add_u32 s28, s28, 315392\n"
    "s_lshl_b32 s61, s59, 10\n"
    "s_add_u32 s29, s29, s61\n"
    "s_add_u32 s29, s29, 20140032\n"
    "s_lshl_b32 s61, s59, 7\n"
    "s_mul_i32 s30, s58, 40960\n"
    "s_mul_i32 s62, s55, 5120\n"
    "s_add_u32 s30, s30, s62\n"
    "s_add_u32 s30, s30, s61\n"
    "s_add_u32 s30, s30, 13815808\n"
    "s_lshl_b32 s62, s60, 9\n"
    "s_add_u32 s31, s30, s62\n"
    "s_add_u32 s31, s31, 4096\n"
    "s_lshl_b32 s63, s60, 11\n"
    "s_add_u32 s30, s30, s63\n"
    "s_mul_i32 s32, s55, 655360\n"
    "s_lshl_b32 s63, s58, 9\n"
    "s_and_b32 s63, s63, -1\n"
    "s_add_u32 s32, s32, s63\n"
    "s_add_u32 s32, s32, s61\n"
    "s_add_u32 s32, s32, 6606848\n"
    "s_lshl_b32 s63, s60, 16\n"
    "s_add_u32 s33, s32, s63\n"
    "s_add_u32 s33, s33, 524288\n"
    "s_lshl_b32 s63, s60, 18\n"
    "s_add_u32 s32, s32, s63\n"
    "s_lshl_b32 s35, s58, 13\n"
    "s_add_u32 s35, s35, s61\n"
    "s_add_u32 s35, s35, s61\n"
    "s_add_u32 s35, s35, 19582976\n"
    "s_lshl_b32 s63, s55, 10\n"
    "s_add_u32 s34, s35, s63\n"
    "s_add_u32 s34, s34, s62\n"
    "s_lshl_b32 s36, s58, 12\n"
    "s_lshl_b32 s63, s55, 9\n"
    "s_add_u32 s36, s36, s63\n"
    "s_add_u32 s36, s36, s61\n"
    "s_lshl_b32 s63, s60, 8\n"
    "s_add_u32 s36, s36, s63\n"
    "s_add_u32 s36, s36, 19320832\n"
    "s_lshl_b32 s37, s58, 3\n"
    "s_add_u32 s37, s37, s55\n"
    "s_lshl_b32 s37, s37, 8\n"
    "s_add_u32 s37, s37, 184320\n"
    "s_lshl_b32 s38, s58, 2\n"
    "s_add_u32 s38, s38, s59\n"
    "s_mul_i32 s38, s38, 10240\n"
    "s_mul_i32 s63, s55, 160\n"
    "s_add_u32 s38, s38, s63\n"
    "s_lshl_b32 s63, s60, 4\n"
    "s_add_u32 s39, s38, s63\n"
    "s_add_u32 s39, s39, 128\n"
    "s_lshl_b32 s63, s60, 6\n"
    "s_add_u32 s38, s38, s63\n"
    "s_lshl_b32 s96, s58, 9\n"
    "s_add_u32 s96, s96, s61\n"
    "s_add_u32 s96, s96, 20107264\n"
    "s_lshr_b32 s57, s4, 1\n"
    "s_lshl_b32 s57, s57, 8\n"
    "s_and_b32 s58, s4, 1\n"
    "s_lshl_b32 s58, s58, 3\n"
    "s_add_u32 s57, s57, s58\n"
    "s_add_u32 s57, s57, 123008\n"
    "s_movk_i32 s59, 384\n"
    "v_bfe_u32 v160, v1, 4, 1\n"
    "v_and_b32 v168, 0xf0, v2\n"
    "v_lshl_add_u32 v168, v160, 10, v168\n"
    "v_lshrrev_b32 v162, 5, v1\n"
    "v_lshl_add_u32 v164, v162, 9, v168\n"
    "v_add_u32 v164, s57, v164\n"
    "v_lshl_add_u32 v168, v162, 8, v168\n"
    "v_add_u32 v168, s92, v168\n"
    "v_bfe_u32 v161, v1, 2, 2\n"
    "v_lshl_add_u32 v162, v162, 3, v161\n"
    "v_mul_u32_u24 v162, 96, v162\n"
    "v_and_b32 v163, 3, v1\n"
    "v_lshlrev_b32 v163, 3, v163\n"
    "v_add3_u32 v162, v162, v163, s92\n"
    "v_mul_u32_u24 v166, 3456, v160\n"
    "v_mul_u32_u24 v167, 2688, v160\n"
    "v_add_u32 v166, v166, v162\n"
    "v_add3_u32 v167, v167, v162, s59\n"
    "v_xor_b32 v160, v160, v161\n"
    "v_and_b32 v160, 1, v160\n"
    "v_add_u32 v160, -1, v160\n"
    "v_and_b32 v160, 0x3f803f80, v160\n"
    "v_mov_b32 v161, v160\n"
    "v_mov_b32 v162, v160\n"
    "v_mov_b32 v163, v160\n"
    "s_waitcnt vmcnt(0)\n"
    "v_readfirstlane_b32 s50, v253\n"
    "s_nop 0\n"
    "s_mul_i32 s50, s50, 0x19660d\n"
    "s_add_u32 s50, s50, 0x3c6ef35f\n"
    "s_not_b32 s51, s50\n"
    "v_mov_b32 v252, s50\n"
    "v_mov_b32 v253, s51\n"
    "s_waitcnt lgkmcnt(0)\n"
    "s_barrier\n"
    "s_sleep 40\n"
    "s_add_u32 m0, s92, 0\n"
    "s_nop 0\n"
    "buffer_load_dwordx4 v169, s[12:15], s77 offen offset:0 lds\n"
    "buffer_load_dwordx4 v170, s[12:15], s77 offen offset:1024 lds\n"
    "buffer_load_dwordx4 v171, s[12:15], s77 offen offset:2048 lds\n"
    "s_add_u32 m0, s92, 3072\n"
    "s_nop 0\n"
    "buffer_load_dwordx4 v172, s[16:19], s77 offen offset:0 lds\n"
    "buffer_load_dwordx4 v173, s[16:19], s77 offen offset:1024 lds\n"
    "buffer_load_dwordx4 v174, s[16:19], s77 offen offset:2048 lds\n"
    "s_add_u32 s92, s92, 6144\n"
    "s_add_u32 s77, s77, 98304\n"
    "s_cmp_eq_u32 s77, s94\n"
    "s_cselect_b32 s92, s95, s92\n"
    "s_mov_b32 m0, s92\n"
    "s_cselect_b32 s77, s93, s77\n"
    "buffer_load_dwordx4 v169, s[12:15], s77 offen offset:0 lds\n"
    "buffer_load_dwordx4 v170, s[12:15], s77 offen offset:1024 lds\n"
    "buffer_load_dwordx4 v171, s[12:15], s77 offen offset:2048 lds\n"
    "s_add_u32 m0, s92, 3072\n"
    "s_nop 0\n"
    "buffer_load_dwordx4 v172, s[16:19], s77 offen offset:0 lds\n"
    "buffer_load_dwordx4 v173, s[16:19], s77 offen offset:1024 lds\n"
    "buffer_load_dwordx4 v174, s[16:19], s77 offen offset:2048 lds\n"
    "s_add_u32 s92, s92, 6144\n"
    "s_add_u32 s77, s77, 98304\n"
    "s_cmp_eq_u32 s77, s94\n"
    "s_cselect_b32 s92, s95, s92\n"
    "s_mov_b32 m0, s92\n"
    "s_cselect_b32 s77, s93, s77\n"
    "buffer_load_dwordx4 v169, s[12:15], s77 offen offset:0 lds\n"
    "buffer_load_dwordx4 v170, s[12:15], s77 offen offset:1024 lds\n"
    "buffer_load_dwordx4 v171, s[12:15], s77 offen offset:2048 lds\n"
    "s_add_u32 m0, s92, 3072\n"
    "s_nop 0\n"
    "buffer_load_dwordx4 v172, s[16:19], s77 offen offset:0 lds\n"
    "buffer_load_dwordx4 v173, s[16:19], s77 offen offset:1024 lds\n"
    "buffer_load_dwordx4 v174, s[16:19], s77 offen offset:2048 lds\n"
    "buffer_load_dwordx4 v[208:211], v156, s[20:23], s38 offen\n"
    "buffer_load_dword v231, v157, s[20:23], s39 offen\n"
    "s_add_u32 s88, s38, 1310720\n"
    "buffer_load_dwordx4 v[176:179], v156, s[20:23], s88 offen\n"
    "s_add_u32 s89, s39, 1310720\n"
    "buffer_load_dword v180, v157, s[20:23], s89 offen\n"
    "buffer_load_dwordx2 v[244:245], v175, s[8:11], s78 offen sc1\n"
    "s_add_u32 s92, s92, 6144\n"
    "s_add_u32 s77, s77, 98304\n"
    "s_cmp_eq_u32 s77, s94\n"
    "s_cselect_b32 s92, s95, s92\n"
    "s_mov_b32 m0, s92\n"
    "s_cselect_b32 s77, s93, s77\n"
    "buffer_load_dwordx4 v169, s[12:15], s77 offen offset:0 lds\n"
    "buffer_load_dwordx4 v170, s[12:15], s77 offen offset:1024 lds\n"
    "buffer_load_dwordx4 v171, s[12:15], s77 offen offset:2048 lds\n"
    "s_add_u32 m0, s92, 3072\n"
    "s_nop 0\n"
    "buffer_load_dwordx4 v172, s[16:19], s77 offen offset:0 lds\n"
    "buffer_load_dwordx4 v173, s[16:19], s77 offen offset:1024 lds\n"
    "buffer_load_dwordx4 v174, s[16:19], s77 offen offset:2048 lds\n"
    "s_waitcnt vmcnt(23)\n"
    "ds_read_b64_tr_b16 a[144:145], v166 offset:0\n"
    "ds_read_b64_tr_b16 a[146:147], v167 offset:0\n"
    "ds_read_b64_tr_b16 a[148:149], v166 offset:32\n"
    "ds_read_b64_tr_b16 a[150:151], v167 offset:32\n"
    "ds_read_b64_tr_b16 a[152:153], v166 offset:64\n"
    "ds_read_b64_tr_b16 a[154:155], v167 offset:64\n"
    "ds_read_b64_tr_b16 a[156:157], v166 offset:1536\n"
    "ds_read_b64_tr_b16 a[158:159], v167 offset:1536\n"
    "ds_read_b64_tr_b16 a[160:161], v166 offset:1568\n"
    "ds_read_b64_tr_b16 a[162:163], v167 offset:1568\n"
    "ds_read_b64_tr_b16 a[164:165], v166 offset:1600\n"
    "ds_read_b64_tr_b16 a[166:167], v167 offset:1600\n"
    "s_waitcnt lgkmcnt(10)\n"
    "ds_write_b128 v168, a[144:147] offset:0\n"
    "v_mfma_f32_16x16x32_bf16 a[192:195], v[160:163], a[144:147], 0\n"
    "s_waitcnt lgkmcnt(9)\n"
    "ds_write_b128 v168, a[148:151] offset:2048\n"
    "v_mfma_f32_16x16x32_bf16 a[196:199], v[160:163], a[148:151], 0\n"
    "s_waitcnt lgkmcnt(8)\n"
    "ds_write_b128 v168, a[152:155] offset:4096\n"
    "v_mfma_f32_16x16x32_bf16 a[200:203], v[160:163], a[152:155], 0\n"
    "s_waitcnt lgkmcnt(7)\n"
    "ds_write_b128 v168, a[156:159] offset:512\n"
    "v_mfma_f32_16x16x32_bf16 a[192:195], v[160:163], a[156:159], a[192:195]\n"
    "s_waitcnt lgkmcnt(6)\n"
    "ds_write_b128 v168, a[160:163] offset:2560\n"
    "v_mfma_f32_16x16x32_bf16 a[196:199], v[160:163], a[160:163], a[196:199]\n"
    "s_waitcnt lgkmcnt(5)\n"
    "ds_write_b128 v168, a[164:167] offset:4608\n"
    "v_mfma_f32_16x16x32_bf16 a[200:203], v[160:163], a[164:167], a[200:203]\n"
    "s_add_u32 s92, s92, 6144\n"
    "s_add_u32 s77, s77, 98304\n"
    "s_cmp_eq_u32 s77, s94\n"
    "s_cselect_b32 s92, s95, s92\n"
    "s_mov_b32 m0, s92\n"
    "s_cselect_b32 s77, s93, s77\n"
    "buffer_load_dwordx4 v169, s[12:15], s77 offen offset:0 lds\n"
    "buffer_load_dwordx4 v170, s[12:15], s77 offen offset:1024 lds\n"
    "buffer_load_dwordx4 v171, s[12:15], s77 offen offset:2048 lds\n"
    "s_add_u32 m0, s92, 3072\n"
    "s_nop 0\n"
    "buffer_load_dwordx4 v172, s[16:19], s77 offen offset:0 lds\n"
    "buffer_load_dwordx4 v173, s[16:19], s77 offen offset:1024 lds\n"
    "buffer_load_dwordx4 v174, s[16:19], s77 offen offset:2048 lds\n"
    "s_cmp_eq_u32 s98, 4\n"
    "s_cselect_b32 s67, -4, 1\n"
    "s_mulk_i32 s67, 6144\n"
    "v_add_u32 v166, s67, v166\n"
    "v_add_u32 v167, s67, v167\n"
    "s_waitcnt vmcnt(23)\n"
    "ds_read_b64_tr_b16 a[168:169], v166 offset:0\n"
    "ds_read_b64_tr_b16 a[170:171], v167 offset:0\n"
    "ds_read_b64_tr_b16 a[172:173], v166 offset:32\n"
    "ds_read_b64_tr_b16 a[174:175], v167 offset:32\n"
    "ds_read_b64_tr_b16 a[176:177], v166 offset:64\n"
    "ds_read_b64_tr_b16 a[178:179], v167 offset:64\n"
    "ds_read_b64_tr_b16 a[180:181], v166 offset:1536\n"
    "ds_read_b64_tr_b16 a[182:183], v167 offset:1536\n"
    "ds_read_b64_tr_b16 a[184:185], v166 offset:1568\n"
    "ds_read_b64_tr_b16 a[186:187], v167 offset:1568\n"
    "ds_read_b64_tr_b16 a[188:189], v166 offset:1600\n"
    "ds_read_b64_tr_b16 a[190:191], v167 offset:1600\n"
    "v_add_u32 v168, s67, v168\n"
    "s_waitcnt lgkmcnt(10)\n"
    "ds_write_b128 v168, a[168:171] offset:0\n"
    "v_mfma_f32_16x16x32_bf16 a[192:195], v[160:163], a[168:171], a[192:195]\n"
    "s_waitcnt lgkmcnt(9)\n"
    "ds_write_b128 v168, a[172:175] offset:2048\n"
    "v_mfma_f32_16x16x32_bf16 a[196:199], v[160:163], a[172:175], a[196:199]\n"
    "s_waitcnt lgkmcnt(8)\n"
    "ds_write_b128 v168, a[176:179] offset:4096\n"
    "v_mfma_f32_16x16x32_bf16 a[200:203], v[160:163], a[176:179], a[200:203]\n"
    "s_waitcnt lgkmcnt(7)\n"
    "ds_write_b128 v168, a[180:183] offset:512\n"
    "v_mfma_f32_16x16x32_bf16 a[192:195], v[160:163], a[180:183], a[192:195]\n"
    "s_waitcnt lgkmcnt(6)\n"
    "ds_write_b128 v168, a[184:187] offset:2560\n"
    "v_mfma_f32_16x16x32_bf16 a[196:199], v[160:163], a[184:187], a[196:199]\n"
    "s_waitcnt lgkmcnt(5)\n"
    "ds_write_b128 v168, a[188:191] offset:4608\n"
    "v_mfma_f32_16x16x32_bf16 a[200:203], v[160:163], a[188:191], a[200:203]\n"
    "s_add_u32 s72, s96, 0\n"
    "s_add_u32 s68, s32, 0\n"
    "s_add_u32 s69, s32, 32768\n"
    "s_add_u32 s70, s33, 0\n"
    "buffer_load_dwordx4 v[188:191], v158, s[8:11], s72 offen sc1\n"
    "buffer_load_dwordx4 v[212:215], v153, s[8:11], s68 offen sc1\n"
    "buffer_load_dwordx4 v[216:219], v153, s[8:11], s69 offen sc1\n"
    "buffer_load_dwordx2 v[220:221], v154, s[8:11], s70 offen sc1\n"
    "s_cmp_eq_u32 s98, 3\n"
    "s_cselect_b32 s66, -4, 1\n"
    "s_mulk_i32 s66, 6144\n"
    "v_add_u32 v166, s66, v166\n"
    "v_add_u32 v167, s66, v167\n"
    "s_waitcnt vmcnt(21)\n"
    "ds_read_b64_tr_b16 a[144:145], v166 offset:0\n"
    "ds_read_b64_tr_b16 a[146:147], v167 offset:0\n"
    "ds_read_b64_tr_b16 a[148:149], v166 offset:32\n"
    "ds_read_b64_tr_b16 a[150:151], v167 offset:32\n"
    "ds_read_b64_tr_b16 a[152:153], v166 offset:64\n"
    "ds_read_b64_tr_b16 a[154:155], v167 offset:64\n"
    "ds_read_b64_tr_b16 a[156:157], v166 offset:1536\n"
    "ds_read_b64_tr_b16 a[158:159], v167 offset:1536\n"
    "ds_read_b64_tr_b16 a[160:161], v166 offset:1568\n"
    "ds_read_b64_tr_b16 a[162:163], v167 offset:1568\n"
    "ds_read_b64_tr_b16 a[164:165], v166 offset:1600\n"
    "ds_read_b64_tr_b16 a[166:167], v167 offset:1600\n"
    "v_add_u32 v168, s66, v168\n"
    "s_waitcnt lgkmcnt(10)\n"
    "ds_write_b128 v168, a[144:147] offset:0\n"
    "v_mfma_f32_16x16x32_bf16 a[192:195], v[160:163], a[144:147], a[192:195]\n"
    "s_waitcnt lgkmcnt(9)\n"
    "ds_write_b128 v168, a[148:151] offset:2048\n"
    "v_mfma_f32_16x16x32_bf16 a[196:199], v[160:163], a[148:151], a[196:199]\n"
    "s_waitcnt lgkmcnt(8)\n"
    "ds_write_b128 v168, a[152:155] offset:4096\n"
    "v_mfma_f32_16x16x32_bf16 a[200:203], v[160:163], a[152:155], a[200:203]\n"
    "s_waitcnt lgkmcnt(7)\n"
    "ds_write_b128 v168, a[156:159] offset:512\n"
    "v_mfma_f32_16x16x32_bf16 a[192:195], v[160:163], a[156:159], a[192:195]\n"
    "s_waitcnt lgkmcnt(6)\n"
    "ds_write_b128 v168, a[160:163] offset:2560\n"
    "v_mfma_f32_16x16x32_bf16 a[196:199], v[160:163], a[160:163], a[196:199]\n"
    "s_waitcnt lgkmcnt(5)\n"
    "ds_write_b128 v168, a[164:167] offset:4608\n"
    "v_mfma_f32_16x16x32_bf16 a[200:203], v[160:163], a[164:167], a[200:203]\n"
    "s_waitcnt vmcnt(16)\n"
    "v_cmp_eq_u64 vcc, s[50:51], v[244:245]\n"
    "s_nop 0\n"
    "s_andn2_b32 s79, 0xfff, vcc_lo\n"
    "s_cbranch_scc1 .Lf_slow16\n"
    ".Lf_back17:\n"
    "buffer_load_dwordx4 a[0:3], v6, s[8:11], s76 offen offset:0 sc1\n"
    "buffer_load_dwordx4 a[4:7], v6, s[8:11], s76 offen offset:1024 sc1\n"
    "buffer_load_dwordx4 a[8:11], v7, s[8:11], s76 offen offset:0 sc1\n"
    "buffer_load_dwordx4 a[12:15], v7, s[8:11], s76 offen offset:1024 sc1\n"
    "buffer_load_dwordx4 a[16:19], v15, s[8:11], s76 offen offset:0 sc1\n"
    "s_add_u32 s76, s76, 5120\n"
    "buffer_load_dwordx4 a[20:23], v6, s[8:11], s76 offen offset:0 sc1\n"
    "v_lshlrev_b32 v222, 16, v208\n"
    "v_and_b32 v223, 0xffff0000, v208\n"
    "v_lshlrev_b32 v224, 16, v209\n"
    "v_and_b32 v225, 0xffff0000, v209\n"
    "v_lshlrev_b32 v226, 16, v210\n"
    "v_and_b32 v227, 0xffff0000, v210\n"
    "v_lshlrev_b32 v228, 16, v211\n"
    "v_and_b32 v229, 0xffff0000, v211\n"
    "v_lshlrev_b32 v230, 16, v231\n"
    "v_and_b32 v231, 0xffff0000, v231\n"
    "s_waitcnt vmcnt(9)\n"
    "v_xor_b32 v246, s50, v189\n"
    "v_bitop3_b32 v246, v246, v191, s50 bitop3:0xf6\n"
    "s_waitcnt vmcnt(8)\n"
    "v_bitop3_b32 v246, v246, v213, s50 bitop3:0xf6\n"
    "v_bitop3_b32 v246, v246, v215, s50 bitop3:0xf6\n"
    "s_waitcnt vmcnt(7)\n"
    "v_bitop3_b32 v246, v246, v217, s50 bitop3:0xf6\n"
    "v_bitop3_b32 v246, v246, v219, s50 bitop3:0xf6\n"
    "s_waitcnt vmcnt(6)\n"
    "v_bitop3_b32 v246, v246, v221, s50 bitop3:0xf6\n"
    "v_cmp_ne_u32 vcc, 0, v246\n"
    "s_nop 0\n"
    "s_cmp_lg_u64 vcc, 0\n"
    "s_cbranch_scc1 .Lf_gslow18\n"
    ".Lf_gback19:\n"
    "v_and_b32 v213, 0xffff0000, v212\n"
    "v_lshlrev_b32 v212, 16, v212\n"
    "v_and_b32 v215, 0xffff0000, v214\n"
    "v_lshlrev_b32 v214, 16, v214\n"
    "v_and_b32 v217, 0xffff0000, v216\n"
    "v_lshlrev_b32 v216, 16, v216\n"
    "v_and_b32 v219, 0xffff0000, v218\n"
    "v_lshlrev_b32 v218, 16, v218\n"
    "v_and_b32 v221, 0xffff0000, v220\n"
    "v_lshlrev_b32 v220, 16, v220\n"
    "v_pk_fma_f32 v[192:193], v[222:223], v[190:191], v[188:189] op_sel_hi:[1,0,0] neg_lo:[0,0,1] neg_hi:[0,0,1]\n"
    "v_pk_fma_f32 v[194:195], v[224:225], v[190:191], v[188:189] op_sel_hi:[1,0,0] neg_lo:[0,0,1] neg_hi:[0,0,1]\n"
    "v_pk_fma_f32 v[196:197], v[226:227], v[190:191], v[188:189] op_sel_hi:[1,0,0] neg_lo:[0,0,1] neg_hi:[0,0,1]\n"
    "v_pk_fma_f32 v[198:199], v[228:229], v[190:191], v[188:189] op_sel_hi:[1,0,0] neg_lo:[0,0,1] neg_hi:[0,0,1]\n"
    "v_pk_fma_f32 v[200:201], v[230:231], v[190:191], v[188:189] op_sel_hi:[1,0,0] neg_lo:[0,0,1] neg_hi:[0,0,1]\n"
    "v_pk_fma_f32 v[222:223], v[192:193], v[212:213], v[222:223]\n"
    "v_pk_fma_f32 v[224:225], v[194:195], v[214:215], v[224:225]\n"
    "v_pk_fma_f32 v[226:227], v[196:197], v[216:217], v[226:227]\n"
    "v_pk_fma_f32 v[228:229], v[198:199], v[218:219], v[228:229]\n"
    "v_pk_fma_f32 v[230:231], v[200:201], v[220:221], v[230:231]\n"
    "v_cvt_pk_bf16_f32 v196, v230, v231\n"
    "v_cvt_pk_bf16_f32 v192, v222, v223\n"
    "v_mov_b32 v197, v196\n"
    "v_cvt_pk_bf16_f32 v193, v224, v225\n"
    "v_cvt_pk_bf16_f32 v194, v226, v227\n"
    "v_cvt_pk_bf16_f32 v195, v228, v229\n"
    "v_permlane16_swap_b32 v196, v197\n"
    "v_mov_b32 v198, v196\n"
    "v_mov_b32 v199, v197\n"
    "v_pk_add_f32 v[188:189], v[222:223], v[224:225]\n"
    "v_pk_mul_f32 v[190:191], v[222:223], v[222:223]\n"
    "buffer_store_dwordx4 v[192:195], v152, s[8:11], s30 offen sc1\n"
    "v_permlane32_swap_b32 v196, v198\n"
    "v_permlane32_swap_b32 v197, v199\n"
    "v_pk_fma_f32 v[190:191], v[224:225], v[224:225], v[190:191]\n"
    "v_pk_add_f32 v[188:189], v[188:189], v[226:227]\n"
    "v_pk_fma_f32 v[190:191], v[226:227], v[226:227], v[190:191]\n"
    "v_pk_add_f32 v[188:189], v[188:189], v[228:229]\n"
    "v_pk_fma_f32 v[190:191], v[228:229], v[228:229], v[190:191]\n"
    "v_pk_add_f32 v[188:189], v[188:189], v[230:231]\n"
    "v_pk_fma_f32 v[190:191], v[230:231], v[230:231], v[190:191]\n"
    "v_add_f32 v188, v188, v189\n"
    "v_add_f32 v190, v190, v191\n"
    "v_mov_b32 v189, v190\n"
    "s_nop 1\n"
    "v_permlane16_swap_b32 v188, v189\n"
    "v_add_f32 v188, v188, v189\n"
    "v_mov_b32 v189, v188\n"
    "s_nop 1\n"
    "v_permlane32_swap_b32 v188, v189\n"
    "v_add_f32 v188, v188, v189\n"
    "v_mov_b32 v189, v188\n"
    "s_nop 1\n"
    "v_permlane16_swap_b32 v188, v189\n"
    "s_add_u32 s90, s36, -262144\n"
    "s_mov_b64 exec, 0xffff\n"
    "buffer_store_dwordx4 v[196:199], v152, s[8:11], s31 offen sc1\n"
    "buffer_store_dwordx2 v[188:189], v155, s[8:11], s90 offen sc1\n"
    "s_mov_b64 exec, -1\n"
    "buffer_load_dwordx4 a[24:27], v6, s[8:11], s76 offen offset:1024 sc1\n"
    "buffer_load_dwordx4 a[28:31], v7, s[8:11], s76 offen offset:0 sc1\n"
    "buffer_load_dwordx4 a[32:35], v7, s[8:11], s76 offen offset:1024 sc1\n"
    "buffer_load_dwordx4 a[36:39], v15, s[8:11], s76 offen offset:0 sc1\n"
    "s_add_u32 s76, s76, 5120\n"
    "buffer_load_dwordx4 a[40:43], v6, s[8:11], s76 offen offset:0 sc1\n"
    "buffer_load_dwordx4 a[44:47], v6, s[8:11], s76 offen offset:1024 sc1\n"
    "s_waitcnt vmcnt(6)\n"
    "s_mov_b64 exec, 1\n"
    "ds_add_rtn_u32 v165, v3, v3 offset:57344\n"
    "s_mov_b64 exec, -1\n"
    "s_waitcnt lgkmcnt(0)\n"
    "v_readfirstlane_b32 s79, v165\n"
    "s_add_u32 s91, s37, -131072\n"
    "s_cmp_eq_u32 s79, 0x30000\n"
    "s_cselect_b64 exec, 0xff, 0\n"
    "buffer_store_dwordx4 v[252:255], v2, s[8:11], s91 offen sc1\n"
    "s_mov_b64 exec, -1\n"
    "s_cmp_eq_u32 s98, 2\n"
    "s_cselect_b32 s67, -4, 1\n"
    "s_mulk_i32 s67, 6144\n"
    "v_add_u32 v166, s67, v166\n"
    "v_add_u32 v167, s67, v167\n"
    "ds_read_b64_tr_b16 a[168:169], v166 offset:0\n"
    "ds_read_b64_tr_b16 a[170:171], v167 offset:0\n"
    "ds_read_b64_tr_b16 a[172:173], v166 offset:32\n"
    "ds_read_b64_tr_b16 a[174:175], v167 offset:32\n"
    "ds_read_b64_tr_b16 a[176:177], v166 offset:64\n"
    "ds_read_b64_tr_b16 a[178:179], v167 offset:64\n"
    "ds_read_b64_tr_b16 a[180:181], v166 offset:1536\n"
    "ds_read_b64_tr_b16 a[182:183], v167 offset:1536\n"
    "ds_read_b64_tr_b16 a[184:185], v166 offset:1568\n"
    "ds_read_b64_tr_b16 a[186:187], v167 offset:1568\n"
    "ds_read_b64_tr_b16 a[188:189], v166 offset:1600\n"
    "ds_read_b64_tr_b16 a[190:191], v167 offset:1600\n"
    "s_cmp_eq_u32 s98, 1\n"
    "s_cselect_b32 s66, -4, 1\n"
    "s_mulk_i32 s66, 6144\n"
    "v_add_u32 v166, s66, v166\n"
    "v_add_u32 v167, s66, v167\n"
    "ds_read_b64_tr_b16 a[144:145], v166 offset:0\n"
    "ds_read_b64_tr_b16 a[146:147], v167 offset:0\n"
    "ds_read_b64_tr_b16 a[148:149], v166 offset:32\n"
    "ds_read_b64_tr_b16 a[150:151], v167 offset:32\n"
    "ds_read_b64_tr_b16 a[152:153], v166 offset:64\n"
    "ds_read_b64_tr_b16 a[154:155], v167 offset:64\n"
    "ds_read_b64_tr_b16 a[156:157], v166 offset:1536\n"
    "ds_read_b64_tr_b16 a[158:159], v167 offset:1536\n"
    "ds_read_b64_tr_b16 a[160:161], v166 offset:1568\n"
    "ds_read_b64_tr_b16 a[162:163], v167 offset:1568\n"
    "ds_read_b64_tr_b16 a[164:165], v166 offset:1600\n"
    "ds_read_b64_tr_b16 a[166:167], v167 offset:1600\n"
    "v_add_u32 v168, s67, v168\n"
    "s_waitcnt lgkmcnt(15)\n"
    "ds_write_b128 v168, a[168:171] offset:0\n"
    "v_mfma_f32_16x16x32_bf16 a[192:195], v[160:163], a[168:171], a[192:195]\n"
    "buffer_load_dwordx4 a[48:51], v7, s[8:11], s76 offen offset:0 sc1\n"
    "s_lshl_b32 s57, s52, 2\n"
    "s_add_u32 s57, s57, s4\n"
    "s_lshl_b32 s23, s57, 11\n"
    "buffer_load_dwordx4 a[52:55], v7, s[8:11], s76 offen offset:1024 sc1\n"
    "s_add_u32 s23, s23, 53248\n"
    "s_mov_b32 s12, 0\n"
    "s_mov_b32 s13, 4096\n"
    "ds_write_b128 v168, a[172:175] offset:2048\n"
    "v_mfma_f32_16x16x32_bf16 a[196:199], v[160:163], a[172:175], a[196:199]\n"
    "buffer_load_dwordx4 a[56:59], v15, s[8:11], s76 offen offset:0 sc1\n"
    "s_mov_b32 s14, 8192\n"
    "s_mov_b32 s15, 12288\n"
    "s_mov_b32 s16, 16384\n"
    "ds_write_b128 v168, a[176:179] offset:4096\n"
    "v_mfma_f32_16x16x32_bf16 a[200:203], v[160:163], a[176:179], a[200:203]\n"
    "s_add_u32 s76, s76, 5120\n"
    "buffer_load_dwordx4 a[60:63], v6, s[8:11], s76 offen offset:0 sc1\n"
    "s_mov_b32 s17, 20480\n"
    "s_mov_b32 s18, 24576\n"
    "s_mov_b32 s19, 28672\n"
    "buffer_load_dwordx4 a[64:67], v6, s[8:11], s76 offen offset:1024 sc1\n"
    "s_mov_b32 s20, 32768\n"
    "s_mov_b32 s40, 36864\n"
    "s_mul_i32 s21, s57, 40960\n"
    "ds_write_b128 v168, a[180:183] offset:512\n"
    "v_mfma_f32_16x16x32_bf16 a[192:195], v[160:163], a[180:183], a[192:195]\n"
    "buffer_load_dwordx4 a[68:71], v7, s[8:11], s76 offen offset:0 sc1\n"
    "s_add_u32 s21, s21, 13815808\n"
    "s_lshl_b32 s22, s57, 12\n"
    "s_add_u32 s22, s22, 19058688\n"
    "s_waitcnt lgkmcnt(15)\n"
    "ds_write_b128 v168, a[184:187] offset:2560\n"
    "v_mfma_f32_16x16x32_bf16 a[196:199], v[160:163], a[184:187], a[196:199]\n"
    "buffer_load_dwordx4 a[72:75], v7, s[8:11], s76 offen offset:1024 sc1\n"
    "s_lshl_b32 s27, s57, 5\n"
    "s_add_u32 s27, s27, s53\n"
    "s_lshl_b32 s27, s27, 8\n"
    "buffer_load_dwordx4 a[76:79], v15, s[8:11], s76 offen offset:0 sc1\n"
    "s_add_u32 s27, s27, 315392\n"
    "s_mul_i32 s61, s57, 98304\n"
    "s_add_u32 s61, s61, 20140032\n"
    "ds_write_b128 v168, a[188:191] offset:4608\n"
    "v_mfma_f32_16x16x32_bf16 a[200:203], v[160:163], a[188:191], a[200:203]\n"
    "s_add_u32 s76, s76, 5120\n"
    "buffer_load_dwordx4 a[80:83], v6, s[8:11], s76 offen offset:0 sc1\n"
    "s_lshr_b32 s62, s53, 3\n"
    "s_lshl_b32 s62, s62, 14\n"
    "s_add_u32 s61, s61, s62\n"
    "v_add_u32 v168, s66, v168\n"
    "s_waitcnt lgkmcnt(15)\n"
    "ds_write_b128 v168, a[144:147] offset:0\n"
    "v_mfma_f32_16x16x32_bf16 a[192:195], v[160:163], a[144:147], a[192:195]\n"
    "buffer_load_dwordx4 a[84:87], v6, s[8:11], s76 offen offset:1024 sc1\n"
    "s_lshr_b32 s62, s53, 1\n"
    "s_lshl_b32 s62, s62, 11\n"
    "s_add_u32 s61, s61, s62\n"
    "buffer_load_dwordx4 a[88:91], v7, s[8:11], s76 offen offset:0 sc1\n"
    "s_and_b32 s62, s53, 1\n"
    "s_lshl_b32 s62, s62, 9\n"
    "s_add_u32 s24, s61, s62\n"
    "s_waitcnt lgkmcnt(15)\n"
    "ds_write_b128 v168, a[148:151] offset:2048\n"
    "v_mfma_f32_16x16x32_bf16 a[196:199], v[160:163], a[148:151], a[196:199]\n"
    "buffer_load_dwordx4 a[92:95], v7, s[8:11], s76 offen offset:1024 sc1\n"
    "s_add_u32 s25, s24, 8192\n"
    "s_add_u32 s26, s24, 16384\n"
    "v_and_b32 v0, 0xf0, v2\n"
    "s_waitcnt lgkmcnt(14)\n"
    "ds_write_b128 v168, a[152:155] offset:4096\n"
    "v_mfma_f32_16x16x32_bf16 a[200:203], v[160:163], a[152:155], a[200:203]\n"
    "buffer_load_dwordx4 a[96:99], v15, s[8:11], s76 offen offset:0 sc1\n"
    "v_lshrrev_b32 v232, 4, v1\n"
    "v_lshrrev_b32 v233, 5, v1\n"
    "v_and_b32 v234, 1, v232\n"
    "s_add_u32 s76, s76, 5120\n"
    "buffer_load_dwordx4 a[100:103], v6, s[8:11], s76 offen offset:0 sc1\n"
    "v_lshl_add_u32 v8, v232, 11, v0\n"
    "v_and_b32 v0, 0x70, v2\n"
    "v_and_b32 v11, 8, v1\n"
    "s_waitcnt lgkmcnt(13)\n"
    "ds_write_b128 v168, a[156:159] offset:512\n"
    "v_mfma_f32_16x16x32_bf16 a[192:195], v[160:163], a[156:159], a[192:195]\n"
    "buffer_load_dwordx4 a[104:107], v6, s[8:11], s76 offen offset:1024 sc1\n"
    "v_lshl_add_u32 v10, v11, 7, v0\n"
    "v_lshl_add_u32 v10, v234, 7, v10\n"
    "v_lshl_add_u32 v10, v233, 8, v10\n"
    "s_waitcnt lgkmcnt(12)\n"
    "ds_write_b128 v168, a[160:163] offset:2560\n"
    "v_mfma_f32_16x16x32_bf16 a[196:199], v[160:163], a[160:163], a[196:199]\n"
    "buffer_load_dwordx4 a[108:111], v7, s[8:11], s76 offen offset:0 sc1\n"
    "v_lshl_add_u32 v13, v11, 5, v0\n"
    "v_add_u32 v11, v11, v0\n"
    "v_lshl_add_u32 v12, v232, 9, v13\n"
    "v_lshl_add_u32 v14, v232, 16, v13\n"
    "v_lshl_add_u32 v5, v233, 15, v13\n"
    "v_lshl_add_u32 v5, v234, 3, v5\n"
    "s_waitcnt lgkmcnt(11)\n"
    "ds_write_b128 v168, a[164:167] offset:4608\n"
    "v_mfma_f32_16x16x32_bf16 a[200:203], v[160:163], a[164:167], a[200:203]\n"
    "v_lshl_add_u32 v13, v232, 2, v13\n"
    "v_add_u32 v235, s21, v2\n"
    "v_and_b32 v62, 0x1ff, v12\n"
    "v_accvgpr_read_b32 v16, a192\n"
    "v_and_b32 v18, 0xffff0000, v16\n"
    "v_sub_f32 v17, v16, v18\n"
    "v_and_b32 v19, 0xffff0000, v17\n"
    "v_sub_f32 v17, v17, v19\n"
    "v_lshrrev_b32 v16, 16, v16\n"
    "v_or_b32 v16, v16, v19\n"
    "v_lshrrev_b32 v17, 16, v17\n"
    "ds_write_b64 v164, v[16:17] offset:0\n"
    "v_accvgpr_read_b32 v20, a196\n"
    "v_and_b32 v22, 0xffff0000, v20\n"
    "v_sub_f32 v21, v20, v22\n"
    "v_and_b32 v23, 0xffff0000, v21\n"
    "v_sub_f32 v21, v21, v23\n"
    "v_lshrrev_b32 v20, 16, v20\n"
    "v_or_b32 v20, v20, v23\n"
    "v_lshrrev_b32 v21, 16, v21\n"
    "ds_write_b64 v164, v[20:21] offset:2048\n"
    "v_accvgpr_read_b32 v24, a200\n"
    "v_and_b32 v26, 0xffff0000, v24\n"
    "v_sub_f32 v25, v24, v26\n"
    "v_and_b32 v27, 0xffff0000, v25\n"
    "v_sub_f32 v25, v25, v27\n"
    "v_lshrrev_b32 v24, 16, v24\n"
    "v_or_b32 v24, v24, v27\n"
    "v_lshrrev_b32 v25, 16, v25\n"
    "ds_write_b64 v164, v[24:25] offset:4096\n"
    "v_lshl_add_u32 v235, v232, 8, v235\n"
    "s_add_u32 s84, s96, 16384\n"
    "s_add_u32 s74, s32, 16384\n"
    "s_add_u32 s75, s32, 49152\n"
    "s_add_u32 s77, s33, 16384\n"
    "s_movk_i32 s41, 0\n"
    "s_movk_i32 s42, 4096\n"
    "s_movk_i32 s43, 8192\n"
    "s_movk_i32 s48, 12288\n"
    "s_movk_i32 s49, 16384\n"
    "s_movk_i32 s85, 20480\n"
    "s_lshl_b32 s63, s4, 11\n"
    "s_add_u32 s63, s63, 131072\n"
    "v_add_u32 v254, s63, v2\n"
    "v_xor_b32 v232, 0x800, v254\n"
    "v_xor_b32 v233, 0x1000, v254\n"
    "v_xor_b32 v234, 0x1800, v254\n"
    "v_lshlrev_b32 v198, 16, v176\n"
    "v_and_b32 v199, 0xffff0000, v176\n"
    "v_lshlrev_b32 v200, 16, v177\n"
    "v_and_b32 v201, 0xffff0000, v177\n"
    "v_lshlrev_b32 v202, 16, v178\n"
    "v_and_b32 v203, 0xffff0000, v178\n"
    "v_lshlrev_b32 v204, 16, v179\n"
    "v_and_b32 v205, 0xffff0000, v179\n"
    "v_lshlrev_b32 v206, 16, v180\n"
    "v_and_b32 v207, 0xffff0000, v180\n"
    "buffer_load_dwordx2 v[238:239], v4, s[8:11], s23 offen sc1\n"
    "buffer_load_dwordx4 a[112:115], v7, s[8:11], s76 offen offset:1024 sc1\n"
    "buffer_load_dwordx4 a[116:119], v15, s[8:11], s76 offen offset:0 sc1\n"
    "s_add_u32 s76, s76, 5120\n"
    "buffer_load_dwordx4 a[120:123], v6, s[8:11], s76 offen offset:0 sc1\n"
    "buffer_load_dwordx4 a[124:127], v6, s[8:11], s76 offen offset:1024 sc1\n"
    "buffer_load_dwordx4 a[128:131], v7, s[8:11], s76 offen offset:0 sc1\n"
    "buffer_load_dwordx4 a[132:135], v7, s[8:11], s76 offen offset:1024 sc1\n"
    "buffer_load_dwordx4 a[136:139], v15, s[8:11], s76 offen offset:0 sc1\n"
    "s_add_u32 s76, s76, 5120\n"
    "buffer_load_dwordx4 a[140:143], v6, s[8:11], s76 offen offset:0 sc1\n"
    ".Lf_ffn2:\n"
    "s_waitcnt lgkmcnt(0)\n"
    "s_barrier\n"
    "buffer_load_dwordx4 v[48:51], v62, s[8:11], s84 offen sc1\n"
    "buffer_load_dwordx4 v[188:191], v14, s[8:11], s74 offen sc1\n"
    "buffer_load_dwordx4 v[192:195], v14, s[8:11], s75 offen sc1\n"
    "buffer_load_dwordx2 v[196:197], v5, s[8:11], s77 offen sc1\n"
    "ds_read_b128 v[208:211], v2 offset:0\n"
    "ds_read_b128 v[212:215], v2 offset:2048\n"
    "ds_read_b128 v[216:219], v2 offset:4096\n"
    "ds_read_b128 v[220:223], v2 offset:1024\n"
    "ds_read_b128 v[224:227], v2 offset:3072\n"
    "ds_read_b128 v[228:231], v2 offset:5120\n"
    "s_mov_b32 s83, s23\n"
    "s_waitcnt vmcnt(12)\n"
    "v_cmp_eq_u64 s[86:87], s[50:51], v[238:239]\n"
    "s_nop 0\n"
    "s_andn2_b32 s79, 0xff, s86\n"
    "s_cbranch_scc1 .Lf_slow20\n"
    ".Lf_back21:\n"
    "buffer_load_dwordx4 v[64:67], v235, s[8:11], s12 offen offset:0 sc1\n"
    "buffer_load_dwordx4 v[68:71], v235, s[8:11], s12 offen offset:256 sc1\n"
    "buffer_load_dwordx4 v[72:75], v235, s[8:11], s12 offen offset:2048 sc1\n"
    "buffer_load_dwordx4 v[76:79], v235, s[8:11], s12 offen offset:2304 sc1\n"
    "buffer_load_dwordx4 v[80:83], v235, s[8:11], s13 offen offset:0 sc1\n"
    "buffer_load_dwordx4 v[168:171], v2, s[8:11], s22 offen offset:0 sc1\n"
    "v_mov_b32 v240, -1\n"
    "v_mov_b32 v242, -1\n"
    "s_waitcnt vmcnt(9)\n"
    "v_xor_b32 v248, s50, v49\n"
    "v_bitop3_b32 v248, v248, v51, s50 bitop3:0xf6\n"
    "s_waitcnt vmcnt(8)\n"
    "v_bitop3_b32 v248, v248, v189, s50 bitop3:0xf6\n"
    "v_bitop3_b32 v248, v248, v191, s50 bitop3:0xf6\n"
    "s_waitcnt vmcnt(7)\n"
    "v_bitop3_b32 v248, v248, v193, s50 bitop3:0xf6\n"
    "v_bitop3_b32 v248, v248, v195, s50 bitop3:0xf6\n"
    "s_waitcnt vmcnt(6)\n"
    "v_bitop3_b32 v248, v248, v197, s50 bitop3:0xf6\n"
    "v_cmp_ne_u32 vcc, 0, v248\n"
    "s_nop 0\n"
    "s_cmp_lg_u64 vcc, 0\n"
    "s_cbranch_scc1 .Lf_gslow22\n"
    ".Lf_gback23:\n"
    "buffer_load_dwordx4 v[84:87], v235, s[8:11], s13 offen offset:256 sc1\n"
    "v_and_b32 v189, 0xffff0000, v188\n"
    "v_lshlrev_b32 v188, 16, v188\n"
    "v_and_b32 v191, 0xffff0000, v190\n"
    "v_lshlrev_b32 v190, 16, v190\n"
    "buffer_load_dwordx4 v[172:175], v2, s[8:11], s22 offen offset:1024 sc1\n"
    "v_and_b32 v193, 0xffff0000, v192\n"
    "v_lshlrev_b32 v192, 16, v192\n"
    "v_and_b32 v195, 0xffff0000, v194\n"
    "v_lshlrev_b32 v194, 16, v194\n"
    "v_and_b32 v197, 0xffff0000, v196\n"
    "v_lshlrev_b32 v196, 16, v196\n"
    "buffer_load_dwordx4 v[88:91], v235, s[8:11], s13 offen offset:2048 sc1\n"
    "v_pk_fma_f32 v[52:53], v[198:199], v[50:51], v[48:49] op_sel_hi:[1,0,0] neg_lo:[0,0,1] neg_hi:[0,0,1]\n"
    "v_pk_fma_f32 v[54:55], v[200:201], v[50:51], v[48:49] op_sel_hi:[1,0,0] neg_lo:[0,0,1] neg_hi:[0,0,1]\n"
    "v_pk_fma_f32 v[56:57], v[202:203], v[50:51], v[48:49] op_sel_hi:[1,0,0] neg_lo:[0,0,1] neg_hi:[0,0,1]\n"
    "v_pk_fma_f32 v[58:59], v[204:205], v[50:51], v[48:49] op_sel_hi:[1,0,0] neg_lo:[0,0,1] neg_hi:[0,0,1]\n"
    "v_pk_fma_f32 v[60:61], v[206:207], v[50:51], v[48:49] op_sel_hi:[1,0,0] neg_lo:[0,0,1] neg_hi:[0,0,1]\n"
    "buffer_load_dwordx4 v[176:179], v2, s[8:11], s22 offen offset:2048 sc1\n"
    "v_pk_fma_f32 v[198:199], v[52:53], v[188:189], v[198:199]\n"
    "v_pk_fma_f32 v[200:201], v[54:55], v[190:191], v[200:201]\n"
    "v_pk_fma_f32 v[202:203], v[56:57], v[192:193], v[202:203]\n"
    "v_pk_fma_f32 v[204:205], v[58:59], v[194:195], v[204:205]\n"
    "v_pk_fma_f32 v[206:207], v[60:61], v[196:197], v[206:207]\n"
    "v_cvt_pk_bf16_f32 v56, v206, v207\n"
    "buffer_load_dwordx4 v[92:95], v235, s[8:11], s13 offen offset:2304 sc1\n"
    "v_cvt_pk_bf16_f32 v52, v198, v199\n"
    "v_mov_b32 v57, v56\n"
    "v_cvt_pk_bf16_f32 v53, v200, v201\n"
    "v_cvt_pk_bf16_f32 v54, v202, v203\n"
    "v_cvt_pk_bf16_f32 v55, v204, v205\n"
    "buffer_load_dwordx4 v[180:183], v2, s[8:11], s22 offen offset:3072 sc1\n"
    "v_permlane16_swap_b32 v56, v57\n"
    "v_mov_b32 v58, v56\n"
    "v_mov_b32 v59, v57\n"
    "v_pk_add_f32 v[48:49], v[198:199], v[200:201]\n"
    "v_pk_mul_f32 v[50:51], v[198:199], v[198:199]\n"
    "s_add_u32 s88, s30, 1310720\n"
    "buffer_store_dwordx4 v[52:55], v12, s[8:11], s88 offen sc1\n"
    "v_permlane32_swap_b32 v56, v58\n"
    "v_permlane32_swap_b32 v57, v59\n"
    "v_pk_fma_f32 v[50:51], v[200:201], v[200:201], v[50:51]\n"
    "v_pk_add_f32 v[48:49], v[48:49], v[202:203]\n"
    "v_pk_fma_f32 v[50:51], v[202:203], v[202:203], v[50:51]\n"
    "v_pk_add_f32 v[48:49], v[48:49], v[204:205]\n"
    "v_pk_fma_f32 v[50:51], v[204:205], v[204:205], v[50:51]\n"
    "v_pk_add_f32 v[48:49], v[48:49], v[206:207]\n"
    "v_pk_fma_f32 v[50:51], v[206:207], v[206:207], v[50:51]\n"
    "v_add_f32 v48, v48, v49\n"
    "v_add_f32 v50, v50, v51\n"
    "v_mov_b32 v49, v50\n"
    "s_nop 1\n"
    "v_permlane16_swap_b32 v48, v49\n"
    "v_add_f32 v48, v48, v49\n"
    "v_mov_b32 v49, v48\n"
    "s_nop 1\n"
    "v_permlane32_swap_b32 v48, v49\n"
    "v_add_f32 v48, v48, v49\n"
    "v_mov_b32 v49, v48\n"
    "s_nop 1\n"
    "v_permlane16_swap_b32 v48, v49\n"
    "s_add_u32 s72, s30, 2621440\n"
    "buffer_store_dwordx4 v[240:243], v12, s[8:11], s72 offen sc1\n"
    "s_add_u32 s73, s31, 2621440\n"
    "s_add_u32 s89, s31, 1310720\n"
    "s_add_u32 s90, s36, -131072\n"
    "s_mov_b64 exec, 0xffff\n"
    "buffer_store_dwordx4 v[240:243], v12, s[8:11], s73 offen sc1\n"
    "buffer_store_dwordx4 v[56:59], v12, s[8:11], s89 offen sc1\n"
    "buffer_store_dwordx2 v[48:49], v11, s[8:11], s90 offen sc1\n"
    "s_mov_b64 exec, -1\n"
    "s_waitcnt vmcnt(16) lgkmcnt(5)\n"
    "v_mfma_f32_16x16x32_bf16 v[16:19], v[208:211], v[64:67], 0\n"
    "s_waitcnt vmcnt(15)\n"
    "v_mfma_f32_16x16x32_bf16 v[24:27], v[208:211], v[68:71], 0\n"
    "s_waitcnt lgkmcnt(4)\n"
    "ds_read_b128 v[208:211], v2 offset:6144\n"
    "buffer_load_dwordx4 v[96:99], v235, s[8:11], s14 offen offset:0 sc1\n"
    "v_mfma_f32_16x16x32_bf16 v[32:35], v[212:215], v[64:67], 0\n"
    "v_mfma_f32_16x16x32_bf16 v[40:43], v[212:215], v[68:71], 0\n"
    "s_waitcnt lgkmcnt(4)\n"
    "ds_read_b128 v[212:215], v2 offset:8192\n"
    "v_mfma_f32_16x16x32_bf16 v[48:51], v[216:219], v[64:67], 0\n"
    "v_mfma_f32_16x16x32_bf16 v[56:59], v[216:219], v[68:71], 0\n"
    "s_waitcnt lgkmcnt(4)\n"
    "ds_read_b128 v[216:219], v2 offset:10240\n"
    "v_mfma_f32_16x16x32_bf16 v[20:23], v[220:223], v[64:67], 0\n"
    "v_mfma_f32_16x16x32_bf16 v[28:31], v[220:223], v[68:71], 0\n"
    "s_waitcnt lgkmcnt(4)\n"
    "ds_read_b128 v[220:223], v2 offset:7168\n"
    "buffer_load_dwordx4 v[100:103], v235, s[8:11], s14 offen offset:256 sc1\n"
    "v_mfma_f32_16x16x32_bf16 v[36:39], v[224:227], v[64:67], 0\n"
    "v_mfma_f32_16x16x32_bf16 v[44:47], v[224:227], v[68:71], 0\n"
    "s_waitcnt lgkmcnt(4)\n"
    "ds_read_b128 v[224:227], v2 offset:9216\n"
    "v_mfma_f32_16x16x32_bf16 v[52:55], v[228:231], v[64:67], 0\n"
    "v_mfma_f32_16x16x32_bf16 v[60:63], v[228:231], v[68:71], 0\n"
    "s_waitcnt vmcnt(15) lgkmcnt(4)\n"
    "ds_read_b128 v[228:231], v2 offset:11264\n"
    "buffer_load_dwordx4 v[104:107], v235, s[8:11], s14 offen offset:2048 sc1\n"
    "v_mfma_f32_16x16x32_bf16 v[16:19], v[208:211], v[72:75], v[16:19]\n"
    "v_mfma_f32_16x16x32_bf16 v[24:27], v[208:211], v[76:79], v[24:27]\n"
    "s_waitcnt lgkmcnt(4)\n"
    "ds_read_b128 v[208:211], v2 offset:12288\n"
    "v_mfma_f32_16x16x32_bf16 v[32:35], v[212:215], v[72:75], v[32:35]\n"
    "v_mfma_f32_16x16x32_bf16 v[40:43], v[212:215], v[76:79], v[40:43]\n"
    "s_waitcnt lgkmcnt(4)\n"
    "ds_read_b128 v[212:215], v2 offset:14336\n"
    "v_mfma_f32_16x16x32_bf16 v[48:51], v[216:219], v[72:75], v[48:51]\n"
    "s_waitcnt vmcnt(12)\n"
    "v_add_f32 v240, v168, v172\n"
    "v_add_f32 v241, v169, v173\n"
    "v_mfma_f32_16x16x32_bf16 v[56:59], v[216:219], v[76:79], v[56:59]\n"
    "s_waitcnt lgkmcnt(4)\n"
    "ds_read_b128 v[216:219], v2 offset:16384\n"
    "buffer_load_dwordx4 v[108:111], v235, s[8:11], s14 offen offset:2304 sc1\n"
    "v_mfma_f32_16x16x32_bf16 v[20:23], v[220:223], v[72:75], v[20:23]\n"
    "v_add_f32 v242, v170, v174\n"
    "v_add_f32 v243, v171, v175\n"
    "v_mfma_f32_16x16x32_bf16 v[28:31], v[220:223], v[76:79], v[28:31]\n"
    "s_waitcnt lgkmcnt(4)\n"
    "ds_read_b128 v[220:223], v2 offset:13312\n"
    "v_mfma_f32_16x16x32_bf16 v[36:39], v[224:227], v[72:75], v[36:39]\n"
    "s_waitcnt vmcnt(11)\n"
    "v_add_f32 v240, v240, v176\n"
    "v_add_f32 v241, v241, v177\n"
    "v_mfma_f32_16x16x32_bf16 v[44:47], v[224:227], v[76:79], v[44:47]\n"
    "s_waitcnt lgkmcnt(4)\n"
    "ds_read_b128 v[224:227], v2 offset:15360\n"
    "v_mfma_f32_16x16x32_bf16 v[52:55], v[228:231], v[72:75], v[52:55]\n"
    "v_add_f32 v242, v242, v178\n"
    "v_add_f32 v243, v243, v179\n"
    "v_mfma_f32_16x16x32_bf16 v[60:63], v[228:231], v[76:79], v[60:63]\n"
    "s_waitcnt lgkmcnt(4)\n"
    "ds_read_b128 v[228:231], v2 offset:17408\n"
    "buffer_load_dwordx4 v[112:115], v235, s[8:11], s15 offen offset:0 sc1\n"
    "v_mfma_f32_16x16x32_bf16 v[16:19], v[208:211], v[80:83], v[16:19]\n"
    "s_waitcnt vmcnt(10)\n"
    "v_add_f32 v240, v240, v180\n"
    "v_add_f32 v241, v241, v181\n"
    "v_mfma_f32_16x16x32_bf16 v[24:27], v[208:211], v[84:87], v[24:27]\n"
    "s_waitcnt lgkmcnt(4)\n"
    "ds_read_b128 v[208:211], v2 offset:18432\n"
    "v_mfma_f32_16x16x32_bf16 v[32:35], v[212:215], v[80:83], v[32:35]\n"
    "v_add_f32 v242, v242, v182\n"
    "v_add_f32 v243, v243, v183\n"
    "v_mfma_f32_16x16x32_bf16 v[40:43], v[212:215], v[84:87], v[40:43]\n"
    "s_waitcnt lgkmcnt(4)\n"
    "ds_read_b128 v[212:215], v2 offset:20480\n"
    "buffer_load_dwordx4 v[116:119], v235, s[8:11], s15 offen offset:256 sc1\n"
    "v_mfma_f32_16x16x32_bf16 v[48:51], v[216:219], v[80:83], v[48:51]\n"
    "v_permlane16_swap_b32 v240, v242\n"
    "v_mfma_f32_16x16x32_bf16 v[56:59], v[216:219], v[84:87], v[56:59]\n"
    "s_waitcnt lgkmcnt(4)\n"
    "ds_read_b128 v[216:219], v2 offset:22528\n"
    "v_permlane16_swap_b32 v241, v243\n"
    "v_mfma_f32_16x16x32_bf16 v[20:23], v[220:223], v[80:83], v[20:23]\n"
    "v_add_f32 v240, v240, v242\n"
    "v_mfma_f32_16x16x32_bf16 v[28:31], v[220:223], v[84:87], v[28:31]\n"
    "s_waitcnt lgkmcnt(4)\n"
    "ds_read_b128 v[220:223], v2 offset:19456\n"
    "v_add_f32 v241, v241, v243\n"
    "v_mfma_f32_16x16x32_bf16 v[36:39], v[224:227], v[80:83], v[36:39]\n"
    "s_nop 0\n"
    "v_permlane32_swap_b32 v240, v241\n"
    "v_mfma_f32_16x16x32_bf16 v[44:47], v[224:227], v[84:87], v[44:47]\n"
    "s_waitcnt lgkmcnt(4)\n"
    "ds_read_b128 v[224:227], v2 offset:21504\n"
    "buffer_load_dwordx4 v[120:123], v235, s[8:11], s15 offen offset:2048 sc1\n"
    "v_mfma_f32_16x16x32_bf16 v[52:55], v[228:231], v[80:83], v[52:55]\n"
    "v_add_f32 v240, v240, v241\n"
    "v_mfma_f32_16x16x32_bf16 v[60:63], v[228:231], v[84:87], v[60:63]\n"
    "s_waitcnt lgkmcnt(4)\n"
    "ds_read_b128 v[228:231], v2 offset:23552\n"
    "v_mov_b32 v241, v240\n"
    "v_mfma_f32_16x16x32_bf16 v[16:19], v[208:211], v[88:91], v[16:19]\n"
    "s_nop 0\n"
    "v_permlane32_swap_b32 v240, v241\n"
    "v_mfma_f32_16x16x32_bf16 v[24:27], v[208:211], v[92:95], v[24:27]\n"
    "s_waitcnt lgkmcnt(4)\n"
    "ds_read_b128 v[208:211], v2 offset:24576\n"
    "v_mul_f32 v240, 0x3acccccd, v240\n"
    "v_mfma_f32_16x16x32_bf16 v[32:35], v[212:215], v[88:91], v[32:35]\n"
    "v_mul_f32 v241, 0x3acccccd, v241\n"
    "v_mfma_f32_16x16x32_bf16 v[40:43], v[212:215], v[92:95], v[40:43]\n"
    "s_waitcnt lgkmcnt(4)\n"
    "ds_read_b128 v[212:215], v2 offset:26624\n"
    "buffer_load_dwordx4 v[124:127], v235, s[8:11], s15 offen offset:2304 sc1\n"
    "v_mfma_f32_16x16x32_bf16 v[48:51], v[216:219], v[88:91], v[48:51]\n"
    "v_fma_f32 v241, -v240, v240, v241\n"
    "v_mfma_f32_16x16x32_bf16 v[56:59], v[216:219], v[92:95], v[56:59]\n"
    "s_waitcnt lgkmcnt(4)\n"
    "ds_read_b128 v[216:219], v2 offset:28672\n"
    "v_max_f32 v241, 0, v241\n"
    "v_mfma_f32_16x16x32_bf16 v[20:23], v[220:223], v[88:91], v[20:23]\n"
    "v_cvt_pk_bf16_f32 v241, v241, v241\n"
    "v_mfma_f32_16x16x32_bf16 v[28:31], v[220:223], v[92:95], v[28:31]\n"
    "s_waitcnt lgkmcnt(4)\n"
    "ds_read_b128 v[220:223], v2 offset:25600\n"
    "buffer_load_dwordx4 v[128:131], v235, s[8:11], s16 offen offset:0 sc1\n"
    "v_mfma_f32_16x16x32_bf16 v[36:39], v[224:227], v[88:91], v[36:39]\n"
    "v_lshlrev_b32 v241, 16, v241\n"
    "v_mfma_f32_16x16x32_bf16 v[44:47], v[224:227], v[92:95], v[44:47]\n"
    "s_waitcnt lgkmcnt(4)\n"
    "ds_read_b128 v[224:227], v2 offset:27648\n"
    "v_add_f32 v241, 0x3727c5ac, v241\n"
    "v_mfma_f32_16x16x32_bf16 v[52:55], v[228:231], v[88:91], v[52:55]\n"
    "v_cvt_pk_bf16_f32 v241, v241, v241\n"
    "v_mfma_f32_16x16x32_bf16 v[60:63], v[228:231], v[92:95], v[60:63]\n"
    "s_waitcnt vmcnt(7) lgkmcnt(4)\n"
    "ds_read_b128 v[228:231], v2 offset:29696\n"
    "v_lshlrev_b32 v241, 16, v241\n"
    "v_mfma_f32_16x16x32_bf16 v[16:19], v[208:211], v[96:99], v[16:19]\n"
    "v_sqrt_f32 v241, v241\n"
    "v_mfma_f32_16x16x32_bf16 v[24:27], v[208:211], v[100:103], v[24:27]\n"
    "s_waitcnt lgkmcnt(4)\n"
    "ds_read_b128 v[208:211], v2 offset:30720\n"
    "buffer_load_dwordx4 v[132:135], v235, s[8:11], s16 offen offset:256 sc1\n"
    "v_mfma_f32_16x16x32_bf16 v[32:35], v[212:215], v[96:99], v[32:35]\n"
    "v_cvt_pk_bf16_f32 v241, v241, v241\n"
    "v_mfma_f32_16x16x32_bf16 v[40:43], v[212:215], v[100:103], v[40:43]\n"
    "s_waitcnt lgkmcnt(4)\n"
    "ds_read_b128 v[212:215], v2 offset:32768\n"
    "v_lshlrev_b32 v241, 16, v241\n"
    "v_mfma_f32_16x16x32_bf16 v[48:51], v[216:219], v[96:99], v[48:51]\n"
    "v_rcp_f32 v244, v241\n"
    "v_mfma_f32_16x16x32_bf16 v[56:59], v[216:219], v[100:103], v[56:59]\n"
    "s_waitcnt lgkmcnt(4)\n"
    "ds_read_b128 v[216:219], v2 offset:34816\n"
    "v_mul_f32 v240, -0.5, v240\n"
    "v_mfma_f32_16x16x32_bf16 v[20:23], v[220:223], v[96:99], v[20:23]\n"
    "v_mul_f32 v245, v241, v241\n"
    "v_mfma_f32_16x16x32_bf16 v[28:31], v[220:223], v[100:103], v[28:31]\n"
    "s_waitcnt lgkmcnt(4)\n"
    "ds_read_b128 v[220:223], v2 offset:31744\n"
    "buffer_load_dwordx4 v[136:139], v235, s[8:11], s16 offen offset:2048 sc1\n"
    "v_mfma_f32_16x16x32_bf16 v[36:39], v[224:227], v[96:99], v[36:39]\n"
    "v_mul_f32 v247, v241, v241\n"
    "v_mfma_f32_16x16x32_bf16 v[44:47], v[224:227], v[100:103], v[44:47]\n"
    "s_waitcnt lgkmcnt(4)\n"
    "ds_read_b128 v[224:227], v2 offset:33792\n"
    "v_mov_b32 v248, v240\n"
    "v_mfma_f32_16x16x32_bf16 v[52:55], v[228:231], v[96:99], v[52:55]\n"
    "v_mul_f32 v246, 0xbfb8aa3b, v244\n"
    "v_mfma_f32_16x16x32_bf16 v[60:63], v[228:231], v[100:103], v[60:63]\n"
    "s_waitcnt vmcnt(7) lgkmcnt(4)\n"
    "ds_read_b128 v[228:231], v2 offset:35840\n"
    "buffer_load_dwordx4 v[140:143], v235, s[8:11], s16 offen offset:2304 sc1\n"
    "v_mfma_f32_16x16x32_bf16 v[16:19], v[208:211], v[104:107], v[16:19]\n"
    "v_mul_f32 v244, 0xbfb8aa3b, v244\n"
    "v_mfma_f32_16x16x32_bf16 v[24:27], v[208:211], v[108:111], v[24:27]\n"
    "s_waitcnt lgkmcnt(4)\n"
    "ds_read_b128 v[208:211], v2 offset:36864\n"
    "v_permlane16_swap_b32 v240, v248\n"
    "v_mfma_f32_16x16x32_bf16 v[32:35], v[212:215], v[104:107], v[32:35]\n"
    "v_permlane16_swap_b32 v245, v247\n"
    "v_mfma_f32_16x16x32_bf16 v[40:43], v[212:215], v[108:111], v[40:43]\n"
    "s_waitcnt lgkmcnt(4)\n"
    "ds_read_b128 v[212:215], v2 offset:38912\n"
    "v_permlane16_swap_b32 v244, v246\n"
    "v_mfma_f32_16x16x32_bf16 v[48:51], v[216:219], v[104:107], v[48:51]\n"
    "v_cvt_pk_bf16_f32 v243, v240, v240\n"
    "v_mfma_f32_16x16x32_bf16 v[56:59], v[216:219], v[108:111], v[56:59]\n"
    "s_waitcnt lgkmcnt(4)\n"
    "ds_read_b128 v[216:219], v2 offset:40960\n"
    "buffer_load_dwordx4 v[144:147], v235, s[8:11], s17 offen offset:0 sc1\n"
    "v_mfma_f32_16x16x32_bf16 v[20:23], v[220:223], v[104:107], v[20:23]\n"
    "v_cvt_pk_bf16_f32 v251, v248, v248\n"
    "v_mfma_f32_16x16x32_bf16 v[28:31], v[220:223], v[108:111], v[28:31]\n"
    "s_waitcnt lgkmcnt(4)\n"
    "ds_read_b128 v[220:223], v2 offset:37888\n"
    "v_cvt_pk_bf16_f32 v242, v240, v240\n"
    "v_mfma_f32_16x16x32_bf16 v[36:39], v[224:227], v[104:107], v[36:39]\n"
    "s_mov_b64 exec, 1\n"
    "ds_add_rtn_u32 v9, v3, v3 offset:57348\n"
    "s_mov_b64 exec, -1\n"
    "v_mfma_f32_16x16x32_bf16 v[44:47], v[224:227], v[108:111], v[44:47]\n"
    "s_waitcnt lgkmcnt(5)\n"
    "ds_read_b128 v[224:227], v2 offset:39936\n"
    "v_cvt_pk_bf16_f32 v250, v248, v248\n"
    "v_mfma_f32_16x16x32_bf16 v[52:55], v[228:231], v[104:107], v[52:55]\n"
    "v_cvt_pk_bf16_f32 v241, v240, v240\n"
    "v_mfma_f32_16x16x32_bf16 v[60:63], v[228:231], v[108:111], v[60:63]\n"
    "s_waitcnt vmcnt(7) lgkmcnt(5)\n"
    "ds_read_b128 v[228:231], v2 offset:41984\n"
    "buffer_load_dwordx4 v[148:151], v235, s[8:11], s17 offen offset:256 sc1\n"
    "v_mfma_f32_16x16x32_bf16 v[16:19], v[208:211], v[112:115], v[16:19]\n"
    "v_cvt_pk_bf16_f32 v249, v248, v248\n"
    "v_mfma_f32_16x16x32_bf16 v[24:27], v[208:211], v[116:119], v[24:27]\n"
    "s_waitcnt lgkmcnt(5)\n"
    "ds_read_b128 v[208:211], v2 offset:43008\n"
    "v_cvt_pk_bf16_f32 v240, v240, v240\n"
    "v_mfma_f32_16x16x32_bf16 v[32:35], v[212:215], v[112:115], v[32:35]\n"
    "s_waitcnt lgkmcnt(3)\n"
    "v_readfirstlane_b32 s79, v9\n"
    "v_mfma_f32_16x16x32_bf16 v[40:43], v[212:215], v[116:119], v[40:43]\n"
    "ds_read_b128 v[212:215], v2 offset:45056\n"
    "buffer_load_dwordx4 v[152:155], v235, s[8:11], s17 offen offset:2048 sc1\n"
    "v_mfma_f32_16x16x32_bf16 v[48:51], v[216:219], v[112:115], v[48:51]\n"
    "s_add_u32 s72, s37, -65536\n"
    "s_cmp_eq_u32 s79, 0x30000\n"
    "s_cselect_b64 exec, 0xff, 0\n"
    "buffer_store_dwordx4 v[252:255], v2, s[8:11], s72 offen sc1\n"
    "s_mov_b64 exec, -1\n"
    "v_mfma_f32_16x16x32_bf16 v[56:59], v[216:219], v[116:119], v[56:59]\n"
    "ds_read_b128 v[216:219], v2 offset:47104\n"
    "v_cvt_pk_bf16_f32 v248, v248, v248\n"
    "v_mfma_f32_16x16x32_bf16 v[20:23], v[220:223], v[112:115], v[20:23]\n"
    "v_mfma_f32_16x16x32_bf16 v[28:31], v[220:223], v[116:119], v[28:31]\n"
    "s_waitcnt lgkmcnt(4)\n"
    "ds_read_b128 v[220:223], v2 offset:44032\n"
    "v_mfma_f32_16x16x32_bf16 v[36:39], v[224:227], v[112:115], v[36:39]\n"
    "v_mfma_f32_16x16x32_bf16 v[44:47], v[224:227], v[116:119], v[44:47]\n"
    "s_waitcnt lgkmcnt(4)\n"
    "ds_read_b128 v[224:227], v2 offset:46080\n"
    "buffer_load_dwordx4 v[156:159], v235, s[8:11], s17 offen offset:2304 sc1\n"
    "v_mfma_f32_16x16x32_bf16 v[52:55], v[228:231], v[112:115], v[52:55]\n"
    "v_mfma_f32_16x16x32_bf16 v[60:63], v[228:231], v[116:119], v[60:63]\n"
    "s_waitcnt vmcnt(9) lgkmcnt(4)\n"
    "ds_read_b128 v[228:231], v2 offset:48128\n"
    "v_mfma_f32_16x16x32_bf16 v[16:19], v[208:211], v[120:123], v[16:19]\n"
    "v_mfma_f32_16x16x32_bf16 v[24:27], v[208:211], v[124:127], v[24:27]\n"
    "s_waitcnt lgkmcnt(4)\n"
    "ds_read_b128 v[208:211], v2 offset:49152\n"
    "v_mfma_f32_16x16x32_bf16 v[32:35], v[212:215], v[120:123], v[32:35]\n"
    "v_mfma_f32_16x16x32_bf16 v[40:43], v[212:215], v[124:127], v[40:43]\n"
    "s_waitcnt lgkmcnt(4)\n"
    "ds_read_b128 v[212:215], v2 offset:51200\n"
    "buffer_load_dwordx4 v[160:163], v235, s[8:11], s18 offen offset:0 sc1\n"
    "v_mfma_f32_16x16x32_bf16 v[48:51], v[216:219], v[120:123], v[48:51]\n"
    "v_mfma_f32_16x16x32_bf16 v[56:59], v[216:219], v[124:127], v[56:59]\n"
    "s_waitcnt lgkmcnt(4)\n"
    "ds_read_b128 v[216:219], v2 offset:53248\n"
    "v_mfma_f32_16x16x32_bf16 v[20:23], v[220:223], v[120:123], v[20:23]\n"
    "v_mfma_f32_16x16x32_bf16 v[28:31], v[220:223], v[124:127], v[28:31]\n"
    "s_waitcnt lgkmcnt(4)\n"
    "ds_read_b128 v[220:223], v2 offset:50176\n"
    "buffer_load_dwordx4 v[164:167], v235, s[8:11], s18 offen offset:256 sc1\n"
    "v_mfma_f32_16x16x32_bf16 v[36:39], v[224:227], v[120:123], v[36:39]\n"
    "v_mfma_f32_16x16x32_bf16 v[44:47], v[224:227], v[124:127], v[44:47]\n"
    "s_waitcnt lgkmcnt(4)\n"
    "ds_read_b128 v[224:227], v2 offset:52224\n"
    "v_mfma_f32_16x16x32_bf16 v[52:55], v[228:231], v[120:123], v[52:55]\n"
    "v_mfma_f32_16x16x32_bf16 v[60:63], v[228:231], v[124:127], v[60:63]\n"
    "s_waitcnt vmcnt(9) lgkmcnt(4)\n"
    "ds_read_b128 v[228:231], v2 offset:54272\n"
    "v_mfma_f32_16x16x32_bf16 v[16:19], v[208:211], v[128:131], v[16:19]\n"
    "v_mfma_f32_16x16x32_bf16 v[24:27], v[208:211], v[132:135], v[24:27]\n"
    "s_waitcnt lgkmcnt(4)\n"
    "ds_read_b128 v[208:211], v2 offset:55296\n"
    "buffer_load_dwordx4 a[240:243], v235, s[8:11], s18 offen offset:2048 sc1\n"
    "v_mfma_f32_16x16x32_bf16 v[32:35], v[212:215], v[128:131], v[32:35]\n"
    "v_mfma_f32_16x16x32_bf16 v[40:43], v[212:215], v[132:135], v[40:43]\n"
    "s_waitcnt lgkmcnt(4)\n"
    "ds_read_b128 v[212:215], v2 offset:57344\n"
    "v_mfma_f32_16x16x32_bf16 v[48:51], v[216:219], v[128:131], v[48:51]\n"
    "v_mfma_f32_16x16x32_bf16 v[56:59], v[216:219], v[132:135], v[56:59]\n"
    "s_waitcnt lgkmcnt(4)\n"
    "ds_read_b128 v[216:219], v2 offset:56320\n"
    "v_mfma_f32_16x16x32_bf16 v[20:23], v[220:223], v[128:131], v[20:23]\n"
    "v_mfma_f32_16x16x32_bf16 v[28:31], v[220:223], v[132:135], v[28:31]\n"
    "s_waitcnt lgkmcnt(4)\n"
    "ds_read_b128 v[220:223], v2 offset:59392\n"
    "buffer_load_dwordx4 a[244:247], v235, s[8:11], s18 offen offset:2304 sc1\n"
    "v_mfma_f32_16x16x32_bf16 v[36:39], v[224:227], v[128:131], v[36:39]\n"
    "v_mfma_f32_16x16x32_bf16 v[44:47], v[224:227], v[132:135], v[44:47]\n"
    "s_waitcnt lgkmcnt(4)\n"
    "ds_read_b128 v[224:227], v2 offset:61440\n"
    "v_mfma_f32_16x16x32_bf16 v[52:55], v[228:231], v[128:131], v[52:55]\n"
    "v_mfma_f32_16x16x32_bf16 v[60:63], v[228:231], v[132:135], v[60:63]\n"
    "s_waitcnt vmcnt(9) lgkmcnt(4)\n"
    "ds_read_b128 v[228:231], v2 offset:58368\n"
    "buffer_load_dwordx4 a[248:251], v235, s[8:11], s19 offen offset:0 sc1\n"
    "v_mfma_f32_16x16x32_bf16 v[16:19], v[208:211], v[136:139], v[16:19]\n"
    "v_mfma_f32_16x16x32_bf16 v[24:27], v[208:211], v[140:143], v[24:27]\n"
    "s_waitcnt lgkmcnt(4)\n"
    "ds_read_b128 v[208:211], v2 offset:62464\n"
    "v_mfma_f32_16x16x32_bf16 v[32:35], v[212:215], v[136:139], v[32:35]\n"
    "v_mfma_f32_16x16x32_bf16 v[40:43], v[212:215], v[140:143], v[40:43]\n"
    "s_waitcnt lgkmcnt(4)\n"
    "ds_read_b128 v[212:215], v2 offset:60416\n"
    "v_mfma_f32_16x16x32_bf16 v[20:23], v[216:219], v[136:139], v[20:23]\n"
    "v_mfma_f32_16x16x32_bf16 v[28:31], v[216:219], v[140:143], v[28:31]\n"
    "s_waitcnt lgkmcnt(4)\n"
    "ds_read_b128 v[216:219], v3 offset:2048\n"
    "buffer_load_dwordx4 a[252:255], v235, s[8:11], s19 offen offset:256 sc1\n"
    "v_mfma_f32_16x16x32_bf16 v[48:51], v[220:223], v[136:139], v[48:51]\n"
    "v_mfma_f32_16x16x32_bf16 v[56:59], v[220:223], v[140:143], v[56:59]\n"
    "s_waitcnt vmcnt(9) lgkmcnt(4)\n"
    "ds_read_b128 v[220:223], v2 offset:63488\n"
    "v_mfma_f32_16x16x32_bf16 v[16:19], v[224:227], v[144:147], v[16:19]\n"
    "v_mfma_f32_16x16x32_bf16 v[24:27], v[224:227], v[148:151], v[24:27]\n"
    "s_waitcnt lgkmcnt(4)\n"
    "ds_read_b128 v[224:227], v3 offset:3072\n"
    "v_mfma_f32_16x16x32_bf16 v[36:39], v[228:231], v[136:139], v[36:39]\n"
    "v_mfma_f32_16x16x32_bf16 v[44:47], v[228:231], v[140:143], v[44:47]\n"
    "s_waitcnt lgkmcnt(4)\n"
    "ds_read_b128 v[228:231], v3 offset:0\n"
    "buffer_load_dwordx4 v[168:171], v235, s[8:11], s19 offen offset:2048 sc1\n"
    "v_mfma_f32_16x16x32_bf16 v[20:23], v[208:211], v[144:147], v[20:23]\n"
    "v_mfma_f32_16x16x32_bf16 v[28:31], v[208:211], v[148:151], v[28:31]\n"
    "s_waitcnt lgkmcnt(4)\n"
    "ds_read_b128 v[208:211], v3 offset:8192\n"
    "v_mfma_f32_16x16x32_bf16 v[52:55], v[212:215], v[136:139], v[52:55]\n"
    "v_mfma_f32_16x16x32_bf16 v[60:63], v[212:215], v[140:143], v[60:63]\n"
    "s_waitcnt vmcnt(7) lgkmcnt(4)\n"
    "ds_read_b128 v[212:215], v2 offset:64512\n"
    "buffer_load_dwordx4 v[172:175], v235, s[8:11], s19 offen offset:2304 sc1\n"
    "v_mfma_f32_16x16x32_bf16 v[16:19], v[216:219], v[152:155], v[16:19]\n"
    "v_mfma_f32_16x16x32_bf16 v[24:27], v[216:219], v[156:159], v[24:27]\n"
    "s_waitcnt lgkmcnt(4)\n"
    "ds_read_b128 v[216:219], v3 offset:9216\n"
    "v_mfma_f32_16x16x32_bf16 v[32:35], v[220:223], v[144:147], v[32:35]\n"
    "v_mfma_f32_16x16x32_bf16 v[40:43], v[220:223], v[148:151], v[40:43]\n"
    "s_waitcnt lgkmcnt(4)\n"
    "ds_read_b128 v[220:223], v3 offset:1024\n"
    "v_mfma_f32_16x16x32_bf16 v[20:23], v[224:227], v[152:155], v[20:23]\n"
    "v_mfma_f32_16x16x32_bf16 v[28:31], v[224:227], v[156:159], v[28:31]\n"
    "s_waitcnt lgkmcnt(4)\n"
    "ds_read_b128 v[224:227], v3 offset:14336\n"
    "buffer_load_dwordx4 v[176:179], v235, s[8:11], s20 offen offset:0 sc1\n"
    "v_mfma_f32_16x16x32_bf16 v[48:51], v[228:231], v[144:147], v[48:51]\n"
    "v_mfma_f32_16x16x32_bf16 v[56:59], v[228:231], v[148:151], v[56:59]\n"
    "s_waitcnt vmcnt(7) lgkmcnt(4)\n"
    "ds_read_b128 v[228:231], v3 offset:4096\n"
    "v_mfma_f32_16x16x32_bf16 v[16:19], v[208:211], v[160:163], v[16:19]\n"
    "v_mfma_f32_16x16x32_bf16 v[24:27], v[208:211], v[164:167], v[24:27]\n"
    "s_waitcnt lgkmcnt(4)\n"
    "ds_read_b128 v[208:211], v3 offset:15360\n"
    "v_mfma_f32_16x16x32_bf16 v[36:39], v[212:215], v[144:147], v[36:39]\n"
    "v_mfma_f32_16x16x32_bf16 v[44:47], v[212:215], v[148:151], v[44:47]\n"
    "s_waitcnt lgkmcnt(4)\n"
    "ds_read_b128 v[212:215], v3 offset:6144\n"
    "buffer_load_dwordx4 v[180:183], v235, s[8:11], s20 offen offset:256 sc1\n"
    "v_mfma_f32_16x16x32_bf16 v[20:23], v[216:219], v[160:163], v[20:23]\n"
    "v_mfma_f32_16x16x32_bf16 v[28:31], v[216:219], v[164:167], v[28:31]\n"
    "s_waitcnt lgkmcnt(4)\n"
    "ds_read_b128 v[216:219], v3 offset:20480\n"
    "v_mfma_f32_16x16x32_bf16 v[52:55], v[220:223], v[144:147], v[52:55]\n"
    "v_mfma_f32_16x16x32_bf16 v[60:63], v[220:223], v[148:151], v[60:63]\n"
    "s_waitcnt vmcnt(6) lgkmcnt(4)\n"
    "ds_read_b128 v[220:223], v3 offset:5120\n"
    "buffer_load_dwordx4 v[184:187], v235, s[8:11], s20 offen offset:2048 sc1\n"
    "v_mfma_f32_16x16x32_bf16 v[16:19], v[224:227], a[240:243], v[16:19]\n"
    "v_mfma_f32_16x16x32_bf16 v[24:27], v[224:227], a[244:247], v[24:27]\n"
    "s_waitcnt lgkmcnt(4)\n"
    "ds_read_b128 v[224:227], v3 offset:21504\n"
    "v_mfma_f32_16x16x32_bf16 v[32:35], v[228:231], v[152:155], v[32:35]\n"
    "v_mfma_f32_16x16x32_bf16 v[40:43], v[228:231], v[156:159], v[40:43]\n"
    "s_waitcnt lgkmcnt(4)\n"
    "ds_read_b128 v[228:231], v3 offset:7168\n"
    "v_mfma_f32_16x16x32_bf16 v[20:23], v[208:211], a[240:243], v[20:23]\n"
    "v_mfma_f32_16x16x32_bf16 v[28:31], v[208:211], a[244:247], v[28:31]\n"
    "s_waitcnt lgkmcnt(4)\n"
    "ds_read_b128 v[208:211], v3 offset:26624\n"
    "buffer_load_dwordx4 v[188:191], v235, s[8:11], s20 offen offset:2304 sc1\n"
    "v_mfma_f32_16x16x32_bf16 v[48:51], v[212:215], v[152:155], v[48:51]\n"
    "v_mfma_f32_16x16x32_bf16 v[56:59], v[212:215], v[156:159], v[56:59]\n"
    "s_waitcnt vmcnt(6) lgkmcnt(4)\n"
    "ds_read_b128 v[212:215], v3 offset:10240\n"
    "v_mfma_f32_16x16x32_bf16 v[16:19], v[216:219], a[248:251], v[16:19]\n"
    "v_mfma_f32_16x16x32_bf16 v[24:27], v[216:219], a[252:255], v[24:27]\n"
    "s_waitcnt lgkmcnt(4)\n"
    "ds_read_b128 v[216:219], v3 offset:27648\n"
    "v_mfma_f32_16x16x32_bf16 v[36:39], v[220:223], v[152:155], v[36:39]\n"
    "v_mfma_f32_16x16x32_bf16 v[44:47], v[220:223], v[156:159], v[44:47]\n"
    "s_waitcnt lgkmcnt(4)\n"
    "ds_read_b128 v[220:223], v3 offset:12288\n"
    "buffer_load_dwordx4 v[192:195], v235, s[8:11], s40 offen offset:0 sc1\n"
    "v_mfma_f32_16x16x32_bf16 v[20:23], v[224:227], a[248:251], v[20:23]\n"
    "v_mfma_f32_16x16x32_bf16 v[28:31], v[224:227], a[252:255], v[28:31]\n"
    "s_waitcnt lgkmcnt(4)\n"
    "ds_read_b128 v[224:227], v3 offset:32768\n"
    "v_mfma_f32_16x16x32_bf16 v[52:55], v[228:231], v[152:155], v[52:55]\n"
    "v_mfma_f32_16x16x32_bf16 v[60:63], v[228:231], v[156:159], v[60:63]\n"
    "s_waitcnt vmcnt(5) lgkmcnt(4)\n"
    "ds_read_b128 v[228:231], v3 offset:11264\n"
    "buffer_load_dwordx4 v[196:199], v235, s[8:11], s40 offen offset:256 sc1\n"
    "v_mfma_f32_16x16x32_bf16 v[16:19], v[208:211], v[168:171], v[16:19]\n"
    "v_mfma_f32_16x16x32_bf16 v[24:27], v[208:211], v[172:175], v[24:27]\n"
    "s_waitcnt lgkmcnt(4)\n"
    "ds_read_b128 v[208:211], v3 offset:33792\n"
    "v_mfma_f32_16x16x32_bf16 v[32:35], v[212:215], v[160:163], v[32:35]\n"
    "v_mfma_f32_16x16x32_bf16 v[40:43], v[212:215], v[164:167], v[40:43]\n"
    "s_waitcnt lgkmcnt(4)\n"
    "ds_read_b128 v[212:215], v3 offset:13312\n"
    "v_mfma_f32_16x16x32_bf16 v[20:23], v[216:219], v[168:171], v[20:23]\n"
    "s_add_u32 s83, s23, 65536\n"
    "buffer_load_dwordx2 v[238:239], v4, s[8:11], s83 offen sc1\n"
    "v_mfma_f32_16x16x32_bf16 v[28:31], v[216:219], v[172:175], v[28:31]\n"
    "s_waitcnt lgkmcnt(4)\n"
    "ds_read_b128 v[216:219], v3 offset:38912\n"
    "buffer_load_dwordx4 v[200:203], v235, s[8:11], s40 offen offset:2048 sc1\n"
    "v_mfma_f32_16x16x32_bf16 v[48:51], v[220:223], v[160:163], v[48:51]\n"
    "v_mfma_f32_16x16x32_bf16 v[56:59], v[220:223], v[164:167], v[56:59]\n"
    "s_waitcnt vmcnt(6) lgkmcnt(4)\n"
    "ds_read_b128 v[220:223], v3 offset:16384\n"
    "v_mfma_f32_16x16x32_bf16 v[16:19], v[224:227], v[176:179], v[16:19]\n"
    "v_mfma_f32_16x16x32_bf16 v[24:27], v[224:227], v[180:183], v[24:27]\n"
    "s_waitcnt lgkmcnt(4)\n"
    "ds_read_b128 v[224:227], v3 offset:39936\n"
    "v_mfma_f32_16x16x32_bf16 v[36:39], v[228:231], v[160:163], v[36:39]\n"
    "v_mfma_f32_16x16x32_bf16 v[44:47], v[228:231], v[164:167], v[44:47]\n"
    "s_waitcnt lgkmcnt(4)\n"
    "ds_read_b128 v[228:231], v3 offset:18432\n"
    "buffer_load_dwordx4 v[204:207], v235, s[8:11], s40 offen offset:2304 sc1\n"
    "v_mfma_f32_16x16x32_bf16 v[20:23], v[208:211], v[176:179], v[20:23]\n"
    "v_add_u32 v235, 1310720, v235\n"
    "v_mfma_f32_16x16x32_bf16 v[28:31], v[208:211], v[180:183], v[28:31]\n"
    "s_waitcnt lgkmcnt(4)\n"
    "ds_read_b128 v[208:211], v3 offset:45056\n"
    "v_mfma_f32_16x16x32_bf16 v[52:55], v[212:215], v[160:163], v[52:55]\n"
    "v_mfma_f32_16x16x32_bf16 v[60:63], v[212:215], v[164:167], v[60:63]\n"
    "s_waitcnt vmcnt(5) lgkmcnt(4)\n"
    "ds_read_b128 v[212:215], v3 offset:17408\n"
    "buffer_load_dwordx4 a[144:147], v6, s[8:11], s76 offen offset:1024 sc1\n"
    "v_mfma_f32_16x16x32_bf16 v[16:19], v[216:219], v[184:187], v[16:19]\n"
    "v_mfma_f32_16x16x32_bf16 v[24:27], v[216:219], v[188:191], v[24:27]\n"
    "s_waitcnt lgkmcnt(4)\n"
    "ds_read_b128 v[216:219], v3 offset:46080\n"
    "v_mfma_f32_16x16x32_bf16 v[32:35], v[220:223], a[240:243], v[32:35]\n"
    "v_mfma_f32_16x16x32_bf16 v[40:43], v[220:223], a[244:247], v[40:43]\n"
    "s_waitcnt lgkmcnt(4)\n"
    "ds_read_b128 v[220:223], v3 offset:19456\n"
    "buffer_load_dwordx4 a[148:151], v7, s[8:11], s76 offen offset:0 sc1\n"
    "v_mfma_f32_16x16x32_bf16 v[20:23], v[224:227], v[184:187], v[20:23]\n"
    "v_mfma_f32_16x16x32_bf16 v[28:31], v[224:227], v[188:191], v[28:31]\n"
    "s_waitcnt lgkmcnt(4)\n"
    "ds_read_b128 v[224:227], v3 offset:51200\n"
    "v_mfma_f32_16x16x32_bf16 v[48:51], v[228:231], a[240:243], v[48:51]\n"
    "v_mfma_f32_16x16x32_bf16 v[56:59], v[228:231], a[244:247], v[56:59]\n"
    "s_waitcnt vmcnt(5) lgkmcnt(4)\n"
    "ds_read_b128 v[228:231], v3 offset:52224\n"
    "buffer_load_dwordx4 a[152:155], v7, s[8:11], s76 offen offset:1024 sc1\n"
    "v_mfma_f32_16x16x32_bf16 v[16:19], v[208:211], v[192:195], v[16:19]\n"
    "v_mfma_f32_16x16x32_bf16 v[24:27], v[208:211], v[196:199], v[24:27]\n"
    "s_waitcnt lgkmcnt(4)\n"
    "ds_read_b128 v[208:211], v3 offset:57472\n"
    "v_mfma_f32_16x16x32_bf16 v[36:39], v[212:215], a[240:243], v[36:39]\n"
    "v_mfma_f32_16x16x32_bf16 v[44:47], v[212:215], a[244:247], v[44:47]\n"
    "s_waitcnt lgkmcnt(4)\n"
    "ds_read_b128 v[212:215], v3 offset:58496\n"
    "buffer_load_dwordx4 a[156:159], v15, s[8:11], s76 offen offset:0 sc1\n"
    "v_mfma_f32_16x16x32_bf16 v[20:23], v[216:219], v[192:195], v[20:23]\n"
    "v_mfma_f32_16x16x32_bf16 v[28:31], v[216:219], v[196:199], v[28:31]\n"
    "s_waitcnt lgkmcnt(4)\n"
    "ds_read_b128 v[216:219], v3 offset:22528\n"
    "v_mfma_f32_16x16x32_bf16 v[52:55], v[220:223], a[240:243], v[52:55]\n"
    "v_mfma_f32_16x16x32_bf16 v[60:63], v[220:223], a[244:247], v[60:63]\n"
    "s_waitcnt vmcnt(4) lgkmcnt(4)\n"
    "ds_read_b128 v[220:223], v3 offset:23552\n"
    "v_mfma_f32_16x16x32_bf16 v[16:19], v[224:227], v[200:203], v[16:19]\n"
    "v_mfma_f32_16x16x32_bf16 v[24:27], v[224:227], v[204:207], v[24:27]\n"
    "s_waitcnt lgkmcnt(4)\n"
    "ds_read_b128 v[224:227], v3 offset:28672\n"
    "v_mfma_f32_16x16x32_bf16 v[20:23], v[228:231], v[200:203], v[20:23]\n"
    "v_mfma_f32_16x16x32_bf16 v[28:31], v[228:231], v[204:207], v[28:31]\n"
    "s_waitcnt lgkmcnt(4)\n"
    "ds_read_b128 v[228:231], v3 offset:29696\n"
    "v_mfma_f32_16x16x32_bf16 v[16:19], v[208:211], v[240:243], v[16:19]\n"
    "v_cmp_eq_u64 s[86:87], s[50:51], v[238:239]\n"
    "v_mfma_f32_16x16x32_bf16 v[24:27], v[208:211], v[248:251], v[24:27]\n"
    "s_waitcnt lgkmcnt(4)\n"
    "ds_read_b128 v[208:211], v3 offset:34816\n"
    "v_mfma_f32_16x16x32_bf16 v[20:23], v[212:215], v[240:243], v[20:23]\n"
    "s_andn2_b32 s79, 0xff, s86\n"
    "s_cbranch_scc1 .Lf_slow24\n"
    ".Lf_back25:\n"
    "v_mfma_f32_16x16x32_bf16 v[28:31], v[212:215], v[248:251], v[28:31]\n"
    "s_waitcnt lgkmcnt(4)\n"
    "ds_read_b128 v[212:215], v3 offset:35840\n"
    "buffer_load_dwordx4 v[64:67], v235, s[8:11], s12 offen offset:0 sc1\n"
    "v_mfma_f32_16x16x32_bf16 v[32:35], v[216:219], a[248:251], v[32:35]\n"
    "v_mfma_f32_16x16x32_bf16 v[40:43], v[216:219], a[252:255], v[40:43]\n"
    "s_waitcnt lgkmcnt(4)\n"
    "ds_read_b128 v[216:219], v3 offset:40960\n"
    "v_mul_f32 v20, v16, v20\n"
    "v_mul_f32 v16, v244, v16\n"
    "v_mfma_f32_16x16x32_bf16 v[36:39], v[220:223], a[248:251], v[36:39]\n"
    "v_exp_f32 v16, v16\n"
    "v_mul_f32 v21, v17, v21\n"
    "v_mfma_f32_16x16x32_bf16 v[44:47], v[220:223], a[252:255], v[44:47]\n"
    "s_waitcnt lgkmcnt(4)\n"
    "ds_read_b128 v[220:223], v3 offset:41984\n"
    "buffer_load_dwordx4 v[68:71], v235, s[8:11], s12 offen offset:256 sc1\n"
    "v_fma_f32 v16, v16, v245, v245\n"
    "v_mul_f32 v17, v244, v17\n"
    "v_mul_f32 v22, v18, v22\n"
    "v_mfma_f32_16x16x32_bf16 v[32:35], v[224:227], v[168:171], v[32:35]\n"
    "v_rcp_f32 v16, v16\n"
    "v_mul_f32 v18, v244, v18\n"
    "v_mfma_f32_16x16x32_bf16 v[40:43], v[224:227], v[172:175], v[40:43]\n"
    "s_waitcnt lgkmcnt(4)\n"
    "ds_read_b128 v[224:227], v3 offset:47104\n"
    "v_mul_f32 v16, v16, v20\n"
    "v_mul_f32 v23, v19, v23\n"
    "v_mfma_f32_16x16x32_bf16 v[36:39], v[228:231], v[168:171], v[36:39]\n"
    "v_exp_f32 v17, v17\n"
    "v_mul_f32 v19, v244, v19\n"
    "v_mfma_f32_16x16x32_bf16 v[44:47], v[228:231], v[172:175], v[44:47]\n"
    "s_waitcnt lgkmcnt(4)\n"
    "ds_read_b128 v[228:231], v3 offset:48128\n"
    "buffer_load_dwordx4 v[72:75], v235, s[8:11], s12 offen offset:2048 sc1\n"
    "v_fma_f32 v17, v17, v245, v245\n"
    "v_mul_f32 v28, v24, v28\n"
    "v_mul_f32 v24, v246, v24\n"
    "v_mfma_f32_16x16x32_bf16 v[32:35], v[208:211], v[176:179], v[32:35]\n"
    "v_rcp_f32 v17, v17\n"
    "v_mul_f32 v29, v25, v29\n"
    "v_mfma_f32_16x16x32_bf16 v[40:43], v[208:211], v[180:183], v[40:43]\n"
    "s_waitcnt lgkmcnt(4)\n"
    "ds_read_b128 v[208:211], v3 offset:53248\n"
    "v_mul_f32 v17, v17, v21\n"
    "v_mul_f32 v25, v246, v25\n"
    "v_mfma_f32_16x16x32_bf16 v[36:39], v[212:215], v[176:179], v[36:39]\n"
    "v_exp_f32 v18, v18\n"
    "v_mul_f32 v30, v26, v30\n"
    "v_mfma_f32_16x16x32_bf16 v[44:47], v[212:215], v[180:183], v[44:47]\n"
    "s_waitcnt lgkmcnt(4)\n"
    "ds_read_b128 v[212:215], v3 offset:54272\n"
    "buffer_load_dwordx4 v[76:79], v235, s[8:11], s12 offen offset:2304 sc1\n"
    "v_fma_f32 v18, v18, v245, v245\n"
    "v_mul_f32 v26, v246, v26\n"
    "v_mul_f32 v31, v27, v31\n"
    "v_mfma_f32_16x16x32_bf16 v[32:35], v[216:219], v[184:187], v[32:35]\n"
    "v_rcp_f32 v18, v18\n"
    "v_mul_f32 v27, v246, v27\n"
    "v_mfma_f32_16x16x32_bf16 v[40:43], v[216:219], v[188:191], v[40:43]\n"
    "s_waitcnt lgkmcnt(4)\n"
    "ds_read_b128 v[216:219], v3 offset:59520\n"
    "v_mul_f32 v18, v18, v22\n"
    "v_cvt_pk_bf16_f32 v20, v16, v17\n"
    "v_exp_f32 v19, v19\n"
    "v_mfma_f32_16x16x32_bf16 v[36:39], v[220:223], v[184:187], v[36:39]\n"
    "v_exp_f32 v24, v24\n"
    "v_mfma_f32_16x16x32_bf16 v[44:47], v[220:223], v[188:191], v[44:47]\n"
    "s_waitcnt lgkmcnt(4)\n"
    "ds_read_b128 v[220:223], v3 offset:60544\n"
    "buffer_load_dwordx4 v[80:83], v235, s[8:11], s13 offen offset:0 sc1\n"
    "v_fma_f32 v19, v19, v245, v245\n"
    "v_fma_f32 v24, v24, v247, v247\n"
    "v_mfma_f32_16x16x32_bf16 v[32:35], v[224:227], v[192:195], v[32:35]\n"
    "v_rcp_f32 v19, v19\n"
    "v_rcp_f32 v24, v24\n"
    "v_mfma_f32_16x16x32_bf16 v[40:43], v[224:227], v[196:199], v[40:43]\n"
    "s_waitcnt lgkmcnt(4)\n"
    "ds_read_b128 v[224:227], v3 offset:24576\n"
    "v_mul_f32 v19, v19, v23\n"
    "v_mul_f32 v24, v24, v28\n"
    "v_mfma_f32_16x16x32_bf16 v[36:39], v[228:231], v[192:195], v[36:39]\n"
    "v_exp_f32 v25, v25\n"
    "v_mfma_f32_16x16x32_bf16 v[44:47], v[228:231], v[196:199], v[44:47]\n"
    "s_waitcnt lgkmcnt(4)\n"
    "ds_read_b128 v[228:231], v3 offset:25600\n"
    "buffer_load_dwordx4 v[84:87], v235, s[8:11], s13 offen offset:256 sc1\n"
    "v_fma_f32 v25, v25, v247, v247\n"
    "v_cvt_pk_bf16_f32 v21, v18, v19\n"
    "v_exp_f32 v26, v26\n"
    "v_mfma_f32_16x16x32_bf16 v[32:35], v[208:211], v[200:203], v[32:35]\n"
    "v_rcp_f32 v25, v25\n"
    "v_mfma_f32_16x16x32_bf16 v[40:43], v[208:211], v[204:207], v[40:43]\n"
    "s_waitcnt lgkmcnt(4)\n"
    "ds_read_b128 v[208:211], v3 offset:30720\n"
    "v_mul_f32 v25, v25, v29\n"
    "v_fma_f32 v26, v26, v247, v247\n"
    "v_exp_f32 v27, v27\n"
    "v_mfma_f32_16x16x32_bf16 v[36:39], v[212:215], v[200:203], v[36:39]\n"
    "v_rcp_f32 v26, v26\n"
    "v_mfma_f32_16x16x32_bf16 v[44:47], v[212:215], v[204:207], v[44:47]\n"
    "s_waitcnt lgkmcnt(4)\n"
    "ds_read_b128 v[212:215], v3 offset:31744\n"
    "buffer_load_dwordx4 v[88:91], v235, s[8:11], s13 offen offset:2048 sc1\n"
    "v_mul_f32 v26, v26, v30\n"
    "v_fma_f32 v27, v27, v247, v247\n"
    "v_mfma_f32_16x16x32_bf16 v[32:35], v[216:219], v[240:243], v[32:35]\n"
    "v_rcp_f32 v27, v27\n"
    "v_cvt_pk_bf16_f32 v22, v24, v25\n"
    "v_mfma_f32_16x16x32_bf16 v[40:43], v[216:219], v[248:251], v[40:43]\n"
    "s_waitcnt lgkmcnt(4)\n"
    "ds_read_b128 v[216:219], v3 offset:36864\n"
    "v_mul_f32 v27, v27, v31\n"
    "v_permlane16_swap_b32 v20, v22\n"
    "v_mfma_f32_16x16x32_bf16 v[36:39], v[220:223], v[240:243], v[36:39]\n"
    "v_cvt_pk_bf16_f32 v23, v26, v27\n"
    "v_mfma_f32_16x16x32_bf16 v[44:47], v[220:223], v[248:251], v[44:47]\n"
    "s_waitcnt lgkmcnt(4)\n"
    "ds_read_b128 v[220:223], v3 offset:37888\n"
    "buffer_load_dwordx4 v[92:95], v235, s[8:11], s13 offen offset:2304 sc1\n"
    "v_permlane16_swap_b32 v21, v23\n"
    "v_mfma_f32_16x16x32_bf16 v[48:51], v[224:227], a[248:251], v[48:51]\n"
    "buffer_store_dwordx4 v[20:23], v10, s[8:11], s24 offen sc1\n"
    "v_mfma_f32_16x16x32_bf16 v[56:59], v[224:227], a[252:255], v[56:59]\n"
    "s_waitcnt lgkmcnt(4)\n"
    "ds_read_b128 v[224:227], v3 offset:43008\n"
    "v_mul_f32 v36, v32, v36\n"
    "v_mul_f32 v32, v244, v32\n"
    "v_mfma_f32_16x16x32_bf16 v[52:55], v[228:231], a[248:251], v[52:55]\n"
    "v_exp_f32 v32, v32\n"
    "v_mul_f32 v37, v33, v37\n"
    "v_mfma_f32_16x16x32_bf16 v[60:63], v[228:231], a[252:255], v[60:63]\n"
    "s_waitcnt lgkmcnt(4)\n"
    "ds_read_b128 v[228:231], v3 offset:44032\n"
    "buffer_load_dwordx4 v[96:99], v235, s[8:11], s14 offen offset:0 sc1\n"
    "v_fma_f32 v32, v32, v245, v245\n"
    "v_mul_f32 v33, v244, v33\n"
    "v_mul_f32 v38, v34, v38\n"
    "v_mfma_f32_16x16x32_bf16 v[48:51], v[208:211], v[168:171], v[48:51]\n"
    "v_rcp_f32 v32, v32\n"
    "v_mul_f32 v34, v244, v34\n"
    "v_mfma_f32_16x16x32_bf16 v[56:59], v[208:211], v[172:175], v[56:59]\n"
    "s_waitcnt lgkmcnt(4)\n"
    "ds_read_b128 v[208:211], v3 offset:49152\n"
    "v_mul_f32 v32, v32, v36\n"
    "v_mul_f32 v39, v35, v39\n"
    "v_mfma_f32_16x16x32_bf16 v[52:55], v[212:215], v[168:171], v[52:55]\n"
    "v_exp_f32 v33, v33\n"
    "v_mul_f32 v35, v244, v35\n"
    "v_mfma_f32_16x16x32_bf16 v[60:63], v[212:215], v[172:175], v[60:63]\n"
    "s_waitcnt lgkmcnt(4)\n"
    "ds_read_b128 v[212:215], v3 offset:50176\n"
    "buffer_load_dwordx4 v[100:103], v235, s[8:11], s14 offen offset:256 sc1\n"
    "v_fma_f32 v33, v33, v245, v245\n"
    "v_mul_f32 v44, v40, v44\n"
    "v_mul_f32 v40, v246, v40\n"
    "v_mfma_f32_16x16x32_bf16 v[48:51], v[216:219], v[176:179], v[48:51]\n"
    "v_rcp_f32 v33, v33\n"
    "v_mul_f32 v45, v41, v45\n"
    "v_mfma_f32_16x16x32_bf16 v[56:59], v[216:219], v[180:183], v[56:59]\n"
    "s_waitcnt lgkmcnt(4)\n"
    "ds_read_b128 v[216:219], v3 offset:55296\n"
    "v_mul_f32 v33, v33, v37\n"
    "v_mul_f32 v41, v246, v41\n"
    "v_mfma_f32_16x16x32_bf16 v[52:55], v[220:223], v[176:179], v[52:55]\n"
    "v_exp_f32 v34, v34\n"
    "v_mul_f32 v46, v42, v46\n"
    "v_mfma_f32_16x16x32_bf16 v[60:63], v[220:223], v[180:183], v[60:63]\n"
    "s_waitcnt lgkmcnt(4)\n"
    "ds_read_b128 v[220:223], v3 offset:56320\n"
    "buffer_load_dwordx4 v[104:107], v235, s[8:11], s14 offen offset:2048 sc1\n"
    "v_fma_f32 v34, v34, v245, v245\n"
    "v_mul_f32 v42, v246, v42\n"
    "v_mul_f32 v47, v43, v47\n"
    "v_mfma_f32_16x16x32_bf16 v[48:51], v[224:227], v[184:187], v[48:51]\n"
    "v_rcp_f32 v34, v34\n"
    "v_mul_f32 v43, v246, v43\n"
    "v_mfma_f32_16x16x32_bf16 v[56:59], v[224:227], v[188:191], v[56:59]\n"
    "s_waitcnt lgkmcnt(4)\n"
    "ds_read_b128 v[224:227], v3 offset:61568\n"
    "v_mul_f32 v34, v34, v38\n"
    "v_cvt_pk_bf16_f32 v36, v32, v33\n"
    "v_exp_f32 v35, v35\n"
    "v_mfma_f32_16x16x32_bf16 v[52:55], v[228:231], v[184:187], v[52:55]\n"
    "v_exp_f32 v40, v40\n"
    "v_mfma_f32_16x16x32_bf16 v[60:63], v[228:231], v[188:191], v[60:63]\n"
    "s_waitcnt lgkmcnt(4)\n"
    "ds_read_b128 v[228:231], v3 offset:62592\n"
    "buffer_load_dwordx4 v[108:111], v235, s[8:11], s14 offen offset:2304 sc1\n"
    "v_fma_f32 v35, v35, v245, v245\n"
    "v_fma_f32 v40, v40, v247, v247\n"
    "v_mfma_f32_16x16x32_bf16 v[48:51], v[208:211], v[192:195], v[48:51]\n"
    "v_rcp_f32 v35, v35\n"
    "v_rcp_f32 v40, v40\n"
    "v_mfma_f32_16x16x32_bf16 v[56:59], v[208:211], v[196:199], v[56:59]\n"
    "s_waitcnt lgkmcnt(4)\n"
    "ds_read_b128 v[208:211], v2 offset:0\n"
    "v_mul_f32 v35, v35, v39\n"
    "v_mul_f32 v40, v40, v44\n"
    "v_mfma_f32_16x16x32_bf16 v[52:55], v[212:215], v[192:195], v[52:55]\n"
    "v_exp_f32 v41, v41\n"
    "v_mfma_f32_16x16x32_bf16 v[60:63], v[212:215], v[196:199], v[60:63]\n"
    "s_waitcnt lgkmcnt(4)\n"
    "ds_read_b128 v[212:215], v2 offset:1024\n"
    "buffer_load_dwordx4 v[112:115], v235, s[8:11], s15 offen offset:0 sc1\n"
    "v_fma_f32 v41, v41, v247, v247\n"
    "v_cvt_pk_bf16_f32 v37, v34, v35\n"
    "v_exp_f32 v42, v42\n"
    "v_mfma_f32_16x16x32_bf16 v[48:51], v[216:219], v[200:203], v[48:51]\n"
    "v_rcp_f32 v41, v41\n"
    "v_mfma_f32_16x16x32_bf16 v[56:59], v[216:219], v[204:207], v[56:59]\n"
    "s_waitcnt lgkmcnt(4)\n"
    "ds_read_b128 v[216:219], v2 offset:6144\n"
    "v_mul_f32 v41, v41, v45\n"
    "v_fma_f32 v42, v42, v247, v247\n"
    "v_exp_f32 v43, v43\n"
    "v_mfma_f32_16x16x32_bf16 v[52:55], v[220:223], v[200:203], v[52:55]\n"
    "v_rcp_f32 v42, v42\n"
    "v_mfma_f32_16x16x32_bf16 v[60:63], v[220:223], v[204:207], v[60:63]\n"
    "s_waitcnt lgkmcnt(4)\n"
    "ds_read_b128 v[220:223], v2 offset:7168\n"
    "v_mul_f32 v42, v42, v46\n"
    "v_fma_f32 v43, v43, v247, v247\n"
    "v_mfma_f32_16x16x32_bf16 v[48:51], v[224:227], v[240:243], v[48:51]\n"
    "s_add_u32 s91, s22, 131072\n"
    "buffer_load_dwordx4 v[192:195], v2, s[8:11], s91 offen offset:0 sc1\n"
    "v_cvt_pk_bf16_f32 v38, v40, v41\n"
    "v_rcp_f32 v43, v43\n"
    "v_mfma_f32_16x16x32_bf16 v[56:59], v[224:227], v[248:251], v[56:59]\n"
    "s_waitcnt lgkmcnt(4)\n"
    "ds_read_b128 v[224:227], v2 offset:2048\n"
    "buffer_load_dwordx4 v[116:119], v235, s[8:11], s15 offen offset:256 sc1\n"
    "v_mul_f32 v43, v43, v47\n"
    "v_permlane16_swap_b32 v36, v38\n"
    "v_mfma_f32_16x16x32_bf16 v[52:55], v[228:231], v[240:243], v[52:55]\n"
    "v_cvt_pk_bf16_f32 v39, v42, v43\n"
    "v_mfma_f32_16x16x32_bf16 v[60:63], v[228:231], v[248:251], v[60:63]\n"
    "s_waitcnt vmcnt(14) lgkmcnt(4)\n"
    "ds_read_b128 v[228:231], v2 offset:12288\n"
    "v_permlane16_swap_b32 v37, v39\n"
    "v_mfma_f32_16x16x32_bf16 v[16:19], v[208:211], v[64:67], 0\n"
    "buffer_store_dwordx4 v[36:39], v10, s[8:11], s25 offen sc1\n"
    "v_mfma_f32_16x16x32_bf16 v[24:27], v[208:211], v[68:71], 0\n"
    "s_waitcnt lgkmcnt(4)\n"
    "ds_read_b128 v[208:211], v2 offset:3072\n"
    "buffer_load_dwordx4 v[120:123], v235, s[8:11], s15 offen offset:2048 sc1\n"
    "v_mul_f32 v52, v48, v52\n"
    "v_mul_f32 v48, v244, v48\n"
    "v_mfma_f32_16x16x32_bf16 v[20:23], v[212:215], v[64:67], 0\n"
    "v_exp_f32 v48, v48\n"
    "v_mfma_f32_16x16x32_bf16 v[28:31], v[212:215], v[68:71], 0\n"
    "s_waitcnt vmcnt(14) lgkmcnt(4)\n"
    "ds_read_b128 v[212:215], v2 offset:13312\n"
    "v_fma_f32 v48, v48, v245, v245\n"
    "v_mul_f32 v53, v49, v53\n"
    "v_mfma_f32_16x16x32_bf16 v[16:19], v[216:219], v[72:75], v[16:19]\n"
    "v_rcp_f32 v48, v48\n"
    "v_mul_f32 v49, v244, v49\n"
    "v_mfma_f32_16x16x32_bf16 v[24:27], v[216:219], v[76:79], v[24:27]\n"
    "s_waitcnt lgkmcnt(4)\n"
    "ds_read_b128 v[216:219], v2 offset:8192\n"
    "buffer_load_dwordx4 v[124:127], v235, s[8:11], s15 offen offset:2304 sc1\n"
    "v_mul_f32 v48, v48, v52\n"
    "v_mul_f32 v54, v50, v54\n"
    "v_mfma_f32_16x16x32_bf16 v[20:23], v[220:223], v[72:75], v[20:23]\n"
    "v_exp_f32 v49, v49\n"
    "v_mfma_f32_16x16x32_bf16 v[28:31], v[220:223], v[76:79], v[28:31]\n"
    "s_waitcnt lgkmcnt(4)\n"
    "ds_read_b128 v[220:223], v2 offset:18432\n"
    "v_fma_f32 v49, v49, v245, v245\n"
    "v_mul_f32 v50, v244, v50\n"
    "v_mfma_f32_16x16x32_bf16 v[32:35], v[224:227], v[64:67], 0\n"
    "v_rcp_f32 v49, v49\n"
    "v_mul_f32 v55, v51, v55\n"
    "v_mfma_f32_16x16x32_bf16 v[40:43], v[224:227], v[68:71], 0\n"
    "s_waitcnt vmcnt(13) lgkmcnt(4)\n"
    "ds_read_b128 v[224:227], v2 offset:9216\n"
    "v_mul_f32 v49, v49, v53\n"
    "v_mul_f32 v51, v244, v51\n"
    "v_mfma_f32_16x16x32_bf16 v[16:19], v[228:231], v[80:83], v[16:19]\n"
    "s_add_u32 s92, s22, 131072\n"
    "buffer_load_dwordx4 v[196:199], v2, s[8:11], s92 offen offset:1024 sc1\n"
    "v_mul_f32 v60, v56, v60\n"
    "v_mul_f32 v56, v246, v56\n"
    "v_mfma_f32_16x16x32_bf16 v[24:27], v[228:231], v[84:87], v[24:27]\n"
    "s_waitcnt lgkmcnt(4)\n"
    "ds_read_b128 v[228:231], v2 offset:19456\n"
    "buffer_load_dwordx4 v[128:131], v235, s[8:11], s16 offen offset:0 sc1\n"
    "v_mul_f32 v61, v57, v61\n"
    "v_mul_f32 v57, v246, v57\n"
    "v_mfma_f32_16x16x32_bf16 v[36:39], v[208:211], v[64:67], 0\n"
    "v_exp_f32 v50, v50\n"
    "v_mul_f32 v62, v58, v62\n"
    "v_mfma_f32_16x16x32_bf16 v[44:47], v[208:211], v[68:71], 0\n"
    "s_waitcnt lgkmcnt(4)\n"
    "ds_read_b128 v[208:211], v2 offset:14336\n"
    "v_fma_f32 v50, v50, v245, v245\n"
    "v_mul_f32 v58, v246, v58\n"
    "v_mfma_f32_16x16x32_bf16 v[20:23], v[212:215], v[80:83], v[20:23]\n"
    "v_rcp_f32 v50, v50\n"
    "v_mfma_f32_16x16x32_bf16 v[28:31], v[212:215], v[84:87], v[28:31]\n"
    "s_waitcnt lgkmcnt(4)\n"
    "ds_read_b128 v[212:215], v2 offset:24576\n"
    "buffer_load_dwordx4 v[132:135], v235, s[8:11], s16 offen offset:256 sc1\n"
    "v_mul_f32 v50, v50, v54\n"
    "v_mul_f32 v63, v59, v63\n"
    "v_mfma_f32_16x16x32_bf16 v[32:35], v[216:219], v[72:75], v[32:35]\n"
    "v_exp_f32 v51, v51\n"
    "v_mul_f32 v59, v246, v59\n"
    "v_mfma_f32_16x16x32_bf16 v[40:43], v[216:219], v[76:79], v[40:43]\n"
    "s_waitcnt vmcnt(14) lgkmcnt(4)\n"
    "ds_read_b128 v[216:219], v2 offset:15360\n"
    "v_fma_f32 v51, v51, v245, v245\n"
    "v_cvt_pk_bf16_f32 v52, v48, v49\n"
    "v_mfma_f32_16x16x32_bf16 v[16:19], v[220:223], v[88:91], v[16:19]\n"
    "v_rcp_f32 v51, v51\n"
    "v_mfma_f32_16x16x32_bf16 v[24:27], v[220:223], v[92:95], v[24:27]\n"
    "s_waitcnt lgkmcnt(4)\n"
    "ds_read_b128 v[220:223], v2 offset:25600\n"
    "buffer_load_dwordx4 v[136:139], v235, s[8:11], s16 offen offset:2048 sc1\n"
    "v_mul_f32 v51, v51, v55\n"
    "v_exp_f32 v56, v56\n"
    "v_mfma_f32_16x16x32_bf16 v[36:39], v[224:227], v[72:75], v[36:39]\n"
    "v_exp_f32 v57, v57\n"
    "v_mfma_f32_16x16x32_bf16 v[44:47], v[224:227], v[76:79], v[44:47]\n"
    "s_waitcnt lgkmcnt(4)\n"
    "ds_read_b128 v[224:227], v2 offset:20480\n"
    "v_fma_f32 v56, v56, v247, v247\n"
    "v_fma_f32 v57, v57, v247, v247\n"
    "v_mfma_f32_16x16x32_bf16 v[20:23], v[228:231], v[88:91], v[20:23]\n"
    "v_rcp_f32 v56, v56\n"
    "v_mfma_f32_16x16x32_bf16 v[28:31], v[228:231], v[92:95], v[28:31]\n"
    "s_waitcnt lgkmcnt(4)\n"
    "ds_read_b128 v[228:231], v2 offset:30720\n"
    "v_mul_f32 v56, v56, v60\n"
    "v_cvt_pk_bf16_f32 v53, v50, v51\n"
    "v_mfma_f32_16x16x32_bf16 v[32:35], v[208:211], v[80:83], v[32:35]\n"
    "s_add_u32 s65, s22, 131072\n"
    "buffer_load_dwordx4 v[200:203], v2, s[8:11], s65 offen offset:2048 sc1\n"
    "v_rcp_f32 v57, v57\n"
    "v_exp_f32 v58, v58\n"
    "v_mfma_f32_16x16x32_bf16 v[40:43], v[208:211], v[84:87], v[40:43]\n"
    "s_waitcnt vmcnt(13) lgkmcnt(4)\n"
    "ds_read_b128 v[208:211], v2 offset:21504\n"
    "buffer_load_dwordx4 v[140:143], v235, s[8:11], s16 offen offset:2304 sc1\n"
    "v_mul_f32 v57, v57, v61\n"
    "v_mfma_f32_16x16x32_bf16 v[16:19], v[212:215], v[96:99], v[16:19]\n"
    "v_exp_f32 v59, v59\n"
    "v_mfma_f32_16x16x32_bf16 v[24:27], v[212:215], v[100:103], v[24:27]\n"
    "s_waitcnt lgkmcnt(4)\n"
    "ds_read_b128 v[212:215], v2 offset:31744\n"
    "v_fma_f32 v58, v58, v247, v247\n"
    "v_fma_f32 v59, v59, v247, v247\n"
    "v_mfma_f32_16x16x32_bf16 v[36:39], v[216:219], v[80:83], v[36:39]\n"
    "v_rcp_f32 v58, v58\n"
    "v_cvt_pk_bf16_f32 v54, v56, v57\n"
    "v_mfma_f32_16x16x32_bf16 v[44:47], v[216:219], v[84:87], v[44:47]\n"
    "s_waitcnt lgkmcnt(4)\n"
    "ds_read_b128 v[216:219], v2 offset:36864\n"
    "buffer_load_dwordx4 v[144:147], v235, s[8:11], s17 offen offset:0 sc1\n"
    "v_mul_f32 v58, v58, v62\n"
    "v_rcp_f32 v59, v59\n"
    "v_mfma_f32_16x16x32_bf16 v[20:23], v[220:223], v[96:99], v[20:23]\n"
    "v_permlane16_swap_b32 v52, v54\n"
    "v_mfma_f32_16x16x32_bf16 v[28:31], v[220:223], v[100:103], v[28:31]\n"
    "s_waitcnt lgkmcnt(4)\n"
    "ds_read_b128 v[220:223], v2 offset:4096\n"
    "v_mul_f32 v59, v59, v63\n"
    "v_mfma_f32_16x16x32_bf16 v[32:35], v[224:227], v[88:91], v[32:35]\n"
    "v_cvt_pk_bf16_f32 v55, v58, v59\n"
    "v_mfma_f32_16x16x32_bf16 v[40:43], v[224:227], v[92:95], v[40:43]\n"
    "s_waitcnt vmcnt(13) lgkmcnt(4)\n"
    "ds_read_b128 v[224:227], v2 offset:37888\n"
    "buffer_load_dwordx4 v[148:151], v235, s[8:11], s17 offen offset:256 sc1\n"
    "v_permlane16_swap_b32 v53, v55\n"
    "v_mfma_f32_16x16x32_bf16 v[16:19], v[228:231], v[104:107], v[16:19]\n"
    "buffer_store_dwordx4 v[52:55], v10, s[8:11], s26 offen sc1\n"
    "v_mfma_f32_16x16x32_bf16 v[24:27], v[228:231], v[108:111], v[24:27]\n"
    "s_waitcnt lgkmcnt(4)\n"
    "ds_read_b128 v[228:231], v2 offset:5120\n"
    "v_mfma_f32_16x16x32_bf16 v[36:39], v[208:211], v[88:91], v[36:39]\n"
    "v_mfma_f32_16x16x32_bf16 v[44:47], v[208:211], v[92:95], v[44:47]\n"
    "s_waitcnt lgkmcnt(4)\n"
    "ds_read_b128 v[208:211], v2 offset:26624\n"
    "v_mfma_f32_16x16x32_bf16 v[20:23], v[212:215], v[104:107], v[20:23]\n"
    "s_add_u32 s66, s22, 131072\n"
    "buffer_load_dwordx4 v[204:207], v2, s[8:11], s66 offen offset:3072 sc1\n"
    "v_mfma_f32_16x16x32_bf16 v[28:31], v[212:215], v[108:111], v[28:31]\n"
    "s_waitcnt vmcnt(13) lgkmcnt(4)\n"
    "ds_read_b128 v[212:215], v2 offset:43008\n"
    "buffer_load_dwordx4 v[152:155], v235, s[8:11], s17 offen offset:2048 sc1\n"
    "v_mfma_f32_16x16x32_bf16 v[16:19], v[216:219], v[112:115], v[16:19]\n"
    "v_mfma_f32_16x16x32_bf16 v[24:27], v[216:219], v[116:119], v[24:27]\n"
    "s_waitcnt lgkmcnt(4)\n"
    "ds_read_b128 v[216:219], v2 offset:10240\n"
    "v_mfma_f32_16x16x32_bf16 v[48:51], v[220:223], v[64:67], 0\n"
    "v_mfma_f32_16x16x32_bf16 v[56:59], v[220:223], v[68:71], 0\n"
    "s_waitcnt lgkmcnt(4)\n"
    "ds_read_b128 v[220:223], v2 offset:44032\n"
    "buffer_load_dwordx4 v[156:159], v235, s[8:11], s17 offen offset:2304 sc1\n"
    "v_mfma_f32_16x16x32_bf16 v[20:23], v[224:227], v[112:115], v[20:23]\n"
    "v_mfma_f32_16x16x32_bf16 v[28:31], v[224:227], v[116:119], v[28:31]\n"
    "s_waitcnt lgkmcnt(4)\n"
    "ds_read_b128 v[224:227], v2 offset:27648\n"
    "v_mfma_f32_16x16x32_bf16 v[52:55], v[228:231], v[64:67], 0\n"
    "v_mfma_f32_16x16x32_bf16 v[60:63], v[228:231], v[68:71], 0\n"
    "s_waitcnt lgkmcnt(4)\n"
    "ds_read_b128 v[228:231], v2 offset:49152\n"
    "buffer_load_dwordx4 v[160:163], v235, s[8:11], s18 offen offset:0 sc1\n"
    "v_mfma_f32_16x16x32_bf16 v[32:35], v[208:211], v[96:99], v[32:35]\n"
    "v_add_u32 v236, s29, v2\n"
    "v_xor_b32 v237, 0x400, v236\n"
    "v_mfma_f32_16x16x32_bf16 v[40:43], v[208:211], v[100:103], v[40:43]\n"
    "s_waitcnt vmcnt(13) lgkmcnt(4)\n"
    "ds_read_b128 v[208:211], v2 offset:11264\n"
    "v_mfma_f32_16x16x32_bf16 v[16:19], v[212:215], v[120:123], v[16:19]\n"
    "v_mfma_f32_16x16x32_bf16 v[24:27], v[212:215], v[124:127], v[24:27]\n"
    "s_waitcnt lgkmcnt(4)\n"
    "ds_read_b128 v[212:215], v2 offset:50176\n"
    "v_mfma_f32_16x16x32_bf16 v[48:51], v[216:219], v[72:75], v[48:51]\n"
    "v_mfma_f32_16x16x32_bf16 v[56:59], v[216:219], v[76:79], v[56:59]\n"
    "s_waitcnt lgkmcnt(4)\n"
    "ds_read_b128 v[216:219], v2 offset:16384\n"
    "buffer_load_dwordx4 v[164:167], v235, s[8:11], s18 offen offset:256 sc1\n"
    "v_mfma_f32_16x16x32_bf16 v[20:23], v[220:223], v[120:123], v[20:23]\n"
    "v_mfma_f32_16x16x32_bf16 v[28:31], v[220:223], v[124:127], v[28:31]\n"
    "s_waitcnt lgkmcnt(4)\n"
    "ds_read_b128 v[220:223], v2 offset:32768\n"
    "v_mfma_f32_16x16x32_bf16 v[36:39], v[224:227], v[96:99], v[36:39]\n"
    "v_mfma_f32_16x16x32_bf16 v[44:47], v[224:227], v[100:103], v[44:47]\n"
    "s_waitcnt vmcnt(11) lgkmcnt(4)\n"
    "ds_read_b128 v[224:227], v2 offset:55296\n"
    "buffer_load_dwordx4 a[240:243], v235, s[8:11], s18 offen offset:2048 sc1\n"
    "v_mfma_f32_16x16x32_bf16 v[16:19], v[228:231], v[128:131], v[16:19]\n"
    "v_mfma_f32_16x16x32_bf16 v[24:27], v[228:231], v[132:135], v[24:27]\n"
    "s_waitcnt lgkmcnt(4)\n"
    "ds_read_b128 v[228:231], v2 offset:17408\n"
    "v_mfma_f32_16x16x32_bf16 v[52:55], v[208:211], v[72:75], v[52:55]\n"
    "v_mfma_f32_16x16x32_bf16 v[60:63], v[208:211], v[76:79], v[60:63]\n"
    "s_waitcnt lgkmcnt(4)\n"
    "ds_read_b128 v[208:211], v2 offset:56320\n"
    "buffer_load_dwordx4 a[244:247], v235, s[8:11], s18 offen offset:2304 sc1\n"
    "v_mfma_f32_16x16x32_bf16 v[20:23], v[212:215], v[128:131], v[20:23]\n"
    "v_mfma_f32_16x16x32_bf16 v[28:31], v[212:215], v[132:135], v[28:31]\n"
    "s_waitcnt lgkmcnt(4)\n"
    "ds_read_b128 v[212:215], v2 offset:33792\n"
    "v_mfma_f32_16x16x32_bf16 v[48:51], v[216:219], v[80:83], v[48:51]\n"
    "v_mfma_f32_16x16x32_bf16 v[56:59], v[216:219], v[84:87], v[56:59]\n"
    "s_waitcnt lgkmcnt(4)\n"
    "ds_read_b128 v[216:219], v2 offset:61440\n"
    "v_mfma_f32_16x16x32_bf16 v[32:35], v[220:223], v[104:107], v[32:35]\n"
    "v_mfma_f32_16x16x32_bf16 v[40:43], v[220:223], v[108:111], v[40:43]\n"
    "s_waitcnt vmcnt(10) lgkmcnt(4)\n"
    "ds_read_b128 v[220:223], v2 offset:22528\n"
    "buffer_load_dwordx4 a[248:251], v235, s[8:11], s19 offen offset:0 sc1\n"
    "v_mfma_f32_16x16x32_bf16 v[16:19], v[224:227], v[136:139], v[16:19]\n"
    "v_mfma_f32_16x16x32_bf16 v[24:27], v[224:227], v[140:143], v[24:27]\n"
    "s_waitcnt lgkmcnt(4)\n"
    "ds_read_b128 v[224:227], v2 offset:62464\n"
    "v_mfma_f32_16x16x32_bf16 v[52:55], v[228:231], v[80:83], v[52:55]\n"
    "v_mfma_f32_16x16x32_bf16 v[60:63], v[228:231], v[84:87], v[60:63]\n"
    "s_waitcnt lgkmcnt(4)\n"
    "ds_read_b128 v[228:231], v2 offset:23552\n"
    "buffer_load_dwordx4 a[252:255], v235, s[8:11], s19 offen offset:256 sc1\n"
    "v_mfma_f32_16x16x32_bf16 v[20:23], v[208:211], v[136:139], v[20:23]\n"
    "v_mfma_f32_16x16x32_bf16 v[28:31], v[208:211], v[140:143], v[28:31]\n"
    "s_waitcnt lgkmcnt(4)\n"
    "ds_read_b128 v[208:211], v2 offset:38912\n"
    "v_mfma_f32_16x16x32_bf16 v[36:39], v[212:215], v[104:107], v[36:39]\n"
    "v_mfma_f32_16x16x32_bf16 v[44:47], v[212:215], v[108:111], v[44:47]\n"
    "s_waitcnt vmcnt(10) lgkmcnt(4)\n"
    "ds_read_b128 v[212:215], v3 offset:2048\n"
    "buffer_load_dwordx4 v[168:171], v235, s[8:11], s19 offen offset:2048 sc1\n"
    "v_mfma_f32_16x16x32_bf16 v[16:19], v[216:219], v[144:147], v[16:19]\n"
    "v_mfma_f32_16x16x32_bf16 v[24:27], v[216:219], v[148:151], v[24:27]\n"
    "s_waitcnt lgkmcnt(4)\n"
    "ds_read_b128 v[216:219], v2 offset:28672\n"
    "v_mfma_f32_16x16x32_bf16 v[48:51], v[220:223], v[88:91], v[48:51]\n"
    "v_add_f32 v240, v192, v196\n"
    "v_add_f32 v241, v193, v197\n"
    "v_mfma_f32_16x16x32_bf16 v[56:59], v[220:223], v[92:95], v[56:59]\n"
    "s_waitcnt lgkmcnt(4)\n"
    "ds_read_b128 v[220:223], v3 offset:3072\n"
    "v_mfma_f32_16x16x32_bf16 v[20:23], v[224:227], v[144:147], v[20:23]\n"
    "v_add_f32 v242, v194, v198\n"
    "v_add_f32 v243, v195, v199\n"
    "v_mfma_f32_16x16x32_bf16 v[28:31], v[224:227], v[148:151], v[28:31]\n"
    "s_waitcnt lgkmcnt(4)\n"
    "ds_read_b128 v[224:227], v2 offset:39936\n"
    "buffer_load_dwordx4 v[172:175], v235, s[8:11], s19 offen offset:2304 sc1\n"
    "v_mfma_f32_16x16x32_bf16 v[52:55], v[228:231], v[88:91], v[52:55]\n"
    "v_add_f32 v240, v240, v200\n"
    "v_add_f32 v241, v241, v201\n"
    "v_mfma_f32_16x16x32_bf16 v[60:63], v[228:231], v[92:95], v[60:63]\n"
    "s_waitcnt lgkmcnt(4)\n"
    "ds_read_b128 v[228:231], v3 offset:8192\n"
    "v_mfma_f32_16x16x32_bf16 v[32:35], v[208:211], v[112:115], v[32:35]\n"
    "v_add_f32 v242, v242, v202\n"
    "v_add_f32 v243, v243, v203\n"
    "v_mfma_f32_16x16x32_bf16 v[40:43], v[208:211], v[116:119], v[40:43]\n"
    "s_waitcnt vmcnt(8) lgkmcnt(4)\n"
    "ds_read_b128 v[208:211], v2 offset:29696\n"
    "buffer_load_dwordx4 v[176:179], v235, s[8:11], s20 offen offset:0 sc1\n"
    "v_mfma_f32_16x16x32_bf16 v[16:19], v[212:215], v[152:155], v[16:19]\n"
    "v_add_f32 v240, v240, v204\n"
    "v_add_f32 v241, v241, v205\n"
    "v_mfma_f32_16x16x32_bf16 v[24:27], v[212:215], v[156:159], v[24:27]\n"
    "s_waitcnt lgkmcnt(4)\n"
    "ds_read_b128 v[212:215], v3 offset:9216\n"
    "v_mfma_f32_16x16x32_bf16 v[48:51], v[216:219], v[96:99], v[48:51]\n"
    "v_add_f32 v242, v242, v206\n"
    "v_add_f32 v243, v243, v207\n"
    "v_mfma_f32_16x16x32_bf16 v[56:59], v[216:219], v[100:103], v[56:59]\n"
    "s_waitcnt lgkmcnt(4)\n"
    "ds_read_b128 v[216:219], v2 offset:34816\n"
    "buffer_load_dwordx4 v[180:183], v235, s[8:11], s20 offen offset:256 sc1\n"
    "v_mfma_f32_16x16x32_bf16 v[20:23], v[220:223], v[152:155], v[20:23]\n"
    "s_mov_b64 exec, 0xff\n"
    "buffer_store_dwordx4 v[252:255], v2, s[8:11], s27 offen sc1\n"
    "s_mov_b64 exec, -1\n"
    "v_mfma_f32_16x16x32_bf16 v[28:31], v[220:223], v[156:159], v[28:31]\n"
    "s_waitcnt lgkmcnt(4)\n"
    "ds_read_b128 v[220:223], v2 offset:45056\n"
    "v_permlane16_swap_b32 v240, v242\n"
    "v_mfma_f32_16x16x32_bf16 v[36:39], v[224:227], v[112:115], v[36:39]\n"
    "v_permlane16_swap_b32 v241, v243\n"
    "v_mfma_f32_16x16x32_bf16 v[44:47], v[224:227], v[116:119], v[44:47]\n"
    "s_waitcnt vmcnt(9) lgkmcnt(4)\n"
    "ds_read_b128 v[224:227], v3 offset:14336\n"
    "v_add_f32 v240, v240, v242\n"
    "v_mfma_f32_16x16x32_bf16 v[16:19], v[228:231], v[160:163], v[16:19]\n"
    "v_add_f32 v241, v241, v243\n"
    "v_mfma_f32_16x16x32_bf16 v[24:27], v[228:231], v[164:167], v[24:27]\n"
    "s_waitcnt lgkmcnt(4)\n"
    "ds_read_b128 v[228:231], v2 offset:35840\n"
    "buffer_load_dwordx4 v[184:187], v235, s[8:11], s20 offen offset:2048 sc1\n"
    "v_mfma_f32_16x16x32_bf16 v[52:55], v[208:211], v[96:99], v[52:55]\n"
    "v_permlane32_swap_b32 v240, v241\n"
    "v_mfma_f32_16x16x32_bf16 v[60:63], v[208:211], v[100:103], v[60:63]\n"
    "s_waitcnt lgkmcnt(4)\n"
    "ds_read_b128 v[208:211], v3 offset:15360\n"
    "v_add_f32 v240, v240, v241\n"
    "v_mfma_f32_16x16x32_bf16 v[20:23], v[212:215], v[160:163], v[20:23]\n"
    "v_mov_b32 v241, v240\n"
    "v_mfma_f32_16x16x32_bf16 v[28:31], v[212:215], v[164:167], v[28:31]\n"
    "s_waitcnt lgkmcnt(4)\n"
    "ds_read_b128 v[212:215], v2 offset:46080\n"
    "buffer_load_dwordx4 v[188:191], v235, s[8:11], s20 offen offset:2304 sc1\n"
    "v_mfma_f32_16x16x32_bf16 v[48:51], v[216:219], v[104:107], v[48:51]\n"
    "v_permlane32_swap_b32 v240, v241\n"
    "v_mfma_f32_16x16x32_bf16 v[56:59], v[216:219], v[108:111], v[56:59]\n"
    "s_waitcnt lgkmcnt(4)\n"
    "ds_read_b128 v[216:219], v3 offset:20480\n"
    "v_mul_f32 v240, 0x3acccccd, v240\n"
    "v_mfma_f32_16x16x32_bf16 v[32:35], v[220:223], v[120:123], v[32:35]\n"
    "v_mul_f32 v241, 0x3acccccd, v241\n"
    "v_mfma_f32_16x16x32_bf16 v[40:43], v[220:223], v[124:127], v[40:43]\n"
    "s_waitcnt vmcnt(9) lgkmcnt(4)\n"
    "ds_read_b128 v[220:223], v2 offset:40960\n"
    "buffer_load_dwordx4 v[192:195], v235, s[8:11], s40 offen offset:0 sc1\n"
    "v_mfma_f32_16x16x32_bf16 v[16:19], v[224:227], a[240:243], v[16:19]\n"
    "v_fma_f32 v241, -v240, v240, v241\n"
    "v_mfma_f32_16x16x32_bf16 v[24:27], v[224:227], a[244:247], v[24:27]\n"
    "s_waitcnt lgkmcnt(4)\n"
    "ds_read_b128 v[224:227], v3 offset:21504\n"
    "v_max_f32 v241, 0, v241\n"
    "v_mfma_f32_16x16x32_bf16 v[52:55], v[228:231], v[104:107], v[52:55]\n"
    "v_cvt_pk_bf16_f32 v241, v241, v241\n"
    "v_mfma_f32_16x16x32_bf16 v[60:63], v[228:231], v[108:111], v[60:63]\n"
    "s_waitcnt lgkmcnt(4)\n"
    "ds_read_b128 v[228:231], v2 offset:41984\n"
    "v_lshlrev_b32 v241, 16, v241\n"
    "v_mfma_f32_16x16x32_bf16 v[20:23], v[208:211], a[240:243], v[20:23]\n"
    "v_add_f32 v241, 0x3727c5ac, v241\n"
    "v_mfma_f32_16x16x32_bf16 v[28:31], v[208:211], a[244:247], v[28:31]\n"
    "s_waitcnt lgkmcnt(4)\n"
    "ds_read_b128 v[208:211], v2 offset:51200\n"
    "buffer_load_dwordx4 v[196:199], v235, s[8:11], s40 offen offset:256 sc1\n"
    "v_mfma_f32_16x16x32_bf16 v[36:39], v[212:215], v[120:123], v[36:39]\n"
    "v_cvt_pk_bf16_f32 v241, v241, v241\n"
    "v_mfma_f32_16x16x32_bf16 v[44:47], v[212:215], v[124:127], v[44:47]\n"
    "s_waitcnt vmcnt(9) lgkmcnt(4)\n"
    "ds_read_b128 v[212:215], v3 offset:26624\n"
    "v_lshlrev_b32 v241, 16, v241\n"
    "v_mfma_f32_16x16x32_bf16 v[16:19], v[216:219], a[248:251], v[16:19]\n"
    "v_sqrt_f32 v241, v241\n"
    "v_mfma_f32_16x16x32_bf16 v[24:27], v[216:219], a[252:255], v[24:27]\n"
    "s_waitcnt lgkmcnt(4)\n"
    "ds_read_b128 v[216:219], v2 offset:47104\n"
    "buffer_load_dwordx4 v[200:203], v235, s[8:11], s40 offen offset:2048 sc1\n"
    "v_mfma_f32_16x16x32_bf16 v[48:51], v[220:223], v[112:115], v[48:51]\n"
    "v_cvt_pk_bf16_f32 v241, v241, v241\n"
    "v_mfma_f32_16x16x32_bf16 v[56:59], v[220:223], v[116:119], v[56:59]\n"
    "s_waitcnt lgkmcnt(4)\n"
    "ds_read_b128 v[220:223], v3 offset:27648\n"
    "v_lshlrev_b32 v241, 16, v241\n"
    "v_mfma_f32_16x16x32_bf16 v[20:23], v[224:227], a[248:251], v[20:23]\n"
    "v_rcp_f32 v244, v241\n"
    "v_mfma_f32_16x16x32_bf16 v[28:31], v[224:227], a[252:255], v[28:31]\n"
    "s_waitcnt lgkmcnt(4)\n"
    "ds_read_b128 v[224:227], v2 offset:52224\n"
    "buffer_load_dwordx4 v[204:207], v235, s[8:11], s40 offen offset:2304 sc1\n"
    "v_mfma_f32_16x16x32_bf16 v[52:55], v[228:231], v[112:115], v[52:55]\n"
    "v_mul_f32 v240, -0.5, v240\n"
    "v_mfma_f32_16x16x32_bf16 v[60:63], v[228:231], v[116:119], v[60:63]\n"
    "s_waitcnt lgkmcnt(4)\n"
    "ds_read_b128 v[228:231], v3 offset:32768\n"
    "v_mul_f32 v245, v241, v241\n"
    "v_mfma_f32_16x16x32_bf16 v[32:35], v[208:211], v[128:131], v[32:35]\n"
    "v_mul_f32 v247, v241, v241\n"
    "v_mfma_f32_16x16x32_bf16 v[40:43], v[208:211], v[132:135], v[40:43]\n"
    "s_waitcnt vmcnt(9) lgkmcnt(4)\n"
    "ds_read_b128 v[208:211], v2 offset:48128\n"
    "v_mov_b32 v248, v240\n"
    "v_mfma_f32_16x16x32_bf16 v[16:19], v[212:215], v[168:171], v[16:19]\n"
    "s_add_u32 s76, s76, 5120\n"
    "buffer_load_dwordx4 a[160:163], v6, s[8:11], s76 offen offset:0 sc1\n"
    "v_mul_f32 v246, 0xbfb8aa3b, v244\n"
    "v_mfma_f32_16x16x32_bf16 v[24:27], v[212:215], v[172:175], v[24:27]\n"
    "s_waitcnt lgkmcnt(4)\n"
    "ds_read_b128 v[212:215], v3 offset:33792\n"
    "v_mul_f32 v244, 0xbfb8aa3b, v244\n"
    "v_mfma_f32_16x16x32_bf16 v[48:51], v[216:219], v[120:123], v[48:51]\n"
    "v_permlane16_swap_b32 v240, v248\n"
    "v_mfma_f32_16x16x32_bf16 v[56:59], v[216:219], v[124:127], v[56:59]\n"
    "s_waitcnt lgkmcnt(4)\n"
    "ds_read_b128 v[216:219], v2 offset:53248\n"
    "v_permlane16_swap_b32 v245, v247\n"
    "v_mfma_f32_16x16x32_bf16 v[20:23], v[220:223], v[168:171], v[20:23]\n"
    "v_permlane16_swap_b32 v244, v246\n"
    "v_mfma_f32_16x16x32_bf16 v[28:31], v[220:223], v[172:175], v[28:31]\n"
    "s_waitcnt lgkmcnt(4)\n"
    "ds_read_b128 v[220:223], v2 offset:57344\n"
    "buffer_load_dwordx4 a[164:167], v6, s[8:11], s76 offen offset:1024 sc1\n"
    "v_mfma_f32_16x16x32_bf16 v[36:39], v[224:227], v[128:131], v[36:39]\n"
    "v_cvt_pk_bf16_f32 v243, v240, v240\n"
    "v_mfma_f32_16x16x32_bf16 v[44:47], v[224:227], v[132:135], v[44:47]\n"
    "s_waitcnt vmcnt(9) lgkmcnt(4)\n"
    "ds_read_b128 v[224:227], v3 offset:38912\n"
    "v_cvt_pk_bf16_f32 v251, v248, v248\n"
    "v_mfma_f32_16x16x32_bf16 v[16:19], v[228:231], v[176:179], v[16:19]\n"
    "v_cvt_pk_bf16_f32 v242, v240, v240\n"
    "v_mfma_f32_16x16x32_bf16 v[24:27], v[228:231], v[180:183], v[24:27]\n"
    "s_waitcnt lgkmcnt(4)\n"
    "ds_read_b128 v[228:231], v2 offset:54272\n"
    "buffer_load_dwordx4 a[168:171], v7, s[8:11], s76 offen offset:0 sc1\n"
    "v_mfma_f32_16x16x32_bf16 v[52:55], v[208:211], v[120:123], v[52:55]\n"
    "v_cvt_pk_bf16_f32 v250, v248, v248\n"
    "v_mfma_f32_16x16x32_bf16 v[60:63], v[208:211], v[124:127], v[60:63]\n"
    "s_waitcnt lgkmcnt(4)\n"
    "ds_read_b128 v[208:211], v3 offset:39936\n"
    "v_cvt_pk_bf16_f32 v241, v240, v240\n"
    "v_mfma_f32_16x16x32_bf16 v[20:23], v[212:215], v[176:179], v[20:23]\n"
    "v_cvt_pk_bf16_f32 v249, v248, v248\n"
    "v_mfma_f32_16x16x32_bf16 v[28:31], v[212:215], v[180:183], v[28:31]\n"
    "s_waitcnt lgkmcnt(4)\n"
    "ds_read_b128 v[212:215], v2 offset:58368\n"
    "buffer_load_dwordx4 a[172:175], v7, s[8:11], s76 offen offset:1024 sc1\n"
    "v_mfma_f32_16x16x32_bf16 v[48:51], v[216:219], v[128:131], v[48:51]\n"
    "v_cvt_pk_bf16_f32 v240, v240, v240\n"
    "v_mfma_f32_16x16x32_bf16 v[56:59], v[216:219], v[132:135], v[56:59]\n"
    "s_waitcnt lgkmcnt(4)\n"
    "ds_read_b128 v[216:219], v3 offset:45056\n"
    "v_cvt_pk_bf16_f32 v248, v248, v248\n"
    "v_mfma_f32_16x16x32_bf16 v[32:35], v[220:223], v[136:139], v[32:35]\n"
    "v_mfma_f32_16x16x32_bf16 v[40:43], v[220:223], v[140:143], v[40:43]\n"
    "s_waitcnt vmcnt(8) lgkmcnt(4)\n"
    "ds_read_b128 v[220:223], v2 offset:59392\n"
    "buffer_load_dwordx4 a[176:179], v15, s[8:11], s76 offen offset:0 sc1\n"
    "v_mfma_f32_16x16x32_bf16 v[16:19], v[224:227], v[184:187], v[16:19]\n"
    "v_mfma_f32_16x16x32_bf16 v[24:27], v[224:227], v[188:191], v[24:27]\n"
    "s_waitcnt lgkmcnt(4)\n"
    "ds_read_b128 v[224:227], v3 offset:46080\n"
    "v_mfma_f32_16x16x32_bf16 v[52:55], v[228:231], v[128:131], v[52:55]\n"
    "v_mfma_f32_16x16x32_bf16 v[60:63], v[228:231], v[132:135], v[60:63]\n"
    "s_waitcnt lgkmcnt(4)\n"
    "ds_read_b128 v[228:231], v2 offset:60416\n"
    "v_mfma_f32_16x16x32_bf16 v[20:23], v[208:211], v[184:187], v[20:23]\n"
    "s_add_u32 s76, s76, 5120\n"
    "buffer_load_dwordx4 a[180:183], v6, s[8:11], s76 offen offset:0 sc1\n"
    "v_mfma_f32_16x16x32_bf16 v[28:31], v[208:211], v[188:191], v[28:31]\n"
    "s_waitcnt lgkmcnt(4)\n"
    "ds_read_b128 v[208:211], v2 offset:63488\n"
    "v_mfma_f32_16x16x32_bf16 v[36:39], v[212:215], v[136:139], v[36:39]\n"
    "v_mfma_f32_16x16x32_bf16 v[44:47], v[212:215], v[140:143], v[44:47]\n"
    "s_waitcnt vmcnt(8) lgkmcnt(4)\n"
    "ds_read_b128 v[212:215], v3 offset:51200\n"
    "v_mfma_f32_16x16x32_bf16 v[16:19], v[216:219], v[192:195], v[16:19]\n"
    "v_mfma_f32_16x16x32_bf16 v[24:27], v[216:219], v[196:199], v[24:27]\n"
    "s_waitcnt lgkmcnt(4)\n"
    "ds_read_b128 v[216:219], v3 offset:0\n"
    "buffer_load_dwordx4 a[184:187], v6, s[8:11], s76 offen offset:1024 sc1\n"
    "v_mfma_f32_16x16x32_bf16 v[48:51], v[220:223], v[136:139], v[48:51]\n"
    "v_mfma_f32_16x16x32_bf16 v[56:59], v[220:223], v[140:143], v[56:59]\n"
    "s_waitcnt lgkmcnt(4)\n"
    "ds_read_b128 v[220:223], v3 offset:52224\n"
    "v_mfma_f32_16x16x32_bf16 v[20:23], v[224:227], v[192:195], v[20:23]\n"
    "v_mfma_f32_16x16x32_bf16 v[28:31], v[224:227], v[196:199], v[28:31]\n"
    "s_waitcnt lgkmcnt(4)\n"
    "ds_read_b128 v[224:227], v2 offset:64512\n"
    "buffer_load_dwordx4 a[188:191], v7, s[8:11], s76 offen offset:0 sc1\n"
    "v_mfma_f32_16x16x32_bf16 v[52:55], v[228:231], v[136:139], v[52:55]\n"
    "v_mfma_f32_16x16x32_bf16 v[60:63], v[228:231], v[140:143], v[60:63]\n"
    "s_waitcnt lgkmcnt(4)\n"
    "ds_read_b128 v[228:231], v3 offset:57472\n"
    "v_mfma_f32_16x16x32_bf16 v[32:35], v[208:211], v[144:147], v[32:35]\n"
    "v_mfma_f32_16x16x32_bf16 v[40:43], v[208:211], v[148:151], v[40:43]\n"
    "s_waitcnt vmcnt(8) lgkmcnt(4)\n"
    "ds_read_b128 v[208:211], v3 offset:1024\n"
    "buffer_load_dwordx4 a[192:195], v7, s[8:11], s76 offen offset:1024 sc1\n"
    "v_mfma_f32_16x16x32_bf16 v[16:19], v[212:215], v[200:203], v[16:19]\n"
    "v_mfma_f32_16x16x32_bf16 v[24:27], v[212:215], v[204:207], v[24:27]\n"
    "s_waitcnt lgkmcnt(4)\n"
    "ds_read_b128 v[212:215], v3 offset:58496\n"
    "v_mfma_f32_16x16x32_bf16 v[48:51], v[216:219], v[144:147], v[48:51]\n"
    "v_mfma_f32_16x16x32_bf16 v[56:59], v[216:219], v[148:151], v[56:59]\n"
    "s_waitcnt lgkmcnt(4)\n"
    "ds_read_b128 v[216:219], v3 offset:6144\n"
    "buffer_load_dwordx4 a[196:199], v15, s[8:11], s76 offen offset:0 sc1\n"
    "v_mfma_f32_16x16x32_bf16 v[20:23], v[220:223], v[200:203], v[20:23]\n"
    "v_mfma_f32_16x16x32_bf16 v[28:31], v[220:223], v[204:207], v[28:31]\n"
    "s_waitcnt lgkmcnt(4)\n"
    "ds_read_b128 v[220:223], v3 offset:4096\n"
    "v_mfma_f32_16x16x32_bf16 v[36:39], v[224:227], v[144:147], v[36:39]\n"
    "v_mfma_f32_16x16x32_bf16 v[44:47], v[224:227], v[148:151], v[44:47]\n"
    "s_waitcnt lgkmcnt(4)\n"
    "ds_read_b128 v[224:227], v3 offset:5120\n"
    "v_mfma_f32_16x16x32_bf16 v[16:19], v[228:231], v[240:243], v[16:19]\n"
    "s_add_u32 s76, s76, 5120\n"
    "buffer_load_dwordx4 a[200:203], v6, s[8:11], s76 offen offset:0 sc1\n"
    "v_mfma_f32_16x16x32_bf16 v[24:27], v[228:231], v[248:251], v[24:27]\n"
    "s_waitcnt lgkmcnt(4)\n"
    "ds_read_b128 v[228:231], v3 offset:10240\n"
    "v_mfma_f32_16x16x32_bf16 v[52:55], v[208:211], v[144:147], v[52:55]\n"
    "v_mfma_f32_16x16x32_bf16 v[60:63], v[208:211], v[148:151], v[60:63]\n"
    "s_waitcnt lgkmcnt(4)\n"
    "ds_read_b128 v[208:211], v3 offset:11264\n"
    "v_mfma_f32_16x16x32_bf16 v[20:23], v[212:215], v[240:243], v[20:23]\n"
    "s_add_u32 s83, s28, 0\n"
    "buffer_load_dwordx2 v[238:239], v4, s[8:11], s83 offen sc1\n"
    "v_mfma_f32_16x16x32_bf16 v[28:31], v[212:215], v[248:251], v[28:31]\n"
    "s_waitcnt lgkmcnt(4)\n"
    "ds_read_b128 v[212:215], v3 offset:16384\n"
    "buffer_load_dwordx4 a[204:207], v6, s[8:11], s76 offen offset:1024 sc1\n"
    "v_mfma_f32_16x16x32_bf16 v[48:51], v[216:219], v[152:155], v[48:51]\n"
    "v_mfma_f32_16x16x32_bf16 v[56:59], v[216:219], v[156:159], v[56:59]\n"
    "s_waitcnt lgkmcnt(4)\n"
    "ds_read_b128 v[216:219], v3 offset:17408\n"
    "v_mul_f32 v20, v16, v20\n"
    "v_mul_f32 v16, v244, v16\n"
    "v_mfma_f32_16x16x32_bf16 v[32:35], v[220:223], v[152:155], v[32:35]\n"
    "v_exp_f32 v16, v16\n"
    "v_mfma_f32_16x16x32_bf16 v[40:43], v[220:223], v[156:159], v[40:43]\n"
    "s_waitcnt lgkmcnt(4)\n"
    "ds_read_b128 v[220:223], v3 offset:22528\n"
    "buffer_load_dwordx4 a[208:211], v7, s[8:11], s76 offen offset:0 sc1\n"
    "v_fma_f32 v16, v16, v245, v245\n"
    "v_mul_f32 v21, v17, v21\n"
    "v_mfma_f32_16x16x32_bf16 v[36:39], v[224:227], v[152:155], v[36:39]\n"
    "v_rcp_f32 v16, v16\n"
    "v_mul_f32 v17, v244, v17\n"
    "v_mfma_f32_16x16x32_bf16 v[44:47], v[224:227], v[156:159], v[44:47]\n"
    "s_waitcnt lgkmcnt(4)\n"
    "ds_read_b128 v[224:227], v3 offset:23552\n"
    "v_mul_f32 v16, v16, v20\n"
    "v_mul_f32 v22, v18, v22\n"
    "v_mfma_f32_16x16x32_bf16 v[32:35], v[228:231], v[160:163], v[32:35]\n"
    "v_exp_f32 v17, v17\n"
    "v_mfma_f32_16x16x32_bf16 v[40:43], v[228:231], v[164:167], v[40:43]\n"
    "s_waitcnt lgkmcnt(4)\n"
    "ds_read_b128 v[228:231], v3 offset:28672\n"
    "buffer_load_dwordx4 a[212:215], v7, s[8:11], s76 offen offset:1024 sc1\n"
    "v_fma_f32 v17, v17, v245, v245\n"
    "v_mul_f32 v18, v244, v18\n"
    "v_mfma_f32_16x16x32_bf16 v[36:39], v[208:211], v[160:163], v[36:39]\n"
    "v_rcp_f32 v17, v17\n"
    "v_mul_f32 v23, v19, v23\n"
    "v_mfma_f32_16x16x32_bf16 v[44:47], v[208:211], v[164:167], v[44:47]\n"
    "s_waitcnt lgkmcnt(4)\n"
    "ds_read_b128 v[208:211], v3 offset:29696\n"
    "v_mul_f32 v17, v17, v21\n"
    "v_mul_f32 v19, v244, v19\n"
    "v_mfma_f32_16x16x32_bf16 v[32:35], v[212:215], a[240:243], v[32:35]\n"
    "v_exp_f32 v18, v18\n"
    "v_mfma_f32_16x16x32_bf16 v[40:43], v[212:215], a[244:247], v[40:43]\n"
    "s_waitcnt lgkmcnt(4)\n"
    "ds_read_b128 v[212:215], v3 offset:34816\n"
    "buffer_load_dwordx4 a[216:219], v15, s[8:11], s76 offen offset:0 sc1\n"
    "v_fma_f32 v18, v18, v245, v245\n"
    "v_mul_f32 v28, v24, v28\n"
    "v_mfma_f32_16x16x32_bf16 v[36:39], v[216:219], a[240:243], v[36:39]\n"
    "v_rcp_f32 v18, v18\n"
    "v_mul_f32 v24, v246, v24\n"
    "v_mfma_f32_16x16x32_bf16 v[44:47], v[216:219], a[244:247], v[44:47]\n"
    "s_waitcnt lgkmcnt(4)\n"
    "ds_read_b128 v[216:219], v3 offset:35840\n"
    "v_mul_f32 v18, v18, v22\n"
    "v_mul_f32 v29, v25, v29\n"
    "v_mfma_f32_16x16x32_bf16 v[32:35], v[220:223], a[248:251], v[32:35]\n"
    "v_exp_f32 v19, v19\n"
    "v_mfma_f32_16x16x32_bf16 v[40:43], v[220:223], a[252:255], v[40:43]\n"
    "s_waitcnt lgkmcnt(4)\n"
    "ds_read_b128 v[220:223], v3 offset:40960\n"
    "v_fma_f32 v19, v19, v245, v245\n"
    "v_mul_f32 v25, v246, v25\n"
    "v_mfma_f32_16x16x32_bf16 v[36:39], v[224:227], a[248:251], v[36:39]\n"
    "s_add_u32 s76, s76, 5120\n"
    "buffer_load_dwordx4 a[220:223], v6, s[8:11], s76 offen offset:0 sc1\n"
    "v_mul_f32 v30, v26, v30\n"
    "v_mul_f32 v26, v246, v26\n"
    "v_mul_f32 v31, v27, v31\n"
    "v_mfma_f32_16x16x32_bf16 v[44:47], v[224:227], a[252:255], v[44:47]\n"
    "s_waitcnt lgkmcnt(4)\n"
    "ds_read_b128 v[224:227], v3 offset:41984\n"
    "v_mul_f32 v27, v246, v27\n"
    "v_cvt_pk_bf16_f32 v20, v16, v17\n"
    "v_mfma_f32_16x16x32_bf16 v[32:35], v[228:231], v[168:171], v[32:35]\n"
    "v_rcp_f32 v19, v19\n"
    "v_mfma_f32_16x16x32_bf16 v[40:43], v[228:231], v[172:175], v[40:43]\n"
    "s_waitcnt lgkmcnt(4)\n"
    "ds_read_b128 v[228:231], v3 offset:47104\n"
    "v_mul_f32 v19, v19, v23\n"
    "v_exp_f32 v24, v24\n"
    "v_mfma_f32_16x16x32_bf16 v[36:39], v[208:211], v[168:171], v[36:39]\n"
    "v_exp_f32 v25, v25\n"
    "v_mfma_f32_16x16x32_bf16 v[44:47], v[208:211], v[172:175], v[44:47]\n"
    "s_waitcnt lgkmcnt(4)\n"
    "ds_read_b128 v[208:211], v3 offset:48128\n"
    "buffer_load_dwordx4 a[224:227], v6, s[8:11], s76 offen offset:1024 sc1\n"
    "v_fma_f32 v24, v24, v247, v247\n"
    "v_fma_f32 v25, v25, v247, v247\n"
    "v_mfma_f32_16x16x32_bf16 v[32:35], v[212:215], v[176:179], v[32:35]\n"
    "v_rcp_f32 v24, v24\n"
    "v_mfma_f32_16x16x32_bf16 v[40:43], v[212:215], v[180:183], v[40:43]\n"
    "s_waitcnt lgkmcnt(4)\n"
    "ds_read_b128 v[212:215], v3 offset:53248\n"
    "v_mul_f32 v24, v24, v28\n"
    "v_cvt_pk_bf16_f32 v21, v18, v19\n"
    "v_mfma_f32_16x16x32_bf16 v[36:39], v[216:219], v[176:179], v[36:39]\n"
    "v_rcp_f32 v25, v25\n"
    "v_exp_f32 v26, v26\n"
    "v_mfma_f32_16x16x32_bf16 v[44:47], v[216:219], v[180:183], v[44:47]\n"
    "s_waitcnt lgkmcnt(4)\n"
    "ds_read_b128 v[216:219], v3 offset:54272\n"
    "buffer_load_dwordx4 a[228:231], v7, s[8:11], s76 offen offset:0 sc1\n"
    "v_mul_f32 v25, v25, v29\n"
    "v_mfma_f32_16x16x32_bf16 v[32:35], v[220:223], v[184:187], v[32:35]\n"
    "v_exp_f32 v27, v27\n"
    "v_mfma_f32_16x16x32_bf16 v[40:43], v[220:223], v[188:191], v[40:43]\n"
    "s_waitcnt lgkmcnt(4)\n"
    "ds_read_b128 v[220:223], v3 offset:59520\n"
    "v_fma_f32 v26, v26, v247, v247\n"
    "v_fma_f32 v27, v27, v247, v247\n"
    "v_mfma_f32_16x16x32_bf16 v[36:39], v[224:227], v[184:187], v[36:39]\n"
    "v_rcp_f32 v26, v26\n"
    "v_cvt_pk_bf16_f32 v22, v24, v25\n"
    "v_mfma_f32_16x16x32_bf16 v[44:47], v[224:227], v[188:191], v[44:47]\n"
    "s_waitcnt lgkmcnt(4)\n"
    "ds_read_b128 v[224:227], v3 offset:60544\n"
    "buffer_load_dwordx4 a[232:235], v7, s[8:11], s76 offen offset:1024 sc1\n"
    "v_mul_f32 v26, v26, v30\n"
    "v_rcp_f32 v27, v27\n"
    "v_mfma_f32_16x16x32_bf16 v[32:35], v[228:231], v[192:195], v[32:35]\n"
    "s_waitcnt vmcnt(8)\n"
    "v_cmp_eq_u64 s[86:87], s[50:51], v[238:239]\n"
    "v_mul_f32 v27, v27, v31\n"
    "v_mfma_f32_16x16x32_bf16 v[40:43], v[228:231], v[196:199], v[40:43]\n"
    "s_waitcnt lgkmcnt(4)\n"
    "ds_read_b128 v[228:231], v3 offset:7168\n"
    "v_cvt_pk_bf16_f32 v23, v26, v27\n"
    "v_permlane16_swap_b32 v20, v22\n"
    "v_mfma_f32_16x16x32_bf16 v[36:39], v[208:211], v[192:195], v[36:39]\n"
    "s_andn2_b32 s79, 0xff, s86\n"
    "s_cbranch_scc1 .Lf_slow26\n"
    ".Lf_back27:\n"
    "v_permlane16_swap_b32 v21, v23\n"
    "v_mfma_f32_16x16x32_bf16 v[44:47], v[208:211], v[196:199], v[44:47]\n"
    "s_waitcnt lgkmcnt(4)\n"
    "ds_read_b128 v[208:211], v3 offset:12288\n"
    "buffer_load_dwordx4 a[236:239], v15, s[8:11], s76 offen offset:0 sc1\n"
    "v_mfma_f32_16x16x32_bf16 v[32:35], v[212:215], v[200:203], v[32:35]\n"
    "buffer_load_dwordx4 v[64:67], v236, s[8:11], s41 offen offset:0 sc1\n"
    "v_mfma_f32_16x16x32_bf16 v[40:43], v[212:215], v[204:207], v[40:43]\n"
    "s_waitcnt lgkmcnt(4)\n"
    "ds_read_b128 v[212:215], v3 offset:13312\n"
    "s_add_u32 s67, s24, 3145728\n"
    "buffer_store_dwordx4 v[20:23], v10, s[8:11], s67 offen sc1\n"
    "v_mfma_f32_16x16x32_bf16 v[36:39], v[216:219], v[200:203], v[36:39]\n"
    "v_mfma_f32_16x16x32_bf16 v[44:47], v[216:219], v[204:207], v[44:47]\n"
    "s_waitcnt lgkmcnt(4)\n"
    "ds_read_b128 v[216:219], v3 offset:18432\n"
    "v_mfma_f32_16x16x32_bf16 v[32:35], v[220:223], v[240:243], v[32:35]\n"
    "v_mfma_f32_16x16x32_bf16 v[40:43], v[220:223], v[248:251], v[40:43]\n"
    "s_waitcnt lgkmcnt(4)\n"
    "ds_read_b128 v[220:223], v3 offset:19456\n"
    "buffer_load_dwordx4 v[68:71], v237, s[8:11], s41 offen offset:0 sc1\n"
    "v_mfma_f32_16x16x32_bf16 v[36:39], v[224:227], v[240:243], v[36:39]\n"
    "v_mfma_f32_16x16x32_bf16 v[44:47], v[224:227], v[248:251], v[44:47]\n"
    "s_waitcnt lgkmcnt(4)\n"
    "ds_read_b128 v[224:227], v3 offset:24576\n"
    "v_mfma_f32_16x16x32_bf16 v[52:55], v[228:231], v[152:155], v[52:55]\n"
    "v_mfma_f32_16x16x32_bf16 v[60:63], v[228:231], v[156:159], v[60:63]\n"
    "s_waitcnt lgkmcnt(4)\n"
    "ds_read_b128 v[228:231], v3 offset:25600\n"
    "buffer_load_dwordx4 v[72:75], v236, s[8:11], s41 offen offset:2048 sc1\n"
    "v_mul_f32 v36, v32, v36\n"
    "v_mul_f32 v32, v244, v32\n"
    "v_mfma_f32_16x16x32_bf16 v[48:51], v[208:211], v[160:163], v[48:51]\n"
    "v_exp_f32 v32, v32\n"
    "v_mfma_f32_16x16x32_bf16 v[56:59], v[208:211], v[164:167], v[56:59]\n"
    "s_waitcnt lgkmcnt(4)\n"
    "ds_read_b128 v[208:211], v3 offset:30720\n"
    "v_fma_f32 v32, v32, v245, v245\n"
    "v_mul_f32 v37, v33, v37\n"
    "v_mfma_f32_16x16x32_bf16 v[52:55], v[212:215], v[160:163], v[52:55]\n"
    "v_rcp_f32 v32, v32\n"
    "v_mul_f32 v33, v244, v33\n"
    "v_mfma_f32_16x16x32_bf16 v[60:63], v[212:215], v[164:167], v[60:63]\n"
    "s_waitcnt lgkmcnt(4)\n"
    "ds_read_b128 v[212:215], v3 offset:31744\n"
    "buffer_load_dwordx4 v[76:79], v237, s[8:11], s41 offen offset:2048 sc1\n"
    "v_mul_f32 v32, v32, v36\n"
    "v_mul_f32 v38, v34, v38\n"
    "v_mfma_f32_16x16x32_bf16 v[48:51], v[216:219], a[240:243], v[48:51]\n"
    "v_exp_f32 v33, v33\n"
    "v_mfma_f32_16x16x32_bf16 v[56:59], v[216:219], a[244:247], v[56:59]\n"
    "s_waitcnt lgkmcnt(4)\n"
    "ds_read_b128 v[216:219], v3 offset:36864\n"
    "v_fma_f32 v33, v33, v245, v245\n"
    "v_mul_f32 v34, v244, v34\n"
    "v_mfma_f32_16x16x32_bf16 v[52:55], v[220:223], a[240:243], v[52:55]\n"
    "v_rcp_f32 v33, v33\n"
    "v_mul_f32 v39, v35, v39\n"
    "v_mfma_f32_16x16x32_bf16 v[60:63], v[220:223], a[244:247], v[60:63]\n"
    "s_waitcnt lgkmcnt(4)\n"
    "ds_read_b128 v[220:223], v3 offset:37888\n"
    "buffer_load_dwordx4 v[80:83], v236, s[8:11], s42 offen offset:0 sc1\n"
    "v_mul_f32 v33, v33, v37\n"
    "v_mul_f32 v35, v244, v35\n"
    "v_mfma_f32_16x16x32_bf16 v[48:51], v[224:227], a[248:251], v[48:51]\n"
    "v_exp_f32 v34, v34\n"
    "v_mfma_f32_16x16x32_bf16 v[56:59], v[224:227], a[252:255], v[56:59]\n"
    "s_waitcnt lgkmcnt(4)\n"
    "ds_read_b128 v[224:227], v3 offset:43008\n"
    "v_fma_f32 v34, v34, v245, v245\n"
    "v_mul_f32 v44, v40, v44\n"
    "v_mfma_f32_16x16x32_bf16 v[52:55], v[228:231], a[248:251], v[52:55]\n"
    "v_rcp_f32 v34, v34\n"
    "v_mul_f32 v40, v246, v40\n"
    "v_mfma_f32_16x16x32_bf16 v[60:63], v[228:231], a[252:255], v[60:63]\n"
    "s_waitcnt lgkmcnt(4)\n"
    "ds_read_b128 v[228:231], v3 offset:44032\n"
    "buffer_load_dwordx4 v[84:87], v237, s[8:11], s42 offen offset:0 sc1\n"
    "v_mul_f32 v34, v34, v38\n"
    "v_mul_f32 v45, v41, v45\n"
    "v_mfma_f32_16x16x32_bf16 v[48:51], v[208:211], v[168:171], v[48:51]\n"
    "v_exp_f32 v35, v35\n"
    "v_mfma_f32_16x16x32_bf16 v[56:59], v[208:211], v[172:175], v[56:59]\n"
    "s_waitcnt lgkmcnt(4)\n"
    "ds_read_b128 v[208:211], v3 offset:49152\n"
    "v_fma_f32 v35, v35, v245, v245\n"
    "v_mul_f32 v41, v246, v41\n"
    "v_mfma_f32_16x16x32_bf16 v[52:55], v[212:215], v[168:171], v[52:55]\n"
    "v_rcp_f32 v35, v35\n"
    "v_mul_f32 v46, v42, v46\n"
    "v_mfma_f32_16x16x32_bf16 v[60:63], v[212:215], v[172:175], v[60:63]\n"
    "s_waitcnt lgkmcnt(4)\n"
    "ds_read_b128 v[212:215], v3 offset:50176\n"
    "buffer_load_dwordx4 v[88:91], v236, s[8:11], s42 offen offset:2048 sc1\n"
    "v_mul_f32 v35, v35, v39\n"
    "v_mul_f32 v42, v246, v42\n"
    "v_mfma_f32_16x16x32_bf16 v[48:51], v[216:219], v[176:179], v[48:51]\n"
    "v_exp_f32 v40, v40\n"
    "v_mfma_f32_16x16x32_bf16 v[56:59], v[216:219], v[180:183], v[56:59]\n"
    "s_waitcnt lgkmcnt(4)\n"
    "ds_read_b128 v[216:219], v3 offset:55296\n"
    "v_fma_f32 v40, v40, v247, v247\n"
    "v_mul_f32 v47, v43, v47\n"
    "v_mfma_f32_16x16x32_bf16 v[52:55], v[220:223], v[176:179], v[52:55]\n"
    "v_rcp_f32 v40, v40\n"
    "v_mul_f32 v43, v246, v43\n"
    "v_mfma_f32_16x16x32_bf16 v[60:63], v[220:223], v[180:183], v[60:63]\n"
    "s_waitcnt lgkmcnt(4)\n"
    "ds_read_b128 v[220:223], v3 offset:56320\n"
    "buffer_load_dwordx4 v[92:95], v237, s[8:11], s42 offen offset:2048 sc1\n"
    "v_mul_f32 v40, v40, v44\n"
    "v_cvt_pk_bf16_f32 v36, v32, v33\n"
    "v_mfma_f32_16x16x32_bf16 v[48:51], v[224:227], v[184:187], v[48:51]\n"
    "v_exp_f32 v41, v41\n"
    "v_mfma_f32_16x16x32_bf16 v[56:59], v[224:227], v[188:191], v[56:59]\n"
    "s_waitcnt lgkmcnt(4)\n"
    "ds_read_b128 v[224:227], v3 offset:61568\n"
    "v_fma_f32 v41, v41, v247, v247\n"
    "v_cvt_pk_bf16_f32 v37, v34, v35\n"
    "v_mfma_f32_16x16x32_bf16 v[52:55], v[228:231], v[184:187], v[52:55]\n"
    "v_rcp_f32 v41, v41\n"
    "v_exp_f32 v42, v42\n"
    "v_mfma_f32_16x16x32_bf16 v[60:63], v[228:231], v[188:191], v[60:63]\n"
    "s_waitcnt lgkmcnt(4)\n"
    "ds_read_b128 v[228:231], v3 offset:62592\n"
    "buffer_load_dwordx4 v[96:99], v236, s[8:11], s43 offen offset:0 sc1\n"
    "v_mul_f32 v41, v41, v45\n"
    "v_mfma_f32_16x16x32_bf16 v[48:51], v[208:211], v[192:195], v[48:51]\n"
    "v_exp_f32 v43, v43\n"
    "v_mfma_f32_16x16x32_bf16 v[56:59], v[208:211], v[196:199], v[56:59]\n"
    "s_waitcnt lgkmcnt(4)\n"
    "v_fma_f32 v42, v42, v247, v247\n"
    "v_fma_f32 v43, v43, v247, v247\n"
    "v_mfma_f32_16x16x32_bf16 v[52:55], v[212:215], v[192:195], v[52:55]\n"
    "v_rcp_f32 v42, v42\n"
    "v_cvt_pk_bf16_f32 v38, v40, v41\n"
    "v_mfma_f32_16x16x32_bf16 v[60:63], v[212:215], v[196:199], v[60:63]\n"
    "s_waitcnt lgkmcnt(3)\n"
    "buffer_load_dwordx4 v[100:103], v237, s[8:11], s43 offen offset:0 sc1\n"
    "v_mul_f32 v42, v42, v46\n"
    "v_rcp_f32 v43, v43\n"
    "v_mfma_f32_16x16x32_bf16 v[48:51], v[216:219], v[200:203], v[48:51]\n"
    "v_permlane16_swap_b32 v36, v38\n"
    "v_mfma_f32_16x16x32_bf16 v[56:59], v[216:219], v[204:207], v[56:59]\n"
    "s_waitcnt lgkmcnt(2)\n"
    "v_mul_f32 v43, v43, v47\n"
    "v_mfma_f32_16x16x32_bf16 v[52:55], v[220:223], v[200:203], v[52:55]\n"
    "v_cvt_pk_bf16_f32 v39, v42, v43\n"
    "v_mfma_f32_16x16x32_bf16 v[60:63], v[220:223], v[204:207], v[60:63]\n"
    "s_waitcnt lgkmcnt(1)\n"
    "buffer_load_dwordx4 v[104:107], v236, s[8:11], s43 offen offset:2048 sc1\n"
    "v_permlane16_swap_b32 v37, v39\n"
    "v_mfma_f32_16x16x32_bf16 v[48:51], v[224:227], v[240:243], v[48:51]\n"
    "s_add_u32 s68, s25, 3145728\n"
    "buffer_store_dwordx4 v[36:39], v10, s[8:11], s68 offen sc1\n"
    "v_mfma_f32_16x16x32_bf16 v[56:59], v[224:227], v[248:251], v[56:59]\n"
    "s_waitcnt lgkmcnt(0)\n"
    "v_mfma_f32_16x16x32_bf16 v[52:55], v[228:231], v[240:243], v[52:55]\n"
    "s_add_u32 s73, s27, 262272\n"
    "s_waitcnt vmcnt(11)\n"
    "s_mov_b64 exec, 0xff\n"
    "buffer_store_dwordx4 v[252:255], v2, s[8:11], s73 offen sc1\n"
    "s_mov_b64 exec, -1\n"
    "v_mfma_f32_16x16x32_bf16 v[60:63], v[228:231], v[248:251], v[60:63]\n"
    "buffer_load_dwordx4 v[108:111], v237, s[8:11], s43 offen offset:2048 sc1\n"
    "v_mfma_f32_16x16x32_bf16 v[16:19], a[0:3], v[64:67], 0\n"
    "v_mfma_f32_16x16x32_bf16 v[20:23], a[4:7], v[64:67], 0\n"
    "v_mfma_f32_16x16x32_bf16 v[24:27], a[8:11], v[64:67], 0\n"
    "v_pk_mul_f32 v[52:53], v[48:49], v[52:53]\n"
    "v_pk_mul_f32 v[48:49], v[48:49], v[244:245] op_sel_hi:[1,0]\n"
    "v_pk_mul_f32 v[54:55], v[50:51], v[54:55]\n"
    "v_pk_mul_f32 v[50:51], v[50:51], v[244:245] op_sel_hi:[1,0]\n"
    "v_pk_mul_f32 v[60:61], v[56:57], v[60:61]\n"
    "v_pk_mul_f32 v[56:57], v[56:57], v[246:247] op_sel_hi:[1,0]\n"
    "v_pk_mul_f32 v[62:63], v[58:59], v[62:63]\n"
    "v_pk_mul_f32 v[58:59], v[58:59], v[246:247] op_sel_hi:[1,0]\n"
    "v_exp_f32 v48, v48\n"
    "v_exp_f32 v49, v49\n"
    "v_exp_f32 v50, v50\n"
    "v_exp_f32 v51, v51\n"
    "v_exp_f32 v56, v56\n"
    "v_exp_f32 v57, v57\n"
    "v_exp_f32 v58, v58\n"
    "v_exp_f32 v59, v59\n"
    "v_pk_fma_f32 v[48:49], v[48:49], v[244:245], v[244:245] op_sel:[0,1,1] op_sel_hi:[1,1,1]\n"
    "v_pk_fma_f32 v[50:51], v[50:51], v[244:245], v[244:245] op_sel:[0,1,1] op_sel_hi:[1,1,1]\n"
    "v_pk_fma_f32 v[56:57], v[56:57], v[246:247], v[246:247] op_sel:[0,1,1] op_sel_hi:[1,1,1]\n"
    "v_pk_fma_f32 v[58:59], v[58:59], v[246:247], v[246:247] op_sel:[0,1,1] op_sel_hi:[1,1,1]\n"
    "v_rcp_f32 v48, v48\n"
    "v_rcp_f32 v49, v49\n"
    "v_rcp_f32 v50, v50\n"
    "v_rcp_f32 v51, v51\n"
    "v_rcp_f32 v56, v56\n"
    "v_rcp_f32 v57, v57\n"
    "v_rcp_f32 v58, v58\n"
    "v_rcp_f32 v59, v59\n"
    "v_pk_mul_f32 v[48:49], v[48:49], v[52:53]\n"
    "v_pk_mul_f32 v[50:51], v[50:51], v[54:55]\n"
    "v_pk_mul_f32 v[56:57], v[56:57], v[60:61]\n"
    "v_pk_mul_f32 v[58:59], v[58:59], v[62:63]\n"
    "v_cvt_pk_bf16_f32 v52, v48, v49\n"
    "v_cvt_pk_bf16_f32 v54, v56, v57\n"
    "v_cvt_pk_bf16_f32 v53, v50, v51\n"
    "v_cvt_pk_bf16_f32 v55, v58, v59\n"
    "v_permlane16_swap_b32 v52, v54\n"
    "s_nop 0\n"
    "v_permlane16_swap_b32 v53, v55\n"
    "s_add_u32 s69, s26, 3145728\n"
    "buffer_store_dwordx4 v[52:55], v10, s[8:11], s69 offen sc1\n"
    "v_mfma_f32_16x16x32_bf16 v[28:31], a[12:15], v[64:67], 0\n"
    "buffer_load_dwordx4 v[112:115], v236, s[8:11], s48 offen offset:0 sc1\n"
    "v_mfma_f32_16x16x32_bf16 v[208:211], a[16:19], v[64:67], 0\n"
    "s_waitcnt vmcnt(14)\n"
    "v_mfma_f32_16x16x32_bf16 v[212:215], a[0:3], v[68:71], 0\n"
    "v_mfma_f32_16x16x32_bf16 v[216:219], a[4:7], v[68:71], 0\n"
    "v_mfma_f32_16x16x32_bf16 v[220:223], a[8:11], v[68:71], 0\n"
    "buffer_load_dwordx4 v[116:119], v237, s[8:11], s48 offen offset:0 sc1\n"
    "v_mfma_f32_16x16x32_bf16 v[224:227], a[12:15], v[68:71], 0\n"
    "v_mfma_f32_16x16x32_bf16 v[228:231], a[16:19], v[68:71], 0\n"
    "s_waitcnt vmcnt(14)\n"
    "v_mfma_f32_16x16x32_bf16 v[16:19], a[20:23], v[72:75], v[16:19]\n"
    "v_mfma_f32_16x16x32_bf16 v[20:23], a[24:27], v[72:75], v[20:23]\n"
    "buffer_load_dwordx4 v[120:123], v236, s[8:11], s48 offen offset:2048 sc1\n"
    "v_mfma_f32_16x16x32_bf16 v[24:27], a[28:31], v[72:75], v[24:27]\n"
    "v_mfma_f32_16x16x32_bf16 v[28:31], a[32:35], v[72:75], v[28:31]\n"
    "v_mfma_f32_16x16x32_bf16 v[208:211], a[36:39], v[72:75], v[208:211]\n"
    "s_waitcnt vmcnt(14)\n"
    "v_mfma_f32_16x16x32_bf16 v[212:215], a[20:23], v[76:79], v[212:215]\n"
    "buffer_load_dwordx4 v[124:127], v237, s[8:11], s48 offen offset:2048 sc1\n"
    "v_mfma_f32_16x16x32_bf16 v[216:219], a[24:27], v[76:79], v[216:219]\n"
    "buffer_load_dwordx4 v[160:163], v12, s[8:11], s30 offen sc1\n"
    "v_mfma_f32_16x16x32_bf16 v[220:223], a[28:31], v[76:79], v[220:223]\n"
    "v_mfma_f32_16x16x32_bf16 v[224:227], a[32:35], v[76:79], v[224:227]\n"
    "v_mfma_f32_16x16x32_bf16 v[228:231], a[36:39], v[76:79], v[228:231]\n"
    "buffer_load_dwordx4 v[128:131], v236, s[8:11], s49 offen offset:0 sc1\n"
    "s_waitcnt vmcnt(16)\n"
    "v_mfma_f32_16x16x32_bf16 v[16:19], a[40:43], v[80:83], v[16:19]\n"
    "buffer_load_dword v173, v13, s[8:11], s31 offen sc1\n"
    "v_mfma_f32_16x16x32_bf16 v[20:23], a[44:47], v[80:83], v[20:23]\n"
    "v_mfma_f32_16x16x32_bf16 v[24:27], a[48:51], v[80:83], v[24:27]\n"
    "v_mfma_f32_16x16x32_bf16 v[28:31], a[52:55], v[80:83], v[28:31]\n"
    "buffer_load_dwordx4 v[132:135], v237, s[8:11], s49 offen offset:0 sc1\n"
    "v_mfma_f32_16x16x32_bf16 v[208:211], a[56:59], v[80:83], v[208:211]\n"
    "buffer_load_dwordx4 v[164:167], v14, s[8:11], s32 offen sc1\n"
    "s_waitcnt vmcnt(18)\n"
    "v_mfma_f32_16x16x32_bf16 v[212:215], a[40:43], v[84:87], v[212:215]\n"
    "v_mfma_f32_16x16x32_bf16 v[216:219], a[44:47], v[84:87], v[216:219]\n"
    "v_mfma_f32_16x16x32_bf16 v[220:223], a[48:51], v[84:87], v[220:223]\n"
    "buffer_load_dwordx4 v[136:139], v236, s[8:11], s49 offen offset:2048 sc1\n"
    "v_mfma_f32_16x16x32_bf16 v[224:227], a[52:55], v[84:87], v[224:227]\n"
    "s_add_u32 s70, s32, 32768\n"
    "buffer_load_dwordx4 v[168:171], v14, s[8:11], s70 offen sc1\n"
    "v_mfma_f32_16x16x32_bf16 v[228:231], a[56:59], v[84:87], v[228:231]\n"
    "s_waitcnt vmcnt(19)\n"
    "v_mfma_f32_16x16x32_bf16 v[16:19], a[60:63], v[88:91], v[16:19]\n"
    "v_mfma_f32_16x16x32_bf16 v[20:23], a[64:67], v[88:91], v[20:23]\n"
    "buffer_load_dwordx4 v[140:143], v237, s[8:11], s49 offen offset:2048 sc1\n"
    "v_mfma_f32_16x16x32_bf16 v[24:27], a[68:71], v[88:91], v[24:27]\n"
    "buffer_load_dword v172, v5, s[8:11], s33 offen sc1\n"
    "v_mfma_f32_16x16x32_bf16 v[28:31], a[72:75], v[88:91], v[28:31]\n"
    "v_mfma_f32_16x16x32_bf16 v[208:211], a[76:79], v[88:91], v[208:211]\n"
    "s_waitcnt vmcnt(20)\n"
    "v_mfma_f32_16x16x32_bf16 v[212:215], a[60:63], v[92:95], v[212:215]\n"
    "buffer_load_dwordx4 v[144:147], v236, s[8:11], s85 offen offset:0 sc1\n"
    "v_mfma_f32_16x16x32_bf16 v[216:219], a[64:67], v[92:95], v[216:219]\n"
    "v_mfma_f32_16x16x32_bf16 v[220:223], a[68:71], v[92:95], v[220:223]\n"
    "v_mfma_f32_16x16x32_bf16 v[224:227], a[72:75], v[92:95], v[224:227]\n"
    "v_mfma_f32_16x16x32_bf16 v[228:231], a[76:79], v[92:95], v[228:231]\n"
    "buffer_load_dwordx4 v[148:151], v237, s[8:11], s85 offen offset:0 sc1\n"
    "s_waitcnt vmcnt(21)\n"
    "v_mfma_f32_16x16x32_bf16 v[16:19], a[80:83], v[96:99], v[16:19]\n"
    "v_mfma_f32_16x16x32_bf16 v[20:23], a[84:87], v[96:99], v[20:23]\n"
    "v_mfma_f32_16x16x32_bf16 v[24:27], a[88:91], v[96:99], v[24:27]\n"
    "v_mfma_f32_16x16x32_bf16 v[28:31], a[92:95], v[96:99], v[28:31]\n"
    "buffer_load_dwordx4 v[152:155], v236, s[8:11], s85 offen offset:2048 sc1\n"
    "v_mfma_f32_16x16x32_bf16 v[208:211], a[96:99], v[96:99], v[208:211]\n"
    "s_waitcnt vmcnt(21)\n"
    "v_mfma_f32_16x16x32_bf16 v[212:215], a[80:83], v[100:103], v[212:215]\n"
    "v_mfma_f32_16x16x32_bf16 v[216:219], a[84:87], v[100:103], v[216:219]\n"
    "v_mfma_f32_16x16x32_bf16 v[220:223], a[88:91], v[100:103], v[220:223]\n"
    "buffer_load_dwordx4 v[156:159], v237, s[8:11], s85 offen offset:2048 sc1\n"
    "v_mfma_f32_16x16x32_bf16 v[224:227], a[92:95], v[100:103], v[224:227]\n"
    "s_add_u32 s71, s29, 3145728\n"
    "v_add_u32 v236, s71, v2\n"
    "v_mfma_f32_16x16x32_bf16 v[228:231], a[96:99], v[100:103], v[228:231]\n"
    "s_waitcnt vmcnt(21)\n"
    "v_xor_b32 v237, 0x400, v236\n"
    "v_mfma_f32_16x16x32_bf16 v[16:19], a[100:103], v[104:107], v[16:19]\n"
    "v_mfma_f32_16x16x32_bf16 v[20:23], a[104:107], v[104:107], v[20:23]\n"
    "v_mfma_f32_16x16x32_bf16 v[24:27], a[108:111], v[104:107], v[24:27]\n"
    "v_mfma_f32_16x16x32_bf16 v[28:31], a[112:115], v[104:107], v[28:31]\n"
    "v_mfma_f32_16x16x32_bf16 v[208:211], a[116:119], v[104:107], v[208:211]\n"
    "s_waitcnt vmcnt(18)\n"
    "v_mfma_f32_16x16x32_bf16 v[212:215], a[100:103], v[108:111], v[212:215]\n"
    "v_mfma_f32_16x16x32_bf16 v[216:219], a[104:107], v[108:111], v[216:219]\n"
    "s_add_u32 s83, s28, 262272\n"
    "buffer_load_dwordx2 v[238:239], v4, s[8:11], s83 offen sc1\n"
    "v_mfma_f32_16x16x32_bf16 v[220:223], a[108:111], v[108:111], v[220:223]\n"
    "v_mfma_f32_16x16x32_bf16 v[224:227], a[112:115], v[108:111], v[224:227]\n"
    "v_mfma_f32_16x16x32_bf16 v[228:231], a[116:119], v[108:111], v[228:231]\n"
    "s_waitcnt vmcnt(17)\n"
    "v_mfma_f32_16x16x32_bf16 v[16:19], a[120:123], v[112:115], v[16:19]\n"
    "v_mfma_f32_16x16x32_bf16 v[20:23], a[124:127], v[112:115], v[20:23]\n"
    "v_mfma_f32_16x16x32_bf16 v[24:27], a[128:131], v[112:115], v[24:27]\n"
    "s_add_u32 s88, s27, 262144\n"
    "s_mov_b64 exec, 0xff\n"
    "buffer_store_dwordx4 v[252:255], v2, s[8:11], s88 offen sc1\n"
    "s_mov_b64 exec, -1\n"
    "v_mfma_f32_16x16x32_bf16 v[28:31], a[132:135], v[112:115], v[28:31]\n"
    "v_mfma_f32_16x16x32_bf16 v[208:211], a[136:139], v[112:115], v[208:211]\n"
    "s_waitcnt vmcnt(17)\n"
    "v_mfma_f32_16x16x32_bf16 v[212:215], a[120:123], v[116:119], v[212:215]\n"
    "v_mfma_f32_16x16x32_bf16 v[216:219], a[124:127], v[116:119], v[216:219]\n"
    "v_mfma_f32_16x16x32_bf16 v[220:223], a[128:131], v[116:119], v[220:223]\n"
    "v_mfma_f32_16x16x32_bf16 v[224:227], a[132:135], v[116:119], v[224:227]\n"
    "v_mfma_f32_16x16x32_bf16 v[228:231], a[136:139], v[116:119], v[228:231]\n"
    "s_waitcnt vmcnt(16)\n"
    "v_mfma_f32_16x16x32_bf16 v[16:19], a[140:143], v[120:123], v[16:19]\n"
    "v_mfma_f32_16x16x32_bf16 v[20:23], a[144:147], v[120:123], v[20:23]\n"
    "v_mfma_f32_16x16x32_bf16 v[24:27], a[148:151], v[120:123], v[24:27]\n"
    "v_mfma_f32_16x16x32_bf16 v[28:31], a[152:155], v[120:123], v[28:31]\n"
    "v_mfma_f32_16x16x32_bf16 v[208:211], a[156:159], v[120:123], v[208:211]\n"
    "s_waitcnt vmcnt(15)\n"
    "v_mfma_f32_16x16x32_bf16 v[212:215], a[140:143], v[124:127], v[212:215]\n"
    "v_mfma_f32_16x16x32_bf16 v[216:219], a[144:147], v[124:127], v[216:219]\n"
    "v_mfma_f32_16x16x32_bf16 v[220:223], a[148:151], v[124:127], v[220:223]\n"
    "v_mfma_f32_16x16x32_bf16 v[224:227], a[152:155], v[124:127], v[224:227]\n"
    "v_mfma_f32_16x16x32_bf16 v[228:231], a[156:159], v[124:127], v[228:231]\n"
    "s_waitcnt vmcnt(13)\n"
    "v_mfma_f32_16x16x32_bf16 v[16:19], a[160:163], v[128:131], v[16:19]\n"
    "v_mfma_f32_16x16x32_bf16 v[20:23], a[164:167], v[128:131], v[20:23]\n"
    "v_mfma_f32_16x16x32_bf16 v[24:27], a[168:171], v[128:131], v[24:27]\n"
    "v_lshlrev_b32 v174, 16, v160\n"
    "v_and_b32 v175, 0xffff0000, v160\n"
    "v_mfma_f32_16x16x32_bf16 v[28:31], a[172:175], v[128:131], v[28:31]\n"
    "v_lshlrev_b32 v176, 16, v161\n"
    "v_and_b32 v177, 0xffff0000, v161\n"
    "v_mfma_f32_16x16x32_bf16 v[208:211], a[176:179], v[128:131], v[208:211]\n"
    "s_waitcnt vmcnt(11)\n"
    "v_lshlrev_b32 v178, 16, v162\n"
    "v_mfma_f32_16x16x32_bf16 v[212:215], a[160:163], v[132:135], v[212:215]\n"
    "v_and_b32 v179, 0xffff0000, v162\n"
    "v_lshlrev_b32 v180, 16, v163\n"
    "v_mfma_f32_16x16x32_bf16 v[216:219], a[164:167], v[132:135], v[216:219]\n"
    "v_and_b32 v181, 0xffff0000, v163\n"
    "v_lshlrev_b32 v182, 16, v173\n"
    "v_mfma_f32_16x16x32_bf16 v[220:223], a[168:171], v[132:135], v[220:223]\n"
    "v_and_b32 v183, 0xffff0000, v173\n"
    "s_waitcnt vmcnt(10)\n"
    "v_and_b32 v165, 0xffff0000, v164\n"
    "v_mfma_f32_16x16x32_bf16 v[224:227], a[172:175], v[132:135], v[224:227]\n"
    "v_lshlrev_b32 v164, 16, v164\n"
    "v_and_b32 v167, 0xffff0000, v166\n"
    "v_mfma_f32_16x16x32_bf16 v[228:231], a[176:179], v[132:135], v[228:231]\n"
    "s_waitcnt vmcnt(9)\n"
    "v_lshlrev_b32 v166, 16, v166\n"
    "v_mfma_f32_16x16x32_bf16 v[24:27], a[188:191], v[136:139], v[24:27]\n"
    "s_waitcnt vmcnt(5)\n"
    "v_and_b32 v169, 0xffff0000, v168\n"
    "v_mfma_f32_16x16x32_bf16 v[24:27], a[208:211], v[144:147], v[24:27]\n"
    "s_waitcnt vmcnt(3)\n"
    "v_lshlrev_b32 v168, 16, v168\n"
    "v_mfma_f32_16x16x32_bf16 v[24:27], a[228:231], v[152:155], v[24:27]\n"
    "v_and_b32 v171, 0xffff0000, v170\n"
    "s_waitcnt vmcnt(1)\n"
    "v_cmp_eq_u64 s[86:87], s[50:51], v[238:239]\n"
    "v_mfma_f32_16x16x32_bf16 v[212:215], a[180:183], v[140:143], v[212:215]\n"
    "s_barrier\n"
    "v_lshlrev_b32 v170, 16, v170\n"
    "v_mfma_f32_16x16x32_bf16 v[212:215], a[200:203], v[148:151], v[212:215]\n"
    "ds_write_b128 v232, v[24:27]\n"
    "v_and_b32 v173, 0xffff0000, v172\n"
    "v_mfma_f32_16x16x32_bf16 v[212:215], a[220:223], v[156:159], v[212:215]\n"
    "s_andn2_b32 s79, 0xff, s86\n"
    "s_cbranch_scc1 .Lf_slow28\n"
    ".Lf_back29:\n"
    "v_lshlrev_b32 v172, 16, v172\n"
    "v_mfma_f32_16x16x32_bf16 v[216:219], a[184:187], v[140:143], v[216:219]\n"
    "buffer_load_dwordx4 v[64:67], v236, s[8:11], s41 offen offset:0 sc1\n"
    "v_mfma_f32_16x16x32_bf16 v[216:219], a[204:207], v[148:151], v[216:219]\n"
    "ds_write_b128 v233, v[212:215] offset:8192\n"
    "v_mfma_f32_16x16x32_bf16 v[216:219], a[224:227], v[156:159], v[216:219]\n"
    "v_mfma_f32_16x16x32_bf16 v[220:223], a[188:191], v[140:143], v[220:223]\n"
    "v_mfma_f32_16x16x32_bf16 v[220:223], a[208:211], v[148:151], v[220:223]\n"
    "ds_write_b128 v233, v[216:219] offset:9216\n"
    "buffer_load_dwordx4 v[68:71], v237, s[8:11], s41 offen offset:0 sc1\n"
    "v_mfma_f32_16x16x32_bf16 v[220:223], a[228:231], v[156:159], v[220:223]\n"
    "v_mfma_f32_16x16x32_bf16 v[28:31], a[192:195], v[136:139], v[28:31]\n"
    "v_mfma_f32_16x16x32_bf16 v[28:31], a[212:215], v[144:147], v[28:31]\n"
    "ds_write_b128 v234, v[220:223] offset:16384\n"
    "v_mfma_f32_16x16x32_bf16 v[28:31], a[232:235], v[152:155], v[28:31]\n"
    "buffer_load_dwordx4 v[72:75], v236, s[8:11], s41 offen offset:2048 sc1\n"
    "v_mfma_f32_16x16x32_bf16 v[224:227], a[192:195], v[140:143], v[224:227]\n"
    "v_mfma_f32_16x16x32_bf16 v[224:227], a[212:215], v[148:151], v[224:227]\n"
    "ds_write_b128 v232, v[28:31] offset:1024\n"
    "v_mfma_f32_16x16x32_bf16 v[224:227], a[232:235], v[156:159], v[224:227]\n"
    "v_mfma_f32_16x16x32_bf16 v[208:211], a[196:199], v[136:139], v[208:211]\n"
    "buffer_load_dwordx4 v[76:79], v237, s[8:11], s41 offen offset:2048 sc1\n"
    "v_mfma_f32_16x16x32_bf16 v[208:211], a[216:219], v[144:147], v[208:211]\n"
    "ds_write_b128 v234, v[224:227] offset:17408\n"
    "v_mfma_f32_16x16x32_bf16 v[208:211], a[236:239], v[152:155], v[208:211]\n"
    "v_mfma_f32_16x16x32_bf16 v[228:231], a[196:199], v[140:143], v[228:231]\n"
    "v_mfma_f32_16x16x32_bf16 v[228:231], a[216:219], v[148:151], v[228:231]\n"
    "ds_write_b64 v232, v[210:211] offset:24576\n"
    "buffer_load_dwordx4 v[80:83], v236, s[8:11], s42 offen offset:0 sc1\n"
    "v_mfma_f32_16x16x32_bf16 v[228:231], a[236:239], v[156:159], v[228:231]\n"
    "s_add_u32 s83, s28, 262144\n"
    "buffer_load_dwordx2 v[238:239], v4, s[8:11], s83 offen sc1\n"
    "v_mfma_f32_16x16x32_bf16 v[16:19], a[180:183], v[136:139], v[16:19]\n"
    "v_mfma_f32_16x16x32_bf16 v[16:19], a[200:203], v[144:147], v[16:19]\n"
    "ds_write_b64 v233, v[228:229] offset:24584\n"
    "ds_write_b64 v234, v[230:231] offset:25600\n"
    "v_mfma_f32_16x16x32_bf16 v[16:19], a[220:223], v[152:155], v[16:19]\n"
    "buffer_load_dwordx4 v[84:87], v237, s[8:11], s42 offen offset:0 sc1\n"
    "s_waitcnt lgkmcnt(0)\n"
    "s_barrier\n"
    "v_mfma_f32_16x16x32_bf16 v[20:23], a[184:187], v[136:139], v[20:23]\n"
    "ds_read_b128 v[24:27], v254 offset:0\n"
    "ds_read_b128 v[212:215], v254 offset:8192\n"
    "ds_read_b128 v[220:223], v254 offset:16384\n"
    "ds_read_b128 v[28:31], v254 offset:1024\n"
    "v_mfma_f32_16x16x32_bf16 v[20:23], a[204:207], v[144:147], v[20:23]\n"
    "ds_read_b128 v[216:219], v254 offset:9216\n"
    "ds_read_b128 v[224:227], v254 offset:17408\n"
    "ds_read_b128 v[228:231], v254 offset:24576\n"
    "ds_read_b64 v[210:211], v254 offset:25600\n"
    "v_mfma_f32_16x16x32_bf16 v[20:23], a[224:227], v[152:155], v[20:23]\n"
    "buffer_load_dwordx4 v[88:91], v236, s[8:11], s42 offen offset:2048 sc1\n"
    "buffer_load_dwordx4 v[92:95], v237, s[8:11], s42 offen offset:2048 sc1\n"
    "v_pk_add_f32 v[174:175], v[174:175], v[16:17]\n"
    "v_pk_add_f32 v[176:177], v[176:177], v[18:19]\n"
    "s_waitcnt lgkmcnt(7)\n"
    "v_pk_add_f32 v[174:175], v[174:175], v[24:25]\n"
    "v_pk_add_f32 v[176:177], v[176:177], v[26:27]\n"
    "s_waitcnt lgkmcnt(6)\n"
    "v_pk_add_f32 v[174:175], v[174:175], v[212:213]\n"
    "v_pk_add_f32 v[176:177], v[176:177], v[214:215]\n"
    "s_waitcnt lgkmcnt(5)\n"
    "v_pk_add_f32 v[174:175], v[174:175], v[220:221]\n"
    "v_pk_add_f32 v[176:177], v[176:177], v[222:223]\n"
    "v_pk_add_f32 v[178:179], v[178:179], v[20:21]\n"
    "v_pk_add_f32 v[180:181], v[180:181], v[22:23]\n"
    "s_waitcnt lgkmcnt(4)\n"
    "v_pk_add_f32 v[178:179], v[178:179], v[28:29]\n"
    "v_pk_add_f32 v[180:181], v[180:181], v[30:31]\n"
    "s_waitcnt lgkmcnt(3)\n"
    "v_pk_add_f32 v[178:179], v[178:179], v[216:217]\n"
    "v_pk_add_f32 v[180:181], v[180:181], v[218:219]\n"
    "s_waitcnt lgkmcnt(2)\n"
    "v_pk_add_f32 v[178:179], v[178:179], v[224:225]\n"
    "v_pk_add_f32 v[180:181], v[180:181], v[226:227]\n"
    "v_pk_add_f32 v[182:183], v[182:183], v[208:209]\n"
    "s_waitcnt lgkmcnt(1)\n"
    "v_pk_add_f32 v[182:183], v[182:183], v[228:229]\n"
    "v_pk_add_f32 v[182:183], v[182:183], v[230:231]\n"
    "s_waitcnt lgkmcnt(0)\n"
    "v_pk_add_f32 v[182:183], v[182:183], v[210:211]\n"
    "v_pk_add_f32 v[248:249], v[174:175], v[176:177]\n"
    "v_pk_mul_f32 v[250:251], v[174:175], v[174:175]\n"
    "v_pk_fma_f32 v[250:251], v[176:177], v[176:177], v[250:251]\n"
    "v_pk_add_f32 v[248:249], v[248:249], v[178:179]\n"
    "v_pk_fma_f32 v[250:251], v[178:179], v[178:179], v[250:251]\n"
    "v_pk_add_f32 v[248:249], v[248:249], v[180:181]\n"
    "v_pk_fma_f32 v[250:251], v[180:181], v[180:181], v[250:251]\n"
    "v_pk_add_f32 v[248:249], v[248:249], v[182:183]\n"
    "v_pk_fma_f32 v[250:251], v[182:183], v[182:183], v[250:251]\n"
    "v_add_f32 v248, v248, v249\n"
    "v_add_f32 v250, v250, v251\n"
    "v_mov_b32 v249, s50\n"
    "v_mov_b32 v251, s50\n"
    "v_permlane16_swap_b32 v248, v250\n"
    "v_add_f32 v248, v248, v250\n"
    "v_mov_b32 v250, v248\n"
    "s_nop 1\n"
    "v_permlane32_swap_b32 v248, v250\n"
    "v_add_f32 v248, v248, v250\n"
    "v_mov_b32 v250, v248\n"
    "s_nop 1\n"
    "v_permlane16_swap_b32 v248, v250\n"
    "s_mov_b64 exec, 0xffff\n"
    "buffer_store_dwordx4 v[248:251], v2, s[8:11], s34 offen sc1\n"
    "s_mov_b64 exec, -1\n"
    "s_waitcnt vmcnt(4)\n"
    "v_cmp_eq_u64 s[86:87], s[50:51], v[238:239]\n"
    "s_nop 0\n"
    "s_andn2_b32 s79, 0xff, s86\n"
    "s_cbranch_scc1 .Lf_slow30\n"
    ".Lf_back31:\n"
    "buffer_load_dwordx4 v[96:99], v236, s[8:11], s43 offen offset:0 sc1\n"
    "buffer_load_dwordx4 v[100:103], v237, s[8:11], s43 offen offset:0 sc1\n"
    "buffer_load_dwordx4 v[104:107], v236, s[8:11], s43 offen offset:2048 sc1\n"
    "buffer_load_dwordx4 v[108:111], v237, s[8:11], s43 offen offset:2048 sc1\n"
    "v_mfma_f32_16x16x32_bf16 v[16:19], a[0:3], v[64:67], 0\n"
    "buffer_load_dwordx4 v[112:115], v236, s[8:11], s48 offen offset:0 sc1\n"
    "v_mfma_f32_16x16x32_bf16 v[20:23], a[4:7], v[64:67], 0\n"
    "v_mfma_f32_16x16x32_bf16 v[24:27], a[8:11], v[64:67], 0\n"
    "v_mfma_f32_16x16x32_bf16 v[28:31], a[12:15], v[64:67], 0\n"
    "v_mfma_f32_16x16x32_bf16 v[208:211], a[16:19], v[64:67], 0\n"
    "buffer_load_dwordx4 v[116:119], v237, s[8:11], s48 offen offset:0 sc1\n"
    "v_mfma_f32_16x16x32_bf16 v[212:215], a[0:3], v[68:71], 0\n"
    "v_mfma_f32_16x16x32_bf16 v[216:219], a[4:7], v[68:71], 0\n"
    "v_mfma_f32_16x16x32_bf16 v[220:223], a[8:11], v[68:71], 0\n"
    "v_mfma_f32_16x16x32_bf16 v[224:227], a[12:15], v[68:71], 0\n"
    "buffer_load_dwordx4 v[120:123], v236, s[8:11], s48 offen offset:2048 sc1\n"
    "v_mfma_f32_16x16x32_bf16 v[228:231], a[16:19], v[68:71], 0\n"
    "v_mfma_f32_16x16x32_bf16 v[16:19], a[20:23], v[72:75], v[16:19]\n"
    "v_add_u32 v235, 1310720, v235\n"
    "v_mfma_f32_16x16x32_bf16 v[20:23], a[24:27], v[72:75], v[20:23]\n"
    "v_mfma_f32_16x16x32_bf16 v[24:27], a[28:31], v[72:75], v[24:27]\n"
    "buffer_load_dwordx4 v[124:127], v237, s[8:11], s48 offen offset:2048 sc1\n"
    "v_mfma_f32_16x16x32_bf16 v[28:31], a[32:35], v[72:75], v[28:31]\n"
    "v_mfma_f32_16x16x32_bf16 v[208:211], a[36:39], v[72:75], v[208:211]\n"
    "v_mfma_f32_16x16x32_bf16 v[212:215], a[20:23], v[76:79], v[212:215]\n"
    "v_mfma_f32_16x16x32_bf16 v[216:219], a[24:27], v[76:79], v[216:219]\n"
    "buffer_load_dwordx4 v[128:131], v236, s[8:11], s49 offen offset:0 sc1\n"
    "v_mfma_f32_16x16x32_bf16 v[220:223], a[28:31], v[76:79], v[220:223]\n"
    "v_mfma_f32_16x16x32_bf16 v[224:227], a[32:35], v[76:79], v[224:227]\n"
    "v_mfma_f32_16x16x32_bf16 v[228:231], a[36:39], v[76:79], v[228:231]\n"
    "v_mfma_f32_16x16x32_bf16 v[16:19], a[40:43], v[80:83], v[16:19]\n"
    "buffer_load_dwordx4 v[132:135], v237, s[8:11], s49 offen offset:0 sc1\n"
    "v_mfma_f32_16x16x32_bf16 v[20:23], a[44:47], v[80:83], v[20:23]\n"
    "v_mfma_f32_16x16x32_bf16 v[24:27], a[48:51], v[80:83], v[24:27]\n"
    "v_mfma_f32_16x16x32_bf16 v[28:31], a[52:55], v[80:83], v[28:31]\n"
    "v_mfma_f32_16x16x32_bf16 v[208:211], a[56:59], v[80:83], v[208:211]\n"
    "buffer_load_dwordx4 v[136:139], v236, s[8:11], s49 offen offset:2048 sc1\n"
    "s_waitcnt vmcnt(14)\n"
    "v_mfma_f32_16x16x32_bf16 v[212:215], a[40:43], v[84:87], v[212:215]\n"
    "v_mfma_f32_16x16x32_bf16 v[216:219], a[44:47], v[84:87], v[216:219]\n"
    "v_mfma_f32_16x16x32_bf16 v[220:223], a[48:51], v[84:87], v[220:223]\n"
    "v_mfma_f32_16x16x32_bf16 v[224:227], a[52:55], v[84:87], v[224:227]\n"
    "buffer_load_dwordx4 v[140:143], v237, s[8:11], s49 offen offset:2048 sc1\n"
    "v_mfma_f32_16x16x32_bf16 v[228:231], a[56:59], v[84:87], v[228:231]\n"
    "s_waitcnt vmcnt(14)\n"
    "v_mfma_f32_16x16x32_bf16 v[16:19], a[60:63], v[88:91], v[16:19]\n"
    "v_mfma_f32_16x16x32_bf16 v[20:23], a[64:67], v[88:91], v[20:23]\n"
    "v_mfma_f32_16x16x32_bf16 v[24:27], a[68:71], v[88:91], v[24:27]\n"
    "buffer_load_dwordx4 v[144:147], v236, s[8:11], s85 offen offset:0 sc1\n"
    "v_mfma_f32_16x16x32_bf16 v[28:31], a[72:75], v[88:91], v[28:31]\n"
    "v_mfma_f32_16x16x32_bf16 v[208:211], a[76:79], v[88:91], v[208:211]\n"
    "s_waitcnt vmcnt(14)\n"
    "v_mfma_f32_16x16x32_bf16 v[212:215], a[60:63], v[92:95], v[212:215]\n"
    "v_mfma_f32_16x16x32_bf16 v[216:219], a[64:67], v[92:95], v[216:219]\n"
    "buffer_load_dwordx4 v[148:151], v237, s[8:11], s85 offen offset:0 sc1\n"
    "v_mfma_f32_16x16x32_bf16 v[220:223], a[68:71], v[92:95], v[220:223]\n"
    "v_mfma_f32_16x16x32_bf16 v[224:227], a[72:75], v[92:95], v[224:227]\n"
    "v_mfma_f32_16x16x32_bf16 v[228:231], a[76:79], v[92:95], v[228:231]\n"
    "s_waitcnt vmcnt(13)\n"
    "v_mfma_f32_16x16x32_bf16 v[16:19], a[80:83], v[96:99], v[16:19]\n"
    "buffer_load_dwordx4 v[152:155], v236, s[8:11], s85 offen offset:2048 sc1\n"
    "v_mfma_f32_16x16x32_bf16 v[20:23], a[84:87], v[96:99], v[20:23]\n"
    "v_mfma_f32_16x16x32_bf16 v[24:27], a[88:91], v[96:99], v[24:27]\n"
    "v_mfma_f32_16x16x32_bf16 v[28:31], a[92:95], v[96:99], v[28:31]\n"
    "v_mfma_f32_16x16x32_bf16 v[208:211], a[96:99], v[96:99], v[208:211]\n"
    "buffer_load_dwordx4 v[156:159], v237, s[8:11], s85 offen offset:2048 sc1\n"
    "s_waitcnt vmcnt(14)\n"
    "v_mfma_f32_16x16x32_bf16 v[212:215], a[80:83], v[100:103], v[212:215]\n"
    "v_mfma_f32_16x16x32_bf16 v[216:219], a[84:87], v[100:103], v[216:219]\n"
    "v_mfma_f32_16x16x32_bf16 v[220:223], a[88:91], v[100:103], v[220:223]\n"
    "v_mfma_f32_16x16x32_bf16 v[224:227], a[92:95], v[100:103], v[224:227]\n"
    "v_mfma_f32_16x16x32_bf16 v[228:231], a[96:99], v[100:103], v[228:231]\n"
    "s_waitcnt vmcnt(13)\n"
    "v_mfma_f32_16x16x32_bf16 v[16:19], a[100:103], v[104:107], v[16:19]\n"
    "v_mfma_f32_16x16x32_bf16 v[20:23], a[104:107], v[104:107], v[20:23]\n"
    "v_mfma_f32_16x16x32_bf16 v[24:27], a[108:111], v[104:107], v[24:27]\n"
    "s_add_u32 s84, s35, 0\n"
    "buffer_load_dwordx4 v[48:51], v8, s[8:11], s84 offen offset:0 sc1\n"
    "v_mfma_f32_16x16x32_bf16 v[28:31], a[112:115], v[104:107], v[28:31]\n"
    "v_mfma_f32_16x16x32_bf16 v[208:211], a[116:119], v[104:107], v[208:211]\n"
    "buffer_load_dwordx4 v[52:55], v8, s[8:11], s84 offen offset:512 sc1\n"
    "s_waitcnt vmcnt(14)\n"
    "v_mfma_f32_16x16x32_bf16 v[212:215], a[100:103], v[108:111], v[212:215]\n"
    "v_mfma_f32_16x16x32_bf16 v[216:219], a[104:107], v[108:111], v[216:219]\n"
    "buffer_load_dwordx4 v[56:59], v8, s[8:11], s84 offen offset:1024 sc1\n"
    "v_mfma_f32_16x16x32_bf16 v[220:223], a[108:111], v[108:111], v[220:223]\n"
    "v_mfma_f32_16x16x32_bf16 v[224:227], a[112:115], v[108:111], v[224:227]\n"
    "buffer_load_dwordx4 v[60:63], v8, s[8:11], s84 offen offset:1536 sc1\n"
    "v_mfma_f32_16x16x32_bf16 v[228:231], a[116:119], v[108:111], v[228:231]\n"
    "s_waitcnt vmcnt(15)\n"
    "v_mfma_f32_16x16x32_bf16 v[16:19], a[120:123], v[112:115], v[16:19]\n"
    "v_mfma_f32_16x16x32_bf16 v[20:23], a[124:127], v[112:115], v[20:23]\n"
    "v_mfma_f32_16x16x32_bf16 v[24:27], a[128:131], v[112:115], v[24:27]\n"
    "s_add_u32 s91, s30, 1310720\n"
    "buffer_load_dwordx4 v[184:187], v12, s[8:11], s91 offen sc1\n"
    "v_mfma_f32_16x16x32_bf16 v[28:31], a[132:135], v[112:115], v[28:31]\n"
    "v_mfma_f32_16x16x32_bf16 v[208:211], a[136:139], v[112:115], v[208:211]\n"
    "s_waitcnt vmcnt(15)\n"
    "v_mfma_f32_16x16x32_bf16 v[212:215], a[120:123], v[116:119], v[212:215]\n"
    "s_add_u32 s92, s31, 1310720\n"
    "buffer_load_dword v197, v13, s[8:11], s92 offen sc1\n"
    "v_mfma_f32_16x16x32_bf16 v[216:219], a[124:127], v[116:119], v[216:219]\n"
    "v_mfma_f32_16x16x32_bf16 v[220:223], a[128:131], v[116:119], v[220:223]\n"
    "v_mfma_f32_16x16x32_bf16 v[224:227], a[132:135], v[116:119], v[224:227]\n"
    "s_add_u32 s65, s32, 16384\n"
    "buffer_load_dwordx4 v[188:191], v14, s[8:11], s65 offen sc1\n"
    "v_mfma_f32_16x16x32_bf16 v[228:231], a[136:139], v[116:119], v[228:231]\n"
    "s_waitcnt vmcnt(16)\n"
    "v_mfma_f32_16x16x32_bf16 v[16:19], a[140:143], v[120:123], v[16:19]\n"
    "v_mfma_f32_16x16x32_bf16 v[20:23], a[144:147], v[120:123], v[20:23]\n"
    "s_add_u32 s66, s32, 49152\n"
    "buffer_load_dwordx4 v[192:195], v14, s[8:11], s66 offen sc1\n"
    "v_mfma_f32_16x16x32_bf16 v[24:27], a[148:151], v[120:123], v[24:27]\n"
    "v_mfma_f32_16x16x32_bf16 v[28:31], a[152:155], v[120:123], v[28:31]\n"
    "v_mfma_f32_16x16x32_bf16 v[208:211], a[156:159], v[120:123], v[208:211]\n"
    "s_add_u32 s67, s33, 16384\n"
    "buffer_load_dword v196, v5, s[8:11], s67 offen sc1\n"
    "s_waitcnt vmcnt(17)\n"
    "v_mfma_f32_16x16x32_bf16 v[212:215], a[140:143], v[124:127], v[212:215]\n"
    "v_mfma_f32_16x16x32_bf16 v[216:219], a[144:147], v[124:127], v[216:219]\n"
    "v_mfma_f32_16x16x32_bf16 v[220:223], a[148:151], v[124:127], v[220:223]\n"
    "v_mfma_f32_16x16x32_bf16 v[224:227], a[152:155], v[124:127], v[224:227]\n"
    "v_mfma_f32_16x16x32_bf16 v[228:231], a[156:159], v[124:127], v[228:231]\n"
    "s_waitcnt vmcnt(16)\n"
    "v_mfma_f32_16x16x32_bf16 v[16:19], a[160:163], v[128:131], v[16:19]\n"
    "v_mfma_f32_16x16x32_bf16 v[20:23], a[164:167], v[128:131], v[20:23]\n"
    "v_mfma_f32_16x16x32_bf16 v[24:27], a[168:171], v[128:131], v[24:27]\n"
    "v_mfma_f32_16x16x32_bf16 v[28:31], a[172:175], v[128:131], v[28:31]\n"
    "v_mfma_f32_16x16x32_bf16 v[208:211], a[176:179], v[128:131], v[208:211]\n"
    "s_waitcnt vmcnt(15)\n"
    "v_mfma_f32_16x16x32_bf16 v[212:215], a[160:163], v[132:135], v[212:215]\n"
    "v_mfma_f32_16x16x32_bf16 v[216:219], a[164:167], v[132:135], v[216:219]\n"
    "v_mfma_f32_16x16x32_bf16 v[220:223], a[168:171], v[132:135], v[220:223]\n"
    "v_mfma_f32_16x16x32_bf16 v[224:227], a[172:175], v[132:135], v[224:227]\n"
    "s_waitcnt vmcnt(8)\n"
    "v_xor_b32 v248, s50, v49\n"
    "v_bitop3_b32 v248, v248, v51, s50 bitop3:0xf6\n"
    "s_waitcnt vmcnt(7)\n"
    "v_bitop3_b32 v248, v248, v53, s50 bitop3:0xf6\n"
    "v_bitop3_b32 v248, v248, v55, s50 bitop3:0xf6\n"
    "s_waitcnt vmcnt(6)\n"
    "v_bitop3_b32 v248, v248, v57, s50 bitop3:0xf6\n"
    "v_bitop3_b32 v248, v248, v59, s50 bitop3:0xf6\n"
    "s_waitcnt vmcnt(5)\n"
    "v_bitop3_b32 v248, v248, v61, s50 bitop3:0xf6\n"
    "v_bitop3_b32 v248, v248, v63, s50 bitop3:0xf6\n"
    "v_cmp_ne_u32 vcc, 0, v248\n"
    "s_nop 0\n"
    "s_cmp_lg_u64 vcc, 0\n"
    "s_cbranch_scc1 .Lf_gslow32\n"
    ".Lf_gback33:\n"
    "v_add_f32 v48, v48, v52\n"
    "v_add_f32 v50, v50, v54\n"
    "v_add_f32 v56, v56, v60\n"
    "v_add_f32 v58, v58, v62\n"
    "v_add_f32 v48, v48, v56\n"
    "v_add_f32 v50, v50, v58\n"
    "s_nop 1\n"
    "v_permlane16_swap_b32 v48, v50\n"
    "v_add_f32 v48, v48, v50\n"
    "v_mov_b32 v50, v48\n"
    "s_nop 1\n"
    "v_permlane32_swap_b32 v48, v50\n"
    "v_add_f32 v48, v48, v50\n"
    "v_mov_b32 v50, v48\n"
    "s_nop 1\n"
    "v_permlane16_swap_b32 v48, v50\n"
    "v_mul_f32 v48, 0x3acccccd, v48\n"
    "v_mul_f32 v50, 0x3acccccd, v50\n"
    "v_fma_f32 v50, -v48, v48, v50\n"
    "v_max_f32 v50, 0, v50\n"
    "v_cvt_pk_bf16_f32 v50, v50, v50\n"
    "v_lshlrev_b32 v50, 16, v50\n"
    "v_add_f32 v50, 0x3727c5ac, v50\n"
    "v_cvt_pk_bf16_f32 v50, v50, v50\n"
    "v_lshlrev_b32 v50, 16, v50\n"
    "v_sqrt_f32 v50, v50\n"
    "s_nop 0\n"
    "v_cvt_pk_bf16_f32 v50, v50, v50\n"
    "v_lshlrev_b32 v50, 16, v50\n"
    "v_cvt_pk_bf16_f32 v48, v48, v48\n"
    "v_rcp_f32 v50, v50\n"
    "v_lshlrev_b32 v48, 16, v48\n"
    "v_mul_f32 v48, v48, v50\n"
    "v_pk_fma_f32 v[52:53], v[174:175], v[50:51], v[48:49] op_sel_hi:[1,0,0] neg_lo:[0,0,1] neg_hi:[0,0,1]\n"
    "v_pk_fma_f32 v[54:55], v[176:177], v[50:51], v[48:49] op_sel_hi:[1,0,0] neg_lo:[0,0,1] neg_hi:[0,0,1]\n"
    "v_pk_fma_f32 v[56:57], v[178:179], v[50:51], v[48:49] op_sel_hi:[1,0,0] neg_lo:[0,0,1] neg_hi:[0,0,1]\n"
    "v_pk_fma_f32 v[58:59], v[180:181], v[50:51], v[48:49] op_sel_hi:[1,0,0] neg_lo:[0,0,1] neg_hi:[0,0,1]\n"
    "v_pk_fma_f32 v[60:61], v[182:183], v[50:51], v[48:49] op_sel_hi:[1,0,0] neg_lo:[0,0,1] neg_hi:[0,0,1]\n"
    "v_pk_fma_f32 v[174:175], v[52:53], v[164:165], v[174:175]\n"
    "v_pk_fma_f32 v[176:177], v[54:55], v[166:167], v[176:177]\n"
    "v_pk_fma_f32 v[178:179], v[56:57], v[168:169], v[178:179]\n"
    "v_pk_fma_f32 v[180:181], v[58:59], v[170:171], v[180:181]\n"
    "v_pk_fma_f32 v[182:183], v[60:61], v[172:173], v[182:183]\n"
    "v_cvt_pk_bf16_f32 v56, v182, v183\n"
    "v_min_u32 v56, -2, v56\n"
    "v_cvt_pk_bf16_f32 v52, v174, v175\n"
    "v_mov_b32 v57, v56\n"
    "v_cvt_pk_bf16_f32 v53, v176, v177\n"
    "v_cvt_pk_bf16_f32 v54, v178, v179\n"
    "v_cvt_pk_bf16_f32 v55, v180, v181\n"
    "v_permlane16_swap_b32 v56, v57\n"
    "v_min_u32 v52, -2, v52\n"
    "v_min_u32 v54, -2, v54\n"
    "v_mov_b32 v58, v56\n"
    "v_mov_b32 v59, v57\n"
    "v_pk_add_f32 v[48:49], v[174:175], v[176:177]\n"
    "v_pk_mul_f32 v[50:51], v[174:175], v[174:175]\n"
    "s_add_u32 s89, s30, 2621440\n"
    "buffer_store_dwordx4 v[52:55], v12, s[8:11], s89 offen sc1\n"
    "v_permlane32_swap_b32 v56, v58\n"
    "v_permlane32_swap_b32 v57, v59\n"
    "v_pk_fma_f32 v[50:51], v[176:177], v[176:177], v[50:51]\n"
    "v_pk_add_f32 v[48:49], v[48:49], v[178:179]\n"
    "v_pk_fma_f32 v[50:51], v[178:179], v[178:179], v[50:51]\n"
    "v_pk_add_f32 v[48:49], v[48:49], v[180:181]\n"
    "v_pk_fma_f32 v[50:51], v[180:181], v[180:181], v[50:51]\n"
    "v_pk_add_f32 v[48:49], v[48:49], v[182:183]\n"
    "v_pk_fma_f32 v[50:51], v[182:183], v[182:183], v[50:51]\n"
    "v_add_f32 v48, v48, v49\n"
    "v_add_f32 v50, v50, v51\n"
    "v_mov_b32 v49, v50\n"
    "s_nop 1\n"
    "v_permlane16_swap_b32 v48, v49\n"
    "v_add_f32 v48, v48, v49\n"
    "v_mov_b32 v49, v48\n"
    "s_nop 1\n"
    "v_permlane32_swap_b32 v48, v49\n"
    "v_add_f32 v48, v48, v49\n"
    "v_mov_b32 v49, v48\n"
    "s_nop 1\n"
    "v_permlane16_swap_b32 v48, v49\n"
    "s_add_u32 s90, s31, 2621440\n"
    "s_mov_b64 exec, 0xffff\n"
    "buffer_store_dwordx4 v[56:59], v12, s[8:11], s90 offen sc1\n"
    "buffer_store_dwordx2 v[48:49], v11, s[8:11], s36 offen sc1\n"
    "s_mov_b64 exec, -1\n"
    "v_mfma_f32_16x16x32_bf16 v[228:231], a[176:179], v[132:135], v[228:231]\n"
    "v_mfma_f32_16x16x32_bf16 v[24:27], a[188:191], v[136:139], v[24:27]\n"
    "v_mov_b32 v240, -1\n"
    "v_mov_b32 v242, -1\n"
    "v_mfma_f32_16x16x32_bf16 v[24:27], a[208:211], v[144:147], v[24:27]\n"
    "v_mfma_f32_16x16x32_bf16 v[24:27], a[228:231], v[152:155], v[24:27]\n"
    "v_mfma_f32_16x16x32_bf16 v[212:215], a[180:183], v[140:143], v[212:215]\n"
    "s_add_u32 s68, s24, 9437184\n"
    "buffer_store_dwordx4 v[240:243], v10, s[8:11], s68 offen sc1\n"
    "s_barrier\n"
    "v_mfma_f32_16x16x32_bf16 v[212:215], a[200:203], v[148:151], v[212:215]\n"
    "ds_write_b128 v232, v[24:27]\n"
    "v_mfma_f32_16x16x32_bf16 v[212:215], a[220:223], v[156:159], v[212:215]\n"
    "v_mfma_f32_16x16x32_bf16 v[216:219], a[184:187], v[140:143], v[216:219]\n"
    "s_add_u32 s69, s25, 9437184\n"
    "buffer_store_dwordx4 v[240:243], v10, s[8:11], s69 offen sc1\n"
    "v_mfma_f32_16x16x32_bf16 v[216:219], a[204:207], v[148:151], v[216:219]\n"
    "ds_write_b128 v233, v[212:215] offset:8192\n"
    "v_mfma_f32_16x16x32_bf16 v[216:219], a[224:227], v[156:159], v[216:219]\n"
    "s_waitcnt vmcnt(9)\n"
    "v_lshlrev_b32 v198, 16, v184\n"
    "v_and_b32 v199, 0xffff0000, v184\n"
    "v_mfma_f32_16x16x32_bf16 v[220:223], a[188:191], v[140:143], v[220:223]\n"
    "v_lshlrev_b32 v200, 16, v185\n"
    "v_and_b32 v201, 0xffff0000, v185\n"
    "v_mfma_f32_16x16x32_bf16 v[220:223], a[208:211], v[148:151], v[220:223]\n"
    "ds_write_b128 v233, v[216:219] offset:9216\n"
    "v_lshlrev_b32 v202, 16, v186\n"
    "v_mfma_f32_16x16x32_bf16 v[220:223], a[228:231], v[156:159], v[220:223]\n"
    "v_and_b32 v203, 0xffff0000, v186\n"
    "v_lshlrev_b32 v204, 16, v187\n"
    "v_mfma_f32_16x16x32_bf16 v[28:31], a[192:195], v[136:139], v[28:31]\n"
    "v_and_b32 v205, 0xffff0000, v187\n"
    "s_waitcnt vmcnt(8)\n"
    "v_lshlrev_b32 v206, 16, v197\n"
    "v_mfma_f32_16x16x32_bf16 v[28:31], a[212:215], v[144:147], v[28:31]\n"
    "ds_write_b128 v234, v[220:223] offset:16384\n"
    "v_and_b32 v207, 0xffff0000, v197\n"
    "v_mfma_f32_16x16x32_bf16 v[28:31], a[232:235], v[152:155], v[28:31]\n"
    "s_waitcnt vmcnt(7)\n"
    "v_and_b32 v189, 0xffff0000, v188\n"
    "v_lshlrev_b32 v188, 16, v188\n"
    "v_mfma_f32_16x16x32_bf16 v[224:227], a[192:195], v[140:143], v[224:227]\n"
    "v_and_b32 v191, 0xffff0000, v190\n"
    "v_lshlrev_b32 v190, 16, v190\n"
    "v_mfma_f32_16x16x32_bf16 v[224:227], a[212:215], v[148:151], v[224:227]\n"
    "ds_write_b128 v232, v[28:31] offset:1024\n"
    "s_waitcnt vmcnt(6)\n"
    "v_and_b32 v193, 0xffff0000, v192\n"
    "v_mfma_f32_16x16x32_bf16 v[224:227], a[232:235], v[156:159], v[224:227]\n"
    "v_lshlrev_b32 v192, 16, v192\n"
    "v_and_b32 v195, 0xffff0000, v194\n"
    "v_mfma_f32_16x16x32_bf16 v[208:211], a[196:199], v[136:139], v[208:211]\n"
    "v_lshlrev_b32 v194, 16, v194\n"
    "s_waitcnt vmcnt(5)\n"
    "v_and_b32 v197, 0xffff0000, v196\n"
    "v_mfma_f32_16x16x32_bf16 v[208:211], a[216:219], v[144:147], v[208:211]\n"
    "ds_write_b128 v234, v[224:227] offset:17408\n"
    "v_lshlrev_b32 v196, 16, v196\n"
    "v_mfma_f32_16x16x32_bf16 v[208:211], a[236:239], v[152:155], v[208:211]\n"
    "s_add_u32 s70, s26, 9437184\n"
    "buffer_store_dwordx4 v[240:243], v10, s[8:11], s70 offen sc1\n"
    "v_mfma_f32_16x16x32_bf16 v[228:231], a[196:199], v[140:143], v[228:231]\n"
    "v_mfma_f32_16x16x32_bf16 v[228:231], a[216:219], v[148:151], v[228:231]\n"
    "ds_write_b64 v232, v[210:211] offset:24576\n"
    "v_mfma_f32_16x16x32_bf16 v[228:231], a[236:239], v[156:159], v[228:231]\n"
    "v_mfma_f32_16x16x32_bf16 v[16:19], a[180:183], v[136:139], v[16:19]\n"
    "v_mfma_f32_16x16x32_bf16 v[16:19], a[200:203], v[144:147], v[16:19]\n"
    "ds_write_b64 v233, v[228:229] offset:24584\n"
    "ds_write_b64 v234, v[230:231] offset:25600\n"
    "v_mfma_f32_16x16x32_bf16 v[16:19], a[220:223], v[152:155], v[16:19]\n"
    "s_waitcnt lgkmcnt(0)\n"
    "s_barrier\n"
    "v_mfma_f32_16x16x32_bf16 v[20:23], a[184:187], v[136:139], v[20:23]\n"
    "ds_read_b128 v[24:27], v254 offset:0\n"
    "ds_read_b128 v[212:215], v254 offset:8192\n"
    "ds_read_b128 v[220:223], v254 offset:16384\n"
    "ds_read_b128 v[28:31], v254 offset:1024\n"
    "s_waitcnt vmcnt(3)\n"
    "s_mov_b64 exec, 1\n"
    "ds_add_rtn_u32 v9, v3, v3 offset:57352\n"
    "s_mov_b64 exec, -1\n"
    "v_mfma_f32_16x16x32_bf16 v[20:23], a[204:207], v[144:147], v[20:23]\n"
    "ds_read_b128 v[216:219], v254 offset:9216\n"
    "ds_read_b128 v[224:227], v254 offset:17408\n"
    "ds_read_b128 v[228:231], v254 offset:24576\n"
    "ds_read_b64 v[210:211], v254 offset:25600\n"
    "v_mfma_f32_16x16x32_bf16 v[20:23], a[224:227], v[152:155], v[20:23]\n"
    "s_waitcnt lgkmcnt(4)\n"
    "v_readfirstlane_b32 s79, v9\n"
    "s_nop 0\n"
    "s_cmp_eq_u32 s79, 0x30000\n"
    "s_cselect_b64 exec, 0xff, 0\n"
    "buffer_store_dwordx4 v[252:255], v2, s[8:11], s37 offen sc1\n"
    "s_mov_b64 exec, -1\n"
    "v_pk_add_f32 v[198:199], v[198:199], v[16:17]\n"
    "v_pk_add_f32 v[200:201], v[200:201], v[18:19]\n"
    "v_pk_add_f32 v[198:199], v[198:199], v[24:25]\n"
    "v_pk_add_f32 v[200:201], v[200:201], v[26:27]\n"
    "v_pk_add_f32 v[198:199], v[198:199], v[212:213]\n"
    "v_pk_add_f32 v[200:201], v[200:201], v[214:215]\n"
    "v_pk_add_f32 v[198:199], v[198:199], v[220:221]\n"
    "v_pk_add_f32 v[200:201], v[200:201], v[222:223]\n"
    "v_pk_add_f32 v[202:203], v[202:203], v[20:21]\n"
    "v_pk_add_f32 v[204:205], v[204:205], v[22:23]\n"
    "v_pk_add_f32 v[202:203], v[202:203], v[28:29]\n"
    "v_pk_add_f32 v[204:205], v[204:205], v[30:31]\n"
    "s_waitcnt lgkmcnt(3)\n"
    "v_pk_add_f32 v[202:203], v[202:203], v[216:217]\n"
    "v_pk_add_f32 v[204:205], v[204:205], v[218:219]\n"
    "s_waitcnt lgkmcnt(2)\n"
    "v_pk_add_f32 v[202:203], v[202:203], v[224:225]\n"
    "v_pk_add_f32 v[204:205], v[204:205], v[226:227]\n"
    "v_pk_add_f32 v[206:207], v[206:207], v[208:209]\n"
    "s_waitcnt lgkmcnt(1)\n"
    "v_pk_add_f32 v[206:207], v[206:207], v[228:229]\n"
    "v_pk_add_f32 v[206:207], v[206:207], v[230:231]\n"
    "s_waitcnt lgkmcnt(0)\n"
    "v_pk_add_f32 v[206:207], v[206:207], v[210:211]\n"
    "v_pk_add_f32 v[248:249], v[198:199], v[200:201]\n"
    "v_pk_mul_f32 v[250:251], v[198:199], v[198:199]\n"
    "v_pk_fma_f32 v[250:251], v[200:201], v[200:201], v[250:251]\n"
    "v_pk_add_f32 v[248:249], v[248:249], v[202:203]\n"
    "v_pk_fma_f32 v[250:251], v[202:203], v[202:203], v[250:251]\n"
    "v_pk_add_f32 v[248:249], v[248:249], v[204:205]\n"
    "v_pk_fma_f32 v[250:251], v[204:205], v[204:205], v[250:251]\n"
    "v_pk_add_f32 v[248:249], v[248:249], v[206:207]\n"
    "v_pk_fma_f32 v[250:251], v[206:207], v[206:207], v[250:251]\n"
    "v_add_f32 v248, v248, v249\n"
    "v_add_f32 v250, v250, v251\n"
    "v_mov_b32 v249, s50\n"
    "v_mov_b32 v251, s50\n"
    "v_permlane16_swap_b32 v248, v250\n"
    "v_add_f32 v248, v248, v250\n"
    "v_mov_b32 v250, v248\n"
    "s_nop 1\n"
    "v_permlane32_swap_b32 v248, v250\n"
    "v_add_f32 v248, v248, v250\n"
    "v_mov_b32 v250, v248\n"
    "s_nop 1\n"
    "v_permlane16_swap_b32 v248, v250\n"
    "s_add_u32 s71, s34, 262144\n"
    "s_mov_b64 exec, 0xffff\n"
    "buffer_store_dwordx4 v[248:251], v2, s[8:11], s71 offen sc1\n"
    "s_mov_b64 exec, -1\n"
    "s_sleep 2\n"
    "buffer_load_dwordx4 v[64:67], v235, s[8:11], s12 offen offset:0 sc1\n"
    "buffer_load_dwordx4 v[68:71], v235, s[8:11], s12 offen offset:256 sc1\n"
    "buffer_load_dwordx4 v[72:75], v235, s[8:11], s12 offen offset:2048 sc1\n"
    "buffer_load_dwordx4 v[76:79], v235, s[8:11], s12 offen offset:2304 sc1\n"
    "buffer_load_dwordx4 v[80:83], v235, s[8:11], s13 offen offset:0 sc1\n"
    "buffer_load_dwordx4 v[84:87], v235, s[8:11], s13 offen offset:256 sc1\n"
    "buffer_load_dwordx4 v[88:91], v235, s[8:11], s13 offen offset:2048 sc1\n"
    "buffer_load_dwordx4 v[92:95], v235, s[8:11], s13 offen offset:2304 sc1\n"
    "ds_read_b128 v[208:211], v2 offset:0\n"
    "ds_read_b128 v[212:215], v2 offset:2048\n"
    "ds_read_b128 v[216:219], v2 offset:4096\n"
    "ds_read_b128 v[220:223], v2 offset:1024\n"
    "ds_read_b128 v[224:227], v2 offset:3072\n"
    "ds_read_b128 v[228:231], v2 offset:5120\n"
    "s_waitcnt vmcnt(6)\n"
    "v_max3_u32 v236, v64, v66, v68\n"
    "v_max_u32 v236, v236, v70\n"
    "v_cmp_eq_u32 vcc, -1, v236\n"
    "s_nop 0\n"
    "s_cbranch_vccnz .Lf_xslow34\n"
    ".Lf_xback35:\n"
    "s_waitcnt lgkmcnt(5)\n"
    "v_mfma_f32_16x16x32_bf16 v[16:19], v[208:211], v[64:67], 0\n"
    "v_mfma_f32_16x16x32_bf16 v[24:27], v[208:211], v[68:71], 0\n"
    "s_waitcnt lgkmcnt(4)\n"
    "ds_read_b128 v[208:211], v2 offset:6144\n"
    "buffer_load_dwordx4 v[96:99], v235, s[8:11], s14 offen offset:0 sc1\n"
    "v_mfma_f32_16x16x32_bf16 v[32:35], v[212:215], v[64:67], 0\n"
    "v_mfma_f32_16x16x32_bf16 v[40:43], v[212:215], v[68:71], 0\n"
    "s_waitcnt lgkmcnt(4)\n"
    "ds_read_b128 v[212:215], v2 offset:8192\n"
    "v_mfma_f32_16x16x32_bf16 v[48:51], v[216:219], v[64:67], 0\n"
    "v_mfma_f32_16x16x32_bf16 v[56:59], v[216:219], v[68:71], 0\n"
    "s_waitcnt lgkmcnt(4)\n"
    "ds_read_b128 v[216:219], v2 offset:10240\n"
    "v_mfma_f32_16x16x32_bf16 v[20:23], v[220:223], v[64:67], 0\n"
    "s_waitcnt vmcnt(5)\n"
    "v_max3_u32 v236, v72, v74, v76\n"
    "v_max_u32 v236, v236, v78\n"
    "v_mfma_f32_16x16x32_bf16 v[28:31], v[220:223], v[68:71], 0\n"
    "s_waitcnt lgkmcnt(4)\n"
    "ds_read_b128 v[220:223], v2 offset:7168\n"
    "buffer_load_dwordx4 v[100:103], v235, s[8:11], s14 offen offset:256 sc1\n"
    "v_mfma_f32_16x16x32_bf16 v[36:39], v[224:227], v[64:67], 0\n"
    "v_cmp_eq_u32 vcc, -1, v236\n"
    "v_mfma_f32_16x16x32_bf16 v[44:47], v[224:227], v[68:71], 0\n"
    "s_waitcnt lgkmcnt(4)\n"
    "ds_read_b128 v[224:227], v2 offset:9216\n"
    "v_mfma_f32_16x16x32_bf16 v[52:55], v[228:231], v[64:67], 0\n"
    "s_cbranch_vccnz .Lf_xslow37\n"
    ".Lf_xback38:\n"
    "v_mfma_f32_16x16x32_bf16 v[60:63], v[228:231], v[68:71], 0\n"
    "s_waitcnt lgkmcnt(4)\n"
    "ds_read_b128 v[228:231], v2 offset:11264\n"
    "buffer_load_dwordx4 v[104:107], v235, s[8:11], s14 offen offset:2048 sc1\n"
    "v_mfma_f32_16x16x32_bf16 v[16:19], v[208:211], v[72:75], v[16:19]\n"
    "v_mfma_f32_16x16x32_bf16 v[24:27], v[208:211], v[76:79], v[24:27]\n"
    "s_waitcnt lgkmcnt(4)\n"
    "ds_read_b128 v[208:211], v2 offset:12288\n"
    "v_mfma_f32_16x16x32_bf16 v[32:35], v[212:215], v[72:75], v[32:35]\n"
    "s_add_u32 s83, s23, 131072\n"
    "buffer_load_dwordx2 v[238:239], v4, s[8:11], s83 offen sc1\n"
    "v_mfma_f32_16x16x32_bf16 v[40:43], v[212:215], v[76:79], v[40:43]\n"
    "s_waitcnt lgkmcnt(4)\n"
    "ds_read_b128 v[212:215], v2 offset:14336\n"
    "v_mfma_f32_16x16x32_bf16 v[48:51], v[216:219], v[72:75], v[48:51]\n"
    "s_add_u32 s84, s35, 262144\n"
    "buffer_load_dwordx4 v[144:147], v8, s[8:11], s84 offen offset:0 sc1\n"
    "v_mfma_f32_16x16x32_bf16 v[56:59], v[216:219], v[76:79], v[56:59]\n"
    "s_waitcnt lgkmcnt(4)\n"
    "ds_read_b128 v[216:219], v2 offset:16384\n"
    "buffer_load_dwordx4 v[108:111], v235, s[8:11], s14 offen offset:2304 sc1\n"
    "v_mfma_f32_16x16x32_bf16 v[20:23], v[220:223], v[72:75], v[20:23]\n"
    "s_waitcnt vmcnt(8)\n"
    "v_max3_u32 v236, v80, v82, v84\n"
    "v_max_u32 v236, v236, v86\n"
    "v_mfma_f32_16x16x32_bf16 v[28:31], v[220:223], v[76:79], v[28:31]\n"
    "s_waitcnt lgkmcnt(4)\n"
    "ds_read_b128 v[220:223], v2 offset:13312\n"
    "buffer_load_dwordx4 v[148:151], v8, s[8:11], s84 offen offset:512 sc1\n"
    "v_mfma_f32_16x16x32_bf16 v[36:39], v[224:227], v[72:75], v[36:39]\n"
    "v_cmp_eq_u32 vcc, -1, v236\n"
    "v_mfma_f32_16x16x32_bf16 v[44:47], v[224:227], v[76:79], v[44:47]\n"
    "s_waitcnt lgkmcnt(4)\n"
    "ds_read_b128 v[224:227], v2 offset:15360\n"
    "buffer_load_dwordx4 v[152:155], v8, s[8:11], s84 offen offset:1024 sc1\n"
    "v_mfma_f32_16x16x32_bf16 v[52:55], v[228:231], v[72:75], v[52:55]\n"
    "s_cbranch_vccnz .Lf_xslow39\n"
    ".Lf_xback40:\n"
    "v_mfma_f32_16x16x32_bf16 v[60:63], v[228:231], v[76:79], v[60:63]\n"
    "s_waitcnt lgkmcnt(4)\n"
    "ds_read_b128 v[228:231], v2 offset:17408\n"
    "buffer_load_dwordx4 v[156:159], v8, s[8:11], s84 offen offset:1536 sc1\n"
    "v_mfma_f32_16x16x32_bf16 v[16:19], v[208:211], v[80:83], v[16:19]\n"
    "v_mfma_f32_16x16x32_bf16 v[24:27], v[208:211], v[84:87], v[24:27]\n"
    "s_waitcnt lgkmcnt(4)\n"
    "ds_read_b128 v[208:211], v2 offset:18432\n"
    "buffer_load_dwordx4 v[112:115], v235, s[8:11], s15 offen offset:0 sc1\n"
    "v_mfma_f32_16x16x32_bf16 v[32:35], v[212:215], v[80:83], v[32:35]\n"
    "v_mfma_f32_16x16x32_bf16 v[40:43], v[212:215], v[84:87], v[40:43]\n"
    "s_waitcnt lgkmcnt(4)\n"
    "ds_read_b128 v[212:215], v2 offset:20480\n"
    "buffer_load_dwordx4 v[116:119], v235, s[8:11], s15 offen offset:256 sc1\n"
    "v_mfma_f32_16x16x32_bf16 v[48:51], v[216:219], v[80:83], v[48:51]\n"
    "v_mfma_f32_16x16x32_bf16 v[56:59], v[216:219], v[84:87], v[56:59]\n"
    "s_waitcnt lgkmcnt(4)\n"
    "ds_read_b128 v[216:219], v2 offset:22528\n"
    "v_mfma_f32_16x16x32_bf16 v[20:23], v[220:223], v[80:83], v[20:23]\n"
    "s_waitcnt vmcnt(11)\n"
    "v_max3_u32 v236, v88, v90, v92\n"
    "v_max_u32 v236, v236, v94\n"
    "v_mfma_f32_16x16x32_bf16 v[28:31], v[220:223], v[84:87], v[28:31]\n"
    "s_waitcnt lgkmcnt(4)\n"
    "ds_read_b128 v[220:223], v2 offset:19456\n"
    "v_mfma_f32_16x16x32_bf16 v[36:39], v[224:227], v[80:83], v[36:39]\n"
    "v_cmp_eq_u32 vcc, -1, v236\n"
    "v_mfma_f32_16x16x32_bf16 v[44:47], v[224:227], v[84:87], v[44:47]\n"
    "s_waitcnt lgkmcnt(4)\n"
    "ds_read_b128 v[224:227], v2 offset:21504\n"
    "v_mfma_f32_16x16x32_bf16 v[52:55], v[228:231], v[80:83], v[52:55]\n"
    "s_cbranch_vccnz .Lf_xslow41\n"
    ".Lf_xback42:\n"
    "v_mfma_f32_16x16x32_bf16 v[60:63], v[228:231], v[84:87], v[60:63]\n"
    "s_waitcnt lgkmcnt(4)\n"
    "ds_read_b128 v[228:231], v2 offset:23552\n"
    "v_mfma_f32_16x16x32_bf16 v[16:19], v[208:211], v[88:91], v[16:19]\n"
    "v_mfma_f32_16x16x32_bf16 v[24:27], v[208:211], v[92:95], v[24:27]\n"
    "s_waitcnt lgkmcnt(4)\n"
    "ds_read_b128 v[208:211], v2 offset:24576\n"
    "v_mfma_f32_16x16x32_bf16 v[32:35], v[212:215], v[88:91], v[32:35]\n"
    "s_waitcnt vmcnt(7)\n"
    "v_cmp_eq_u64 s[86:87], s[50:51], v[238:239]\n"
    "v_mfma_f32_16x16x32_bf16 v[40:43], v[212:215], v[92:95], v[40:43]\n"
    "s_waitcnt lgkmcnt(4)\n"
    "ds_read_b128 v[212:215], v2 offset:26624\n"
    "v_mfma_f32_16x16x32_bf16 v[48:51], v[216:219], v[88:91], v[48:51]\n"
    "s_andn2_b32 s79, 0xff, s86\n"
    "s_cbranch_scc1 .Lf_slow43\n"
    ".Lf_back44:\n"
    "v_mfma_f32_16x16x32_bf16 v[56:59], v[216:219], v[92:95], v[56:59]\n"
    "s_waitcnt lgkmcnt(4)\n"
    "ds_read_b128 v[216:219], v2 offset:28672\n"
    "buffer_load_dwordx4 v[120:123], v235, s[8:11], s15 offen offset:2048 sc1\n"
    "v_mfma_f32_16x16x32_bf16 v[20:23], v[220:223], v[88:91], v[20:23]\n"
    "v_max3_u32 v236, v96, v98, v100\n"
    "v_max_u32 v236, v236, v102\n"
    "v_mfma_f32_16x16x32_bf16 v[28:31], v[220:223], v[92:95], v[28:31]\n"
    "s_waitcnt lgkmcnt(4)\n"
    "ds_read_b128 v[220:223], v2 offset:25600\n"
    "buffer_load_dwordx4 v[124:127], v235, s[8:11], s15 offen offset:2304 sc1\n"
    "v_mfma_f32_16x16x32_bf16 v[36:39], v[224:227], v[88:91], v[36:39]\n"
    "v_cmp_eq_u32 vcc, -1, v236\n"
    "v_mfma_f32_16x16x32_bf16 v[44:47], v[224:227], v[92:95], v[44:47]\n"
    "s_waitcnt lgkmcnt(4)\n"
    "ds_read_b128 v[224:227], v2 offset:27648\n"
    "buffer_load_dwordx4 v[128:131], v235, s[8:11], s16 offen offset:0 sc1\n"
    "v_mfma_f32_16x16x32_bf16 v[52:55], v[228:231], v[88:91], v[52:55]\n"
    "s_cbranch_vccnz .Lf_xslow45\n"
    ".Lf_xback46:\n"
    "v_mfma_f32_16x16x32_bf16 v[60:63], v[228:231], v[92:95], v[60:63]\n"
    "s_waitcnt lgkmcnt(4)\n"
    "ds_read_b128 v[228:231], v2 offset:29696\n"
    "v_mfma_f32_16x16x32_bf16 v[16:19], v[208:211], v[96:99], v[16:19]\n"
    "s_waitcnt vmcnt(9)\n"
    "v_xor_b32 v248, s50, v145\n"
    "v_bitop3_b32 v248, v248, v147, s50 bitop3:0xf6\n"
    "s_waitcnt vmcnt(7)\n"
    "v_bitop3_b32 v248, v248, v149, s50 bitop3:0xf6\n"
    "v_bitop3_b32 v248, v248, v151, s50 bitop3:0xf6\n"
    "s_waitcnt vmcnt(6)\n"
    "v_bitop3_b32 v248, v248, v153, s50 bitop3:0xf6\n"
    "v_bitop3_b32 v248, v248, v155, s50 bitop3:0xf6\n"
    "s_waitcnt vmcnt(5)\n"
    "v_bitop3_b32 v248, v248, v157, s50 bitop3:0xf6\n"
    "v_bitop3_b32 v248, v248, v159, s50 bitop3:0xf6\n"
    "v_cmp_ne_u32 vcc, 0, v248\n"
    "s_nop 0\n"
    "s_cmp_lg_u64 vcc, 0\n"
    "s_cbranch_scc1 .Lf_gslow47\n"
    ".Lf_gback48:\n"
    "v_add_f32 v144, v144, v148\n"
    "v_add_f32 v146, v146, v150\n"
    "v_add_f32 v152, v152, v156\n"
    "v_add_f32 v154, v154, v158\n"
    "v_add_f32 v144, v144, v152\n"
    "v_add_f32 v146, v146, v154\n"
    "s_nop 1\n"
    "v_permlane16_swap_b32 v144, v146\n"
    "v_add_f32 v144, v144, v146\n"
    "v_mov_b32 v146, v144\n"
    "s_nop 1\n"
    "v_permlane32_swap_b32 v144, v146\n"
    "v_add_f32 v144, v144, v146\n"
    "v_mov_b32 v146, v144\n"
    "s_nop 1\n"
    "v_permlane16_swap_b32 v144, v146\n"
    "v_mul_f32 v144, 0x3acccccd, v144\n"
    "v_mul_f32 v146, 0x3acccccd, v146\n"
    "v_fma_f32 v146, -v144, v144, v146\n"
    "v_max_f32 v146, 0, v146\n"
    "v_cvt_pk_bf16_f32 v146, v146, v146\n"
    "v_lshlrev_b32 v146, 16, v146\n"
    "v_add_f32 v146, 0x3727c5ac, v146\n"
    "v_cvt_pk_bf16_f32 v146, v146, v146\n"
    "v_lshlrev_b32 v146, 16, v146\n"
    "v_sqrt_f32 v146, v146\n"
    "s_nop 0\n"
    "v_cvt_pk_bf16_f32 v146, v146, v146\n"
    "v_lshlrev_b32 v146, 16, v146\n"
    "v_cvt_pk_bf16_f32 v144, v144, v144\n"
    "v_rcp_f32 v146, v146\n"
    "v_lshlrev_b32 v144, 16, v144\n"
    "v_mul_f32 v144, v144, v146\n"
    "v_pk_fma_f32 v[148:149], v[198:199], v[146:147], v[144:145] op_sel_hi:[1,0,0] neg_lo:[0,0,1] neg_hi:[0,0,1]\n"
    "v_pk_fma_f32 v[150:151], v[200:201], v[146:147], v[144:145] op_sel_hi:[1,0,0] neg_lo:[0,0,1] neg_hi:[0,0,1]\n"
    "v_pk_fma_f32 v[152:153], v[202:203], v[146:147], v[144:145] op_sel_hi:[1,0,0] neg_lo:[0,0,1] neg_hi:[0,0,1]\n"
    "v_pk_fma_f32 v[154:155], v[204:205], v[146:147], v[144:145] op_sel_hi:[1,0,0] neg_lo:[0,0,1] neg_hi:[0,0,1]\n"
    "v_pk_fma_f32 v[156:157], v[206:207], v[146:147], v[144:145] op_sel_hi:[1,0,0] neg_lo:[0,0,1] neg_hi:[0,0,1]\n"
    "v_pk_fma_f32 v[198:199], v[148:149], v[188:189], v[198:199]\n"
    "v_pk_fma_f32 v[200:201], v[150:151], v[190:191], v[200:201]\n"
    "v_pk_fma_f32 v[202:203], v[152:153], v[192:193], v[202:203]\n"
    "v_pk_fma_f32 v[204:205], v[154:155], v[194:195], v[204:205]\n"
    "v_pk_fma_f32 v[206:207], v[156:157], v[196:197], v[206:207]\n"
    "v_cvt_pk_bf16_f32 v152, v206, v207\n"
    "v_cvt_pk_bf16_f32 v148, v198, v199\n"
    "v_mov_b32 v153, v152\n"
    "v_cvt_pk_bf16_f32 v149, v200, v201\n"
    "v_cvt_pk_bf16_f32 v150, v202, v203\n"
    "v_cvt_pk_bf16_f32 v151, v204, v205\n"
    "v_permlane16_swap_b32 v152, v153\n"
    "v_mov_b32 v154, v152\n"
    "v_mov_b32 v155, v153\n"
    "v_pk_add_f32 v[144:145], v[198:199], v[200:201]\n"
    "v_pk_mul_f32 v[146:147], v[198:199], v[198:199]\n"
    "s_add_u32 s72, s30, 3932160\n"
    "buffer_store_dwordx4 v[148:151], v12, s[8:11], s72 offen sc1\n"
    "v_permlane32_swap_b32 v152, v154\n"
    "v_permlane32_swap_b32 v153, v155\n"
    "v_pk_fma_f32 v[146:147], v[200:201], v[200:201], v[146:147]\n"
    "v_pk_add_f32 v[144:145], v[144:145], v[202:203]\n"
    "v_pk_fma_f32 v[146:147], v[202:203], v[202:203], v[146:147]\n"
    "v_pk_add_f32 v[144:145], v[144:145], v[204:205]\n"
    "v_pk_fma_f32 v[146:147], v[204:205], v[204:205], v[146:147]\n"
    "v_pk_add_f32 v[144:145], v[144:145], v[206:207]\n"
    "v_pk_fma_f32 v[146:147], v[206:207], v[206:207], v[146:147]\n"
    "v_add_f32 v144, v144, v145\n"
    "v_add_f32 v146, v146, v147\n"
    "v_mov_b32 v145, v146\n"
    "s_nop 1\n"
    "v_permlane16_swap_b32 v144, v145\n"
    "v_add_f32 v144, v144, v145\n"
    "v_mov_b32 v145, v144\n"
    "s_nop 1\n"
    "v_permlane32_swap_b32 v144, v145\n"
    "v_add_f32 v144, v144, v145\n"
    "v_mov_b32 v145, v144\n"
    "s_nop 1\n"
    "v_permlane16_swap_b32 v144, v145\n"
    "s_add_u32 s73, s31, 3932160\n"
    "s_add_u32 s88, s36, 131072\n"
    "s_mov_b64 exec, 0xffff\n"
    "buffer_store_dwordx4 v[152:155], v12, s[8:11], s73 offen sc1\n"
    "buffer_store_dwordx2 v[144:145], v11, s[8:11], s88 offen sc1\n"
    "s_mov_b64 exec, -1\n"
    "v_mfma_f32_16x16x32_bf16 v[24:27], v[208:211], v[100:103], v[24:27]\n"
    "s_waitcnt lgkmcnt(4)\n"
    "ds_read_b128 v[208:211], v2 offset:30720\n"
    "buffer_load_dwordx4 v[132:135], v235, s[8:11], s16 offen offset:256 sc1\n"
    "v_mfma_f32_16x16x32_bf16 v[32:35], v[212:215], v[96:99], v[32:35]\n"
    "s_add_u32 s89, s22, 262144\n"
    "buffer_load_dwordx4 v[168:171], v2, s[8:11], s89 offen offset:0 sc1\n"
    "v_mfma_f32_16x16x32_bf16 v[40:43], v[212:215], v[100:103], v[40:43]\n"
    "s_waitcnt lgkmcnt(4)\n"
    "ds_read_b128 v[212:215], v2 offset:32768\n"
    "v_mfma_f32_16x16x32_bf16 v[48:51], v[216:219], v[96:99], v[48:51]\n"
    "v_mfma_f32_16x16x32_bf16 v[56:59], v[216:219], v[100:103], v[56:59]\n"
    "s_waitcnt lgkmcnt(4)\n"
    "ds_read_b128 v[216:219], v2 offset:34816\n"
    "v_mfma_f32_16x16x32_bf16 v[20:23], v[220:223], v[96:99], v[20:23]\n"
    "v_max3_u32 v236, v104, v106, v108\n"
    "v_max_u32 v236, v236, v110\n"
    "v_mfma_f32_16x16x32_bf16 v[28:31], v[220:223], v[100:103], v[28:31]\n"
    "s_waitcnt lgkmcnt(4)\n"
    "ds_read_b128 v[220:223], v2 offset:31744\n"
    "buffer_load_dwordx4 v[136:139], v235, s[8:11], s16 offen offset:2048 sc1\n"
    "v_mfma_f32_16x16x32_bf16 v[36:39], v[224:227], v[96:99], v[36:39]\n"
    "v_cmp_eq_u32 vcc, -1, v236\n"
    "v_mfma_f32_16x16x32_bf16 v[44:47], v[224:227], v[100:103], v[44:47]\n"
    "s_waitcnt lgkmcnt(4)\n"
    "ds_read_b128 v[224:227], v2 offset:33792\n"
    "v_mfma_f32_16x16x32_bf16 v[52:55], v[228:231], v[96:99], v[52:55]\n"
    "s_cbranch_vccnz .Lf_xslow49\n"
    ".Lf_xback50:\n"
    "v_mfma_f32_16x16x32_bf16 v[60:63], v[228:231], v[100:103], v[60:63]\n"
    "s_waitcnt lgkmcnt(4)\n"
    "ds_read_b128 v[228:231], v2 offset:35840\n"
    "buffer_load_dwordx4 v[140:143], v235, s[8:11], s16 offen offset:2304 sc1\n"
    "v_mfma_f32_16x16x32_bf16 v[16:19], v[208:211], v[104:107], v[16:19]\n"
    "s_add_u32 s90, s22, 262144\n"
    "buffer_load_dwordx4 v[172:175], v2, s[8:11], s90 offen offset:1024 sc1\n"
    "v_mfma_f32_16x16x32_bf16 v[24:27], v[208:211], v[108:111], v[24:27]\n"
    "s_waitcnt lgkmcnt(4)\n"
    "ds_read_b128 v[208:211], v2 offset:36864\n"
    "v_mfma_f32_16x16x32_bf16 v[32:35], v[212:215], v[104:107], v[32:35]\n"
    "v_mfma_f32_16x16x32_bf16 v[40:43], v[212:215], v[108:111], v[40:43]\n"
    "s_waitcnt lgkmcnt(4)\n"
    "ds_read_b128 v[212:215], v2 offset:38912\n"
    "v_mfma_f32_16x16x32_bf16 v[48:51], v[216:219], v[104:107], v[48:51]\n"
    "v_mfma_f32_16x16x32_bf16 v[56:59], v[216:219], v[108:111], v[56:59]\n"
    "s_waitcnt lgkmcnt(4)\n"
    "ds_read_b128 v[216:219], v2 offset:40960\n"
    "buffer_load_dwordx4 v[144:147], v235, s[8:11], s17 offen offset:0 sc1\n"
    "v_mfma_f32_16x16x32_bf16 v[20:23], v[220:223], v[104:107], v[20:23]\n"
    "s_waitcnt vmcnt(12)\n"
    "v_max3_u32 v236, v112, v114, v116\n"
    "v_max_u32 v236, v236, v118\n"
    "v_mfma_f32_16x16x32_bf16 v[28:31], v[220:223], v[108:111], v[28:31]\n"
    "s_waitcnt lgkmcnt(4)\n"
    "ds_read_b128 v[220:223], v2 offset:37888\n"
    "v_mfma_f32_16x16x32_bf16 v[36:39], v[224:227], v[104:107], v[36:39]\n"
    "s_add_u32 s91, s22, 262144\n"
    "buffer_load_dwordx4 v[176:179], v2, s[8:11], s91 offen offset:2048 sc1\n"
    "v_cmp_eq_u32 vcc, -1, v236\n"
    "v_mfma_f32_16x16x32_bf16 v[44:47], v[224:227], v[108:111], v[44:47]\n"
    "s_waitcnt lgkmcnt(4)\n"
    "ds_read_b128 v[224:227], v2 offset:39936\n"
    "v_mfma_f32_16x16x32_bf16 v[52:55], v[228:231], v[104:107], v[52:55]\n"
    "s_cbranch_vccnz .Lf_xslow51\n"
    ".Lf_xback52:\n"
    "v_mfma_f32_16x16x32_bf16 v[60:63], v[228:231], v[108:111], v[60:63]\n"
    "s_waitcnt lgkmcnt(4)\n"
    "ds_read_b128 v[228:231], v2 offset:41984\n"
    "buffer_load_dwordx4 v[148:151], v235, s[8:11], s17 offen offset:256 sc1\n"
    "v_mfma_f32_16x16x32_bf16 v[16:19], v[208:211], v[112:115], v[16:19]\n"
    "v_mfma_f32_16x16x32_bf16 v[24:27], v[208:211], v[116:119], v[24:27]\n"
    "s_waitcnt lgkmcnt(4)\n"
    "ds_read_b128 v[208:211], v2 offset:43008\n"
    "v_mfma_f32_16x16x32_bf16 v[32:35], v[212:215], v[112:115], v[32:35]\n"
    "v_mfma_f32_16x16x32_bf16 v[40:43], v[212:215], v[116:119], v[40:43]\n"
    "s_waitcnt lgkmcnt(4)\n"
    "ds_read_b128 v[212:215], v2 offset:45056\n"
    "buffer_load_dwordx4 v[152:155], v235, s[8:11], s17 offen offset:2048 sc1\n"
    "v_mfma_f32_16x16x32_bf16 v[48:51], v[216:219], v[112:115], v[48:51]\n"
    "s_add_u32 s92, s22, 262144\n"
    "buffer_load_dwordx4 v[180:183], v2, s[8:11], s92 offen offset:3072 sc1\n"
    "v_mfma_f32_16x16x32_bf16 v[56:59], v[216:219], v[116:119], v[56:59]\n"
    "s_waitcnt lgkmcnt(4)\n"
    "ds_read_b128 v[216:219], v2 offset:47104\n"
    "v_mfma_f32_16x16x32_bf16 v[20:23], v[220:223], v[112:115], v[20:23]\n"
    "v_mfma_f32_16x16x32_bf16 v[28:31], v[220:223], v[116:119], v[28:31]\n"
    "s_waitcnt lgkmcnt(4)\n"
    "ds_read_b128 v[220:223], v2 offset:44032\n"
    "v_mfma_f32_16x16x32_bf16 v[36:39], v[224:227], v[112:115], v[36:39]\n"
    "v_mfma_f32_16x16x32_bf16 v[44:47], v[224:227], v[116:119], v[44:47]\n"
    "s_waitcnt lgkmcnt(4)\n"
    "ds_read_b128 v[224:227], v2 offset:46080\n"
    "buffer_load_dwordx4 v[156:159], v235, s[8:11], s17 offen offset:2304 sc1\n"
    "v_mfma_f32_16x16x32_bf16 v[52:55], v[228:231], v[112:115], v[52:55]\n"
    "v_mfma_f32_16x16x32_bf16 v[60:63], v[228:231], v[116:119], v[60:63]\n"
    "s_waitcnt vmcnt(15) lgkmcnt(4)\n"
    "ds_read_b128 v[228:231], v2 offset:48128\n"
    "v_mfma_f32_16x16x32_bf16 v[16:19], v[208:211], v[120:123], v[16:19]\n"
    "v_mfma_f32_16x16x32_bf16 v[24:27], v[208:211], v[124:127], v[24:27]\n"
    "s_waitcnt lgkmcnt(4)\n"
    "ds_read_b128 v[208:211], v2 offset:49152\n"
    "v_mfma_f32_16x16x32_bf16 v[32:35], v[212:215], v[120:123], v[32:35]\n"
    "v_mfma_f32_16x16x32_bf16 v[40:43], v[212:215], v[124:127], v[40:43]\n"
    "s_waitcnt lgkmcnt(4)\n"
    "ds_read_b128 v[212:215], v2 offset:51200\n"
    "buffer_load_dwordx4 v[160:163], v235, s[8:11], s18 offen offset:0 sc1\n"
    "v_mfma_f32_16x16x32_bf16 v[48:51], v[216:219], v[120:123], v[48:51]\n"
    "s_waitcnt vmcnt(12)\n"
    "s_mov_b64 exec, 1\n"
    "ds_add_rtn_u32 v9, v3, v3 offset:57356\n"
    "s_mov_b64 exec, -1\n"
    "v_mfma_f32_16x16x32_bf16 v[56:59], v[216:219], v[124:127], v[56:59]\n"
    "s_waitcnt lgkmcnt(5)\n"
    "ds_read_b128 v[216:219], v2 offset:53248\n"
    "v_mfma_f32_16x16x32_bf16 v[20:23], v[220:223], v[120:123], v[20:23]\n"
    "v_mfma_f32_16x16x32_bf16 v[28:31], v[220:223], v[124:127], v[28:31]\n"
    "s_waitcnt lgkmcnt(5)\n"
    "ds_read_b128 v[220:223], v2 offset:50176\n"
    "buffer_load_dwordx4 v[164:167], v235, s[8:11], s18 offen offset:256 sc1\n"
    "v_mfma_f32_16x16x32_bf16 v[36:39], v[224:227], v[120:123], v[36:39]\n"
    "v_mfma_f32_16x16x32_bf16 v[44:47], v[224:227], v[124:127], v[44:47]\n"
    "s_waitcnt lgkmcnt(5)\n"
    "ds_read_b128 v[224:227], v2 offset:52224\n"
    "v_mfma_f32_16x16x32_bf16 v[52:55], v[228:231], v[120:123], v[52:55]\n"
    "s_waitcnt lgkmcnt(3)\n"
    "v_readfirstlane_b32 s79, v9\n"
    "v_mfma_f32_16x16x32_bf16 v[60:63], v[228:231], v[124:127], v[60:63]\n"
    "s_waitcnt vmcnt(12)\n"
    "ds_read_b128 v[228:231], v2 offset:54272\n"
    "v_mfma_f32_16x16x32_bf16 v[16:19], v[208:211], v[128:131], v[16:19]\n"
    "s_add_u32 s65, s37, 65536\n"
    "s_cmp_eq_u32 s79, 0x30000\n"
    "s_cselect_b64 exec, 0xff, 0\n"
    "buffer_store_dwordx4 v[252:255], v2, s[8:11], s65 offen sc1\n"
    "s_mov_b64 exec, -1\n"
    "v_mfma_f32_16x16x32_bf16 v[24:27], v[208:211], v[132:135], v[24:27]\n"
    "ds_read_b128 v[208:211], v2 offset:55296\n"
    "buffer_load_dwordx4 a[240:243], v235, s[8:11], s18 offen offset:2048 sc1\n"
    "v_mfma_f32_16x16x32_bf16 v[32:35], v[212:215], v[128:131], v[32:35]\n"
    "v_mfma_f32_16x16x32_bf16 v[40:43], v[212:215], v[132:135], v[40:43]\n"
    "s_waitcnt lgkmcnt(4)\n"
    "ds_read_b128 v[212:215], v2 offset:57344\n"
    "v_mfma_f32_16x16x32_bf16 v[48:51], v[216:219], v[128:131], v[48:51]\n"
    "v_mfma_f32_16x16x32_bf16 v[56:59], v[216:219], v[132:135], v[56:59]\n"
    "s_waitcnt lgkmcnt(4)\n"
    "ds_read_b128 v[216:219], v2 offset:56320\n"
    "v_mfma_f32_16x16x32_bf16 v[20:23], v[220:223], v[128:131], v[20:23]\n"
    "v_mfma_f32_16x16x32_bf16 v[28:31], v[220:223], v[132:135], v[28:31]\n"
    "s_waitcnt lgkmcnt(4)\n"
    "ds_read_b128 v[220:223], v2 offset:59392\n"
    "buffer_load_dwordx4 a[244:247], v235, s[8:11], s18 offen offset:2304 sc1\n"
    "v_mfma_f32_16x16x32_bf16 v[36:39], v[224:227], v[128:131], v[36:39]\n"
    "v_mfma_f32_16x16x32_bf16 v[44:47], v[224:227], v[132:135], v[44:47]\n"
    "s_waitcnt lgkmcnt(4)\n"
    "ds_read_b128 v[224:227], v2 offset:61440\n"
    "v_mfma_f32_16x16x32_bf16 v[52:55], v[228:231], v[128:131], v[52:55]\n"
    "s_waitcnt vmcnt(11)\n"
    "v_add_f32 v240, v168, v172\n"
    "v_add_f32 v241, v169, v173\n"
    "v_mfma_f32_16x16x32_bf16 v[60:63], v[228:231], v[132:135], v[60:63]\n"
    "s_waitcnt lgkmcnt(4)\n"
    "ds_read_b128 v[228:231], v2 offset:58368\n"
    "buffer_load_dwordx4 a[248:251], v235, s[8:11], s19 offen offset:0 sc1\n"
    "v_mfma_f32_16x16x32_bf16 v[16:19], v[208:211], v[136:139], v[16:19]\n"
    "v_add_f32 v242, v170, v174\n"
    "v_add_f32 v243, v171, v175\n"
    "v_mfma_f32_16x16x32_bf16 v[24:27], v[208:211], v[140:143], v[24:27]\n"
    "s_waitcnt lgkmcnt(4)\n"
    "ds_read_b128 v[208:211], v2 offset:62464\n"
    "v_mfma_f32_16x16x32_bf16 v[32:35], v[212:215], v[136:139], v[32:35]\n"
    "s_waitcnt vmcnt(10)\n"
    "v_add_f32 v240, v240, v176\n"
    "v_add_f32 v241, v241, v177\n"
    "v_mfma_f32_16x16x32_bf16 v[40:43], v[212:215], v[140:143], v[40:43]\n"
    "s_waitcnt lgkmcnt(4)\n"
    "ds_read_b128 v[212:215], v2 offset:60416\n"
    "v_mfma_f32_16x16x32_bf16 v[20:23], v[216:219], v[136:139], v[20:23]\n"
    "v_add_f32 v242, v242, v178\n"
    "v_add_f32 v243, v243, v179\n"
    "v_mfma_f32_16x16x32_bf16 v[28:31], v[216:219], v[140:143], v[28:31]\n"
    "s_waitcnt lgkmcnt(4)\n"
    "ds_read_b128 v[216:219], v3 offset:2048\n"
    "buffer_load_dwordx4 a[252:255], v235, s[8:11], s19 offen offset:256 sc1\n"
    "v_mfma_f32_16x16x32_bf16 v[48:51], v[220:223], v[136:139], v[48:51]\n"
    "s_waitcnt vmcnt(8)\n"
    "v_add_f32 v240, v240, v180\n"
    "v_add_f32 v241, v241, v181\n"
    "v_mfma_f32_16x16x32_bf16 v[56:59], v[220:223], v[140:143], v[56:59]\n"
    "s_waitcnt lgkmcnt(4)\n"
    "ds_read_b128 v[220:223], v2 offset:63488\n"
    "v_mfma_f32_16x16x32_bf16 v[16:19], v[224:227], v[144:147], v[16:19]\n"
    "v_add_f32 v242, v242, v182\n"
    "v_add_f32 v243, v243, v183\n"
    "v_mfma_f32_16x16x32_bf16 v[24:27], v[224:227], v[148:151], v[24:27]\n"
    "s_waitcnt lgkmcnt(4)\n"
    "ds_read_b128 v[224:227], v3 offset:3072\n"
    "v_permlane16_swap_b32 v240, v242\n"
    "v_mfma_f32_16x16x32_bf16 v[36:39], v[228:231], v[136:139], v[36:39]\n"
    "v_permlane16_swap_b32 v241, v243\n"
    "v_mfma_f32_16x16x32_bf16 v[44:47], v[228:231], v[140:143], v[44:47]\n"
    "s_waitcnt lgkmcnt(4)\n"
    "ds_read_b128 v[228:231], v3 offset:0\n"
    "buffer_load_dwordx4 v[168:171], v235, s[8:11], s19 offen offset:2048 sc1\n"
    "v_mfma_f32_16x16x32_bf16 v[20:23], v[208:211], v[144:147], v[20:23]\n"
    "v_add_f32 v240, v240, v242\n"
    "v_mfma_f32_16x16x32_bf16 v[28:31], v[208:211], v[148:151], v[28:31]\n"
    "s_waitcnt lgkmcnt(4)\n"
    "ds_read_b128 v[208:211], v3 offset:8192\n"
    "v_add_f32 v241, v241, v243\n"
    "v_mfma_f32_16x16x32_bf16 v[52:55], v[212:215], v[136:139], v[52:55]\n"
    "s_nop 0\n"
    "v_permlane32_swap_b32 v240, v241\n"
    "v_mfma_f32_16x16x32_bf16 v[60:63], v[212:215], v[140:143], v[60:63]\n"
    "s_waitcnt vmcnt(8) lgkmcnt(4)\n"
    "ds_read_b128 v[212:215], v2 offset:64512\n"
    "buffer_load_dwordx4 v[172:175], v235, s[8:11], s19 offen offset:2304 sc1\n"
    "v_mfma_f32_16x16x32_bf16 v[16:19], v[216:219], v[152:155], v[16:19]\n"
    "v_add_f32 v240, v240, v241\n"
    "v_mfma_f32_16x16x32_bf16 v[24:27], v[216:219], v[156:159], v[24:27]\n"
    "s_waitcnt lgkmcnt(4)\n"
    "ds_read_b128 v[216:219], v3 offset:9216\n"
    "v_mov_b32 v241, v240\n"
    "v_mfma_f32_16x16x32_bf16 v[32:35], v[220:223], v[144:147], v[32:35]\n"
    "s_nop 0\n"
    "v_permlane32_swap_b32 v240, v241\n"
    "v_mfma_f32_16x16x32_bf16 v[40:43], v[220:223], v[148:151], v[40:43]\n"
    "s_waitcnt lgkmcnt(4)\n"
    "ds_read_b128 v[220:223], v3 offset:1024\n"
    "v_mul_f32 v240, 0x3acccccd, v240\n"
    "v_mfma_f32_16x16x32_bf16 v[20:23], v[224:227], v[152:155], v[20:23]\n"
    "v_mul_f32 v241, 0x3acccccd, v241\n"
    "v_mfma_f32_16x16x32_bf16 v[28:31], v[224:227], v[156:159], v[28:31]\n"
    "s_waitcnt lgkmcnt(4)\n"
    "ds_read_b128 v[224:227], v3 offset:14336\n"
    "buffer_load_dwordx4 v[176:179], v235, s[8:11], s20 offen offset:0 sc1\n"
    "v_mfma_f32_16x16x32_bf16 v[48:51], v[228:231], v[144:147], v[48:51]\n"
    "v_fma_f32 v241, -v240, v240, v241\n"
    "v_mfma_f32_16x16x32_bf16 v[56:59], v[228:231], v[148:151], v[56:59]\n"
    "s_waitcnt vmcnt(8) lgkmcnt(4)\n"
    "ds_read_b128 v[228:231], v3 offset:4096\n"
    "v_max_f32 v241, 0, v241\n"
    "v_mfma_f32_16x16x32_bf16 v[16:19], v[208:211], v[160:163], v[16:19]\n"
    "v_cvt_pk_bf16_f32 v241, v241, v241\n"
    "v_mfma_f32_16x16x32_bf16 v[24:27], v[208:211], v[164:167], v[24:27]\n"
    "s_waitcnt lgkmcnt(4)\n"
    "ds_read_b128 v[208:211], v3 offset:15360\n"
    "v_lshlrev_b32 v241, 16, v241\n"
    "v_mfma_f32_16x16x32_bf16 v[36:39], v[212:215], v[144:147], v[36:39]\n"
    "v_add_f32 v241, 0x3727c5ac, v241\n"
    "v_mfma_f32_16x16x32_bf16 v[44:47], v[212:215], v[148:151], v[44:47]\n"
    "s_waitcnt lgkmcnt(4)\n"
    "ds_read_b128 v[212:215], v3 offset:6144\n"
    "buffer_load_dwordx4 v[180:183], v235, s[8:11], s20 offen offset:256 sc1\n"
    "v_mfma_f32_16x16x32_bf16 v[20:23], v[216:219], v[160:163], v[20:23]\n"
    "v_cvt_pk_bf16_f32 v241, v241, v241\n"
    "v_mfma_f32_16x16x32_bf16 v[28:31], v[216:219], v[164:167], v[28:31]\n"
    "s_waitcnt lgkmcnt(4)\n"
    "ds_read_b128 v[216:219], v3 offset:20480\n"
    "v_lshlrev_b32 v241, 16, v241\n"
    "v_mfma_f32_16x16x32_bf16 v[52:55], v[220:223], v[144:147], v[52:55]\n"
    "v_sqrt_f32 v241, v241\n"
    "v_mfma_f32_16x16x32_bf16 v[60:63], v[220:223], v[148:151], v[60:63]\n"
    "s_waitcnt vmcnt(6) lgkmcnt(4)\n"
    "ds_read_b128 v[220:223], v3 offset:5120\n"
    "buffer_load_dwordx4 v[184:187], v235, s[8:11], s20 offen offset:2048 sc1\n"
    "v_mfma_f32_16x16x32_bf16 v[16:19], v[224:227], a[240:243], v[16:19]\n"
    "v_cvt_pk_bf16_f32 v241, v241, v241\n"
    "v_mfma_f32_16x16x32_bf16 v[24:27], v[224:227], a[244:247], v[24:27]\n"
    "s_waitcnt lgkmcnt(4)\n"
    "ds_read_b128 v[224:227], v3 offset:21504\n"
    "v_lshlrev_b32 v241, 16, v241\n"
    "v_mfma_f32_16x16x32_bf16 v[32:35], v[228:231], v[152:155], v[32:35]\n"
    "v_rcp_f32 v244, v241\n"
    "v_mfma_f32_16x16x32_bf16 v[40:43], v[228:231], v[156:159], v[40:43]\n"
    "s_waitcnt lgkmcnt(4)\n"
    "ds_read_b128 v[228:231], v3 offset:7168\n"
    "v_mul_f32 v240, -0.5, v240\n"
    "v_mfma_f32_16x16x32_bf16 v[20:23], v[208:211], a[240:243], v[20:23]\n"
    "v_mul_f32 v245, v241, v241\n"
    "v_mfma_f32_16x16x32_bf16 v[28:31], v[208:211], a[244:247], v[28:31]\n"
    "s_waitcnt lgkmcnt(4)\n"
    "ds_read_b128 v[208:211], v3 offset:26624\n"
    "buffer_load_dwordx4 v[188:191], v235, s[8:11], s20 offen offset:2304 sc1\n"
    "v_mfma_f32_16x16x32_bf16 v[48:51], v[212:215], v[152:155], v[48:51]\n"
    "v_mul_f32 v247, v241, v241\n"
    "v_mfma_f32_16x16x32_bf16 v[56:59], v[212:215], v[156:159], v[56:59]\n"
    "s_waitcnt vmcnt(6) lgkmcnt(4)\n"
    "ds_read_b128 v[212:215], v3 offset:10240\n"
    "v_mov_b32 v248, v240\n"
    "v_mfma_f32_16x16x32_bf16 v[16:19], v[216:219], a[248:251], v[16:19]\n"
    "v_mul_f32 v246, 0xbfb8aa3b, v244\n"
    "v_mfma_f32_16x16x32_bf16 v[24:27], v[216:219], a[252:255], v[24:27]\n"
    "s_waitcnt lgkmcnt(4)\n"
    "ds_read_b128 v[216:219], v3 offset:27648\n"
    "v_mul_f32 v244, 0xbfb8aa3b, v244\n"
    "v_mfma_f32_16x16x32_bf16 v[36:39], v[220:223], v[152:155], v[36:39]\n"
    "v_permlane16_swap_b32 v240, v248\n"
    "v_mfma_f32_16x16x32_bf16 v[44:47], v[220:223], v[156:159], v[44:47]\n"
    "s_waitcnt lgkmcnt(4)\n"
    "ds_read_b128 v[220:223], v3 offset:12288\n"
    "buffer_load_dwordx4 v[192:195], v235, s[8:11], s40 offen offset:0 sc1\n"
    "v_mfma_f32_16x16x32_bf16 v[20:23], v[224:227], a[248:251], v[20:23]\n"
    "v_permlane16_swap_b32 v245, v247\n"
    "v_mfma_f32_16x16x32_bf16 v[28:31], v[224:227], a[252:255], v[28:31]\n"
    "s_waitcnt lgkmcnt(4)\n"
    "ds_read_b128 v[224:227], v3 offset:32768\n"
    "v_permlane16_swap_b32 v244, v246\n"
    "v_mfma_f32_16x16x32_bf16 v[52:55], v[228:231], v[152:155], v[52:55]\n"
    "v_cvt_pk_bf16_f32 v243, v240, v240\n"
    "v_mfma_f32_16x16x32_bf16 v[60:63], v[228:231], v[156:159], v[60:63]\n"
    "s_waitcnt vmcnt(5) lgkmcnt(4)\n"
    "ds_read_b128 v[228:231], v3 offset:11264\n"
    "buffer_load_dwordx4 v[196:199], v235, s[8:11], s40 offen offset:256 sc1\n"
    "v_mfma_f32_16x16x32_bf16 v[16:19], v[208:211], v[168:171], v[16:19]\n"
    "v_cvt_pk_bf16_f32 v251, v248, v248\n"
    "v_mfma_f32_16x16x32_bf16 v[24:27], v[208:211], v[172:175], v[24:27]\n"
    "s_waitcnt lgkmcnt(4)\n"
    "ds_read_b128 v[208:211], v3 offset:33792\n"
    "v_cvt_pk_bf16_f32 v242, v240, v240\n"
    "v_mfma_f32_16x16x32_bf16 v[32:35], v[212:215], v[160:163], v[32:35]\n"
    "v_cvt_pk_bf16_f32 v250, v248, v248\n"
    "v_mfma_f32_16x16x32_bf16 v[40:43], v[212:215], v[164:167], v[40:43]\n"
    "s_waitcnt lgkmcnt(4)\n"
    "ds_read_b128 v[212:215], v3 offset:13312\n"
    "v_cvt_pk_bf16_f32 v241, v240, v240\n"
    "v_mfma_f32_16x16x32_bf16 v[20:23], v[216:219], v[168:171], v[20:23]\n"
    "s_add_u32 s83, s23, 196608\n"
    "buffer_load_dwordx2 v[238:239], v4, s[8:11], s83 offen sc1\n"
    "v_cvt_pk_bf16_f32 v249, v248, v248\n"
    "v_mfma_f32_16x16x32_bf16 v[28:31], v[216:219], v[172:175], v[28:31]\n"
    "s_waitcnt lgkmcnt(4)\n"
    "ds_read_b128 v[216:219], v3 offset:38912\n"
    "buffer_load_dwordx4 v[200:203], v235, s[8:11], s40 offen offset:2048 sc1\n"
    "v_mfma_f32_16x16x32_bf16 v[48:51], v[220:223], v[160:163], v[48:51]\n"
    "v_cvt_pk_bf16_f32 v240, v240, v240\n"
    "v_mfma_f32_16x16x32_bf16 v[56:59], v[220:223], v[164:167], v[56:59]\n"
    "s_waitcnt vmcnt(6) lgkmcnt(4)\n"
    "ds_read_b128 v[220:223], v3 offset:16384\n"
    "v_cvt_pk_bf16_f32 v248, v248, v248\n"
    "v_mfma_f32_16x16x32_bf16 v[16:19], v[224:227], v[176:179], v[16:19]\n"
    "v_mfma_f32_16x16x32_bf16 v[24:27], v[224:227], v[180:183], v[24:27]\n"
    "s_waitcnt lgkmcnt(4)\n"
    "ds_read_b128 v[224:227], v3 offset:39936\n"
    "v_mfma_f32_16x16x32_bf16 v[36:39], v[228:231], v[160:163], v[36:39]\n"
    "v_mfma_f32_16x16x32_bf16 v[44:47], v[228:231], v[164:167], v[44:47]\n"
    "s_waitcnt lgkmcnt(4)\n"
    "ds_read_b128 v[228:231], v3 offset:18432\n"
    "buffer_load_dwordx4 v[204:207], v235, s[8:11], s40 offen offset:2304 sc1\n"
    "v_mfma_f32_16x16x32_bf16 v[20:23], v[208:211], v[176:179], v[20:23]\n"
    "v_add_u32 v235, 1310720, v235\n"
    "v_mfma_f32_16x16x32_bf16 v[28:31], v[208:211], v[180:183], v[28:31]\n"
    "s_waitcnt lgkmcnt(4)\n"
    "ds_read_b128 v[208:211], v3 offset:45056\n"
    "v_mfma_f32_16x16x32_bf16 v[52:55], v[212:215], v[160:163], v[52:55]\n"
    "v_mfma_f32_16x16x32_bf16 v[60:63], v[212:215], v[164:167], v[60:63]\n"
    "s_waitcnt vmcnt(5) lgkmcnt(4)\n"
    "ds_read_b128 v[212:215], v3 offset:17408\n"
    "v_mfma_f32_16x16x32_bf16 v[16:19], v[216:219], v[184:187], v[16:19]\n"
    "v_mfma_f32_16x16x32_bf16 v[24:27], v[216:219], v[188:191], v[24:27]\n"
    "s_waitcnt lgkmcnt(4)\n"
    "ds_read_b128 v[216:219], v3 offset:46080\n"
    "v_mfma_f32_16x16x32_bf16 v[32:35], v[220:223], a[240:243], v[32:35]\n"
    "v_mfma_f32_16x16x32_bf16 v[40:43], v[220:223], a[244:247], v[40:43]\n"
    "s_waitcnt lgkmcnt(4)\n"
    "ds_read_b128 v[220:223], v3 offset:19456\n"
    "v_mfma_f32_16x16x32_bf16 v[20:23], v[224:227], v[184:187], v[20:23]\n"
    "v_mfma_f32_16x16x32_bf16 v[28:31], v[224:227], v[188:191], v[28:31]\n"
    "s_waitcnt lgkmcnt(4)\n"
    "ds_read_b128 v[224:227], v3 offset:51200\n"
    "v_mfma_f32_16x16x32_bf16 v[48:51], v[228:231], a[240:243], v[48:51]\n"
    "v_mfma_f32_16x16x32_bf16 v[56:59], v[228:231], a[244:247], v[56:59]\n"
    "s_waitcnt vmcnt(3) lgkmcnt(4)\n"
    "ds_read_b128 v[228:231], v3 offset:52224\n"
    "v_mfma_f32_16x16x32_bf16 v[16:19], v[208:211], v[192:195], v[16:19]\n"
    "v_mfma_f32_16x16x32_bf16 v[24:27], v[208:211], v[196:199], v[24:27]\n"
    "s_waitcnt lgkmcnt(4)\n"
    "ds_read_b128 v[208:211], v3 offset:57472\n"
    "v_mfma_f32_16x16x32_bf16 v[36:39], v[212:215], a[240:243], v[36:39]\n"
    "v_mfma_f32_16x16x32_bf16 v[44:47], v[212:215], a[244:247], v[44:47]\n"
    "s_waitcnt lgkmcnt(4)\n"
    "ds_read_b128 v[212:215], v3 offset:58496\n"
    "v_mfma_f32_16x16x32_bf16 v[20:23], v[216:219], v[192:195], v[20:23]\n"
    "v_mfma_f32_16x16x32_bf16 v[28:31], v[216:219], v[196:199], v[28:31]\n"
    "s_waitcnt lgkmcnt(4)\n"
    "ds_read_b128 v[216:219], v3 offset:22528\n"
    "v_mfma_f32_16x16x32_bf16 v[52:55], v[220:223], a[240:243], v[52:55]\n"
    "v_mfma_f32_16x16x32_bf16 v[60:63], v[220:223], a[244:247], v[60:63]\n"
    "s_waitcnt vmcnt(0) lgkmcnt(4)\n"
    "ds_read_b128 v[220:223], v3 offset:23552\n"
    "v_mfma_f32_16x16x32_bf16 v[16:19], v[224:227], v[200:203], v[16:19]\n"
    "v_mfma_f32_16x16x32_bf16 v[24:27], v[224:227], v[204:207], v[24:27]\n"
    "s_waitcnt lgkmcnt(4)\n"
    "ds_read_b128 v[224:227], v3 offset:28672\n"
    "v_mfma_f32_16x16x32_bf16 v[20:23], v[228:231], v[200:203], v[20:23]\n"
    "v_mfma_f32_16x16x32_bf16 v[28:31], v[228:231], v[204:207], v[28:31]\n"
    "s_waitcnt lgkmcnt(4)\n"
    "ds_read_b128 v[228:231], v3 offset:29696\n"
    "v_mfma_f32_16x16x32_bf16 v[16:19], v[208:211], v[240:243], v[16:19]\n"
    "v_cmp_eq_u64 s[86:87], s[50:51], v[238:239]\n"
    "v_mfma_f32_16x16x32_bf16 v[24:27], v[208:211], v[248:251], v[24:27]\n"
    "s_waitcnt lgkmcnt(4)\n"
    "ds_read_b128 v[208:211], v3 offset:34816\n"
    "v_mfma_f32_16x16x32_bf16 v[20:23], v[212:215], v[240:243], v[20:23]\n"
    "s_andn2_b32 s79, 0xff, s86\n"
    "s_cbranch_scc1 .Lf_slow53\n"
    ".Lf_back54:\n"
    "v_mfma_f32_16x16x32_bf16 v[28:31], v[212:215], v[248:251], v[28:31]\n"
    "s_waitcnt lgkmcnt(4)\n"
    "ds_read_b128 v[212:215], v3 offset:35840\n"
    "buffer_load_dwordx4 v[64:67], v235, s[8:11], s12 offen offset:0 sc1\n"
    "v_mfma_f32_16x16x32_bf16 v[32:35], v[216:219], a[248:251], v[32:35]\n"
    "v_mfma_f32_16x16x32_bf16 v[40:43], v[216:219], a[252:255], v[40:43]\n"
    "s_waitcnt lgkmcnt(4)\n"
    "ds_read_b128 v[216:219], v3 offset:40960\n"
    "v_mul_f32 v20, v16, v20\n"
    "v_mul_f32 v16, v244, v16\n"
    "v_mfma_f32_16x16x32_bf16 v[36:39], v[220:223], a[248:251], v[36:39]\n"
    "v_exp_f32 v16, v16\n"
    "v_mul_f32 v21, v17, v21\n"
    "v_mfma_f32_16x16x32_bf16 v[44:47], v[220:223], a[252:255], v[44:47]\n"
    "s_waitcnt lgkmcnt(4)\n"
    "ds_read_b128 v[220:223], v3 offset:41984\n"
    "buffer_load_dwordx4 v[68:71], v235, s[8:11], s12 offen offset:256 sc1\n"
    "v_fma_f32 v16, v16, v245, v245\n"
    "v_mul_f32 v17, v244, v17\n"
    "v_mul_f32 v22, v18, v22\n"
    "v_mfma_f32_16x16x32_bf16 v[32:35], v[224:227], v[168:171], v[32:35]\n"
    "v_rcp_f32 v16, v16\n"
    "v_mul_f32 v18, v244, v18\n"
    "v_mfma_f32_16x16x32_bf16 v[40:43], v[224:227], v[172:175], v[40:43]\n"
    "s_waitcnt lgkmcnt(4)\n"
    "ds_read_b128 v[224:227], v3 offset:47104\n"
    "v_mul_f32 v16, v16, v20\n"
    "v_mul_f32 v23, v19, v23\n"
    "v_mfma_f32_16x16x32_bf16 v[36:39], v[228:231], v[168:171], v[36:39]\n"
    "v_exp_f32 v17, v17\n"
    "v_mul_f32 v19, v244, v19\n"
    "v_mfma_f32_16x16x32_bf16 v[44:47], v[228:231], v[172:175], v[44:47]\n"
    "s_waitcnt lgkmcnt(4)\n"
    "ds_read_b128 v[228:231], v3 offset:48128\n"
    "buffer_load_dwordx4 v[72:75], v235, s[8:11], s12 offen offset:2048 sc1\n"
    "v_fma_f32 v17, v17, v245, v245\n"
    "v_mul_f32 v28, v24, v28\n"
    "v_mul_f32 v24, v246, v24\n"
    "v_mfma_f32_16x16x32_bf16 v[32:35], v[208:211], v[176:179], v[32:35]\n"
    "v_rcp_f32 v17, v17\n"
    "v_mul_f32 v29, v25, v29\n"
    "v_mfma_f32_16x16x32_bf16 v[40:43], v[208:211], v[180:183], v[40:43]\n"
    "s_waitcnt lgkmcnt(4)\n"
    "ds_read_b128 v[208:211], v3 offset:53248\n"
    "v_mul_f32 v17, v17, v21\n"
    "v_mul_f32 v25, v246, v25\n"
    "v_mfma_f32_16x16x32_bf16 v[36:39], v[212:215], v[176:179], v[36:39]\n"
    "v_exp_f32 v18, v18\n"
    "v_mul_f32 v30, v26, v30\n"
    "v_mfma_f32_16x16x32_bf16 v[44:47], v[212:215], v[180:183], v[44:47]\n"
    "s_waitcnt lgkmcnt(4)\n"
    "ds_read_b128 v[212:215], v3 offset:54272\n"
    "buffer_load_dwordx4 v[76:79], v235, s[8:11], s12 offen offset:2304 sc1\n"
    "v_fma_f32 v18, v18, v245, v245\n"
    "v_mul_f32 v26, v246, v26\n"
    "v_mul_f32 v31, v27, v31\n"
    "v_mfma_f32_16x16x32_bf16 v[32:35], v[216:219], v[184:187], v[32:35]\n"
    "v_rcp_f32 v18, v18\n"
    "v_mul_f32 v27, v246, v27\n"
    "v_mfma_f32_16x16x32_bf16 v[40:43], v[216:219], v[188:191], v[40:43]\n"
    "s_waitcnt lgkmcnt(4)\n"
    "ds_read_b128 v[216:219], v3 offset:59520\n"
    "v_mul_f32 v18, v18, v22\n"
    "v_cvt_pk_bf16_f32 v20, v16, v17\n"
    "v_exp_f32 v19, v19\n"
    "v_mfma_f32_16x16x32_bf16 v[36:39], v[220:223], v[184:187], v[36:39]\n"
    "v_exp_f32 v24, v24\n"
    "v_mfma_f32_16x16x32_bf16 v[44:47], v[220:223], v[188:191], v[44:47]\n"
    "s_waitcnt lgkmcnt(4)\n"
    "ds_read_b128 v[220:223], v3 offset:60544\n"
    "buffer_load_dwordx4 v[80:83], v235, s[8:11], s13 offen offset:0 sc1\n"
    "v_fma_f32 v19, v19, v245, v245\n"
    "v_fma_f32 v24, v24, v247, v247\n"
    "v_mfma_f32_16x16x32_bf16 v[32:35], v[224:227], v[192:195], v[32:35]\n"
    "v_rcp_f32 v19, v19\n"
    "v_rcp_f32 v24, v24\n"
    "v_mfma_f32_16x16x32_bf16 v[40:43], v[224:227], v[196:199], v[40:43]\n"
    "s_waitcnt lgkmcnt(4)\n"
    "ds_read_b128 v[224:227], v3 offset:24576\n"
    "v_mul_f32 v19, v19, v23\n"
    "v_mul_f32 v24, v24, v28\n"
    "v_mfma_f32_16x16x32_bf16 v[36:39], v[228:231], v[192:195], v[36:39]\n"
    "v_exp_f32 v25, v25\n"
    "v_mfma_f32_16x16x32_bf16 v[44:47], v[228:231], v[196:199], v[44:47]\n"
    "s_waitcnt lgkmcnt(4)\n"
    "ds_read_b128 v[228:231], v3 offset:25600\n"
    "buffer_load_dwordx4 v[84:87], v235, s[8:11], s13 offen offset:256 sc1\n"
    "v_fma_f32 v25, v25, v247, v247\n"
    "v_cvt_pk_bf16_f32 v21, v18, v19\n"
    "v_exp_f32 v26, v26\n"
    "v_mfma_f32_16x16x32_bf16 v[32:35], v[208:211], v[200:203], v[32:35]\n"
    "v_rcp_f32 v25, v25\n"
    "v_mfma_f32_16x16x32_bf16 v[40:43], v[208:211], v[204:207], v[40:43]\n"
    "s_waitcnt lgkmcnt(4)\n"
    "ds_read_b128 v[208:211], v3 offset:30720\n"
    "v_mul_f32 v25, v25, v29\n"
    "v_fma_f32 v26, v26, v247, v247\n"
    "v_exp_f32 v27, v27\n"
    "v_mfma_f32_16x16x32_bf16 v[36:39], v[212:215], v[200:203], v[36:39]\n"
    "v_rcp_f32 v26, v26\n"
    "v_mfma_f32_16x16x32_bf16 v[44:47], v[212:215], v[204:207], v[44:47]\n"
    "s_waitcnt lgkmcnt(4)\n"
    "ds_read_b128 v[212:215], v3 offset:31744\n"
    "buffer_load_dwordx4 v[88:91], v235, s[8:11], s13 offen offset:2048 sc1\n"
    "v_mul_f32 v26, v26, v30\n"
    "v_fma_f32 v27, v27, v247, v247\n"
    "v_mfma_f32_16x16x32_bf16 v[32:35], v[216:219], v[240:243], v[32:35]\n"
    "v_rcp_f32 v27, v27\n"
    "v_cvt_pk_bf16_f32 v22, v24, v25\n"
    "v_mfma_f32_16x16x32_bf16 v[40:43], v[216:219], v[248:251], v[40:43]\n"
    "s_waitcnt lgkmcnt(4)\n"
    "ds_read_b128 v[216:219], v3 offset:36864\n"
    "v_mul_f32 v27, v27, v31\n"
    "v_permlane16_swap_b32 v20, v22\n"
    "v_mfma_f32_16x16x32_bf16 v[36:39], v[220:223], v[240:243], v[36:39]\n"
    "v_cvt_pk_bf16_f32 v23, v26, v27\n"
    "v_mfma_f32_16x16x32_bf16 v[44:47], v[220:223], v[248:251], v[44:47]\n"
    "s_waitcnt lgkmcnt(4)\n"
    "ds_read_b128 v[220:223], v3 offset:37888\n"
    "buffer_load_dwordx4 v[92:95], v235, s[8:11], s13 offen offset:2304 sc1\n"
    "v_permlane16_swap_b32 v21, v23\n"
    "v_mfma_f32_16x16x32_bf16 v[48:51], v[224:227], a[248:251], v[48:51]\n"
    "s_add_u32 s69, s24, 6291456\n"
    "buffer_store_dwordx4 v[20:23], v10, s[8:11], s69 offen sc1\n"
    "v_mfma_f32_16x16x32_bf16 v[56:59], v[224:227], a[252:255], v[56:59]\n"
    "s_waitcnt lgkmcnt(4)\n"
    "ds_read_b128 v[224:227], v3 offset:43008\n"
    "v_mul_f32 v36, v32, v36\n"
    "v_mul_f32 v32, v244, v32\n"
    "v_mfma_f32_16x16x32_bf16 v[52:55], v[228:231], a[248:251], v[52:55]\n"
    "v_exp_f32 v32, v32\n"
    "v_mul_f32 v37, v33, v37\n"
    "v_mfma_f32_16x16x32_bf16 v[60:63], v[228:231], a[252:255], v[60:63]\n"
    "s_waitcnt lgkmcnt(4)\n"
    "ds_read_b128 v[228:231], v3 offset:44032\n"
    "buffer_load_dwordx4 v[96:99], v235, s[8:11], s14 offen offset:0 sc1\n"
    "v_fma_f32 v32, v32, v245, v245\n"
    "v_mul_f32 v33, v244, v33\n"
    "v_mul_f32 v38, v34, v38\n"
    "v_mfma_f32_16x16x32_bf16 v[48:51], v[208:211], v[168:171], v[48:51]\n"
    "v_rcp_f32 v32, v32\n"
    "v_mul_f32 v34, v244, v34\n"
    "v_mfma_f32_16x16x32_bf16 v[56:59], v[208:211], v[172:175], v[56:59]\n"
    "s_waitcnt lgkmcnt(4)\n"
    "ds_read_b128 v[208:211], v3 offset:49152\n"
    "v_mul_f32 v32, v32, v36\n"
    "v_mul_f32 v39, v35, v39\n"
    "v_mfma_f32_16x16x32_bf16 v[52:55], v[212:215], v[168:171], v[52:55]\n"
    "v_exp_f32 v33, v33\n"
    "v_mul_f32 v35, v244, v35\n"
    "v_mfma_f32_16x16x32_bf16 v[60:63], v[212:215], v[172:175], v[60:63]\n"
    "s_waitcnt lgkmcnt(4)\n"
    "ds_read_b128 v[212:215], v3 offset:50176\n"
    "buffer_load_dwordx4 v[100:103], v235, s[8:11], s14 offen offset:256 sc1\n"
    "v_fma_f32 v33, v33, v245, v245\n"
    "v_mul_f32 v44, v40, v44\n"
    "v_mul_f32 v40, v246, v40\n"
    "v_mfma_f32_16x16x32_bf16 v[48:51], v[216:219], v[176:179], v[48:51]\n"
    "v_rcp_f32 v33, v33\n"
    "v_mul_f32 v45, v41, v45\n"
    "v_mfma_f32_16x16x32_bf16 v[56:59], v[216:219], v[180:183], v[56:59]\n"
    "s_waitcnt lgkmcnt(4)\n"
    "ds_read_b128 v[216:219], v3 offset:55296\n"
    "v_mul_f32 v33, v33, v37\n"
    "v_mul_f32 v41, v246, v41\n"
    "v_mfma_f32_16x16x32_bf16 v[52:55], v[220:223], v[176:179], v[52:55]\n"
    "v_exp_f32 v34, v34\n"
    "v_mul_f32 v46, v42, v46\n"
    "v_mfma_f32_16x16x32_bf16 v[60:63], v[220:223], v[180:183], v[60:63]\n"
    "s_waitcnt lgkmcnt(4)\n"
    "ds_read_b128 v[220:223], v3 offset:56320\n"
    "buffer_load_dwordx4 v[104:107], v235, s[8:11], s14 offen offset:2048 sc1\n"
    "v_fma_f32 v34, v34, v245, v245\n"
    "v_mul_f32 v42, v246, v42\n"
    "v_mul_f32 v47, v43, v47\n"
    "v_mfma_f32_16x16x32_bf16 v[48:51], v[224:227], v[184:187], v[48:51]\n"
    "v_rcp_f32 v34, v34\n"
    "v_mul_f32 v43, v246, v43\n"
    "v_mfma_f32_16x16x32_bf16 v[56:59], v[224:227], v[188:191], v[56:59]\n"
    "s_waitcnt lgkmcnt(4)\n"
    "ds_read_b128 v[224:227], v3 offset:61568\n"
    "v_mul_f32 v34, v34, v38\n"
    "v_cvt_pk_bf16_f32 v36, v32, v33\n"
    "v_exp_f32 v35, v35\n"
    "v_mfma_f32_16x16x32_bf16 v[52:55], v[228:231], v[184:187], v[52:55]\n"
    "v_exp_f32 v40, v40\n"
    "v_mfma_f32_16x16x32_bf16 v[60:63], v[228:231], v[188:191], v[60:63]\n"
    "s_waitcnt lgkmcnt(4)\n"
    "ds_read_b128 v[228:231], v3 offset:62592\n"
    "buffer_load_dwordx4 v[108:111], v235, s[8:11], s14 offen offset:2304 sc1\n"
    "v_fma_f32 v35, v35, v245, v245\n"
    "v_fma_f32 v40, v40, v247, v247\n"
    "v_mfma_f32_16x16x32_bf16 v[48:51], v[208:211], v[192:195], v[48:51]\n"
    "v_rcp_f32 v35, v35\n"
    "v_rcp_f32 v40, v40\n"
    "v_mfma_f32_16x16x32_bf16 v[56:59], v[208:211], v[196:199], v[56:59]\n"
    "s_waitcnt lgkmcnt(4)\n"
    "ds_read_b128 v[208:211], v2 offset:0\n"
    "v_mul_f32 v35, v35, v39\n"
    "v_mul_f32 v40, v40, v44\n"
    "v_mfma_f32_16x16x32_bf16 v[52:55], v[212:215], v[192:195], v[52:55]\n"
    "v_exp_f32 v41, v41\n"
    "v_mfma_f32_16x16x32_bf16 v[60:63], v[212:215], v[196:199], v[60:63]\n"
    "s_waitcnt lgkmcnt(4)\n"
    "ds_read_b128 v[212:215], v2 offset:1024\n"
    "buffer_load_dwordx4 v[112:115], v235, s[8:11], s15 offen offset:0 sc1\n"
    "v_fma_f32 v41, v41, v247, v247\n"
    "v_cvt_pk_bf16_f32 v37, v34, v35\n"
    "v_exp_f32 v42, v42\n"
    "v_mfma_f32_16x16x32_bf16 v[48:51], v[216:219], v[200:203], v[48:51]\n"
    "v_rcp_f32 v41, v41\n"
    "v_mfma_f32_16x16x32_bf16 v[56:59], v[216:219], v[204:207], v[56:59]\n"
    "s_waitcnt lgkmcnt(4)\n"
    "ds_read_b128 v[216:219], v2 offset:6144\n"
    "v_mul_f32 v41, v41, v45\n"
    "v_fma_f32 v42, v42, v247, v247\n"
    "v_exp_f32 v43, v43\n"
    "v_mfma_f32_16x16x32_bf16 v[52:55], v[220:223], v[200:203], v[52:55]\n"
    "v_rcp_f32 v42, v42\n"
    "v_mfma_f32_16x16x32_bf16 v[60:63], v[220:223], v[204:207], v[60:63]\n"
    "s_waitcnt lgkmcnt(4)\n"
    "ds_read_b128 v[220:223], v2 offset:7168\n"
    "v_mul_f32 v42, v42, v46\n"
    "v_fma_f32 v43, v43, v247, v247\n"
    "v_mfma_f32_16x16x32_bf16 v[48:51], v[224:227], v[240:243], v[48:51]\n"
    "s_add_u32 s65, s22, 393216\n"
    "buffer_load_dwordx4 v[192:195], v2, s[8:11], s65 offen offset:0 sc1\n"
    "v_cvt_pk_bf16_f32 v38, v40, v41\n"
    "v_rcp_f32 v43, v43\n"
    "v_mfma_f32_16x16x32_bf16 v[56:59], v[224:227], v[248:251], v[56:59]\n"
    "s_waitcnt lgkmcnt(4)\n"
    "ds_read_b128 v[224:227], v2 offset:2048\n"
    "buffer_load_dwordx4 v[116:119], v235, s[8:11], s15 offen offset:256 sc1\n"
    "v_mul_f32 v43, v43, v47\n"
    "v_permlane16_swap_b32 v36, v38\n"
    "v_mfma_f32_16x16x32_bf16 v[52:55], v[228:231], v[240:243], v[52:55]\n"
    "v_cvt_pk_bf16_f32 v39, v42, v43\n"
    "v_mfma_f32_16x16x32_bf16 v[60:63], v[228:231], v[248:251], v[60:63]\n"
    "s_waitcnt vmcnt(14) lgkmcnt(4)\n"
    "ds_read_b128 v[228:231], v2 offset:12288\n"
    "v_permlane16_swap_b32 v37, v39\n"
    "v_mfma_f32_16x16x32_bf16 v[16:19], v[208:211], v[64:67], 0\n"
    "s_add_u32 s70, s25, 6291456\n"
    "buffer_store_dwordx4 v[36:39], v10, s[8:11], s70 offen sc1\n"
    "v_mfma_f32_16x16x32_bf16 v[24:27], v[208:211], v[68:71], 0\n"
    "s_waitcnt lgkmcnt(4)\n"
    "ds_read_b128 v[208:211], v2 offset:3072\n"
    "buffer_load_dwordx4 v[120:123], v235, s[8:11], s15 offen offset:2048 sc1\n"
    "v_mul_f32 v52, v48, v52\n"
    "v_mul_f32 v48, v244, v48\n"
    "v_mfma_f32_16x16x32_bf16 v[20:23], v[212:215], v[64:67], 0\n"
    "v_exp_f32 v48, v48\n"
    "v_mfma_f32_16x16x32_bf16 v[28:31], v[212:215], v[68:71], 0\n"
    "s_waitcnt vmcnt(14) lgkmcnt(4)\n"
    "ds_read_b128 v[212:215], v2 offset:13312\n"
    "v_fma_f32 v48, v48, v245, v245\n"
    "v_mul_f32 v53, v49, v53\n"
    "v_mfma_f32_16x16x32_bf16 v[16:19], v[216:219], v[72:75], v[16:19]\n"
    "v_rcp_f32 v48, v48\n"
    "v_mul_f32 v49, v244, v49\n"
    "v_mfma_f32_16x16x32_bf16 v[24:27], v[216:219], v[76:79], v[24:27]\n"
    "s_waitcnt lgkmcnt(4)\n"
    "ds_read_b128 v[216:219], v2 offset:8192\n"
    "buffer_load_dwordx4 v[124:127], v235, s[8:11], s15 offen offset:2304 sc1\n"
    "v_mul_f32 v48, v48, v52\n"
    "v_mul_f32 v54, v50, v54\n"
    "v_mfma_f32_16x16x32_bf16 v[20:23], v[220:223], v[72:75], v[20:23]\n"
    "v_exp_f32 v49, v49\n"
    "v_mfma_f32_16x16x32_bf16 v[28:31], v[220:223], v[76:79], v[28:31]\n"
    "s_waitcnt lgkmcnt(4)\n"
    "ds_read_b128 v[220:223], v2 offset:18432\n"
    "v_fma_f32 v49, v49, v245, v245\n"
    "v_mul_f32 v50, v244, v50\n"
    "v_mfma_f32_16x16x32_bf16 v[32:35], v[224:227], v[64:67], 0\n"
    "v_rcp_f32 v49, v49\n"
    "v_mul_f32 v55, v51, v55\n"
    "v_mfma_f32_16x16x32_bf16 v[40:43], v[224:227], v[68:71], 0\n"
    "s_waitcnt vmcnt(13) lgkmcnt(4)\n"
    "ds_read_b128 v[224:227], v2 offset:9216\n"
    "v_mul_f32 v49, v49, v53\n"
    "v_mul_f32 v51, v244, v51\n"
    "v_mfma_f32_16x16x32_bf16 v[16:19], v[228:231], v[80:83], v[16:19]\n"
    "s_add_u32 s66, s22, 393216\n"
    "buffer_load_dwordx4 v[196:199], v2, s[8:11], s66 offen offset:1024 sc1\n"
    "v_mul_f32 v60, v56, v60\n"
    "v_mul_f32 v56, v246, v56\n"
    "v_mfma_f32_16x16x32_bf16 v[24:27], v[228:231], v[84:87], v[24:27]\n"
    "s_waitcnt lgkmcnt(4)\n"
    "ds_read_b128 v[228:231], v2 offset:19456\n"
    "buffer_load_dwordx4 v[128:131], v235, s[8:11], s16 offen offset:0 sc1\n"
    "v_mul_f32 v61, v57, v61\n"
    "v_mul_f32 v57, v246, v57\n"
    "v_mfma_f32_16x16x32_bf16 v[36:39], v[208:211], v[64:67], 0\n"
    "v_exp_f32 v50, v50\n"
    "v_mul_f32 v62, v58, v62\n"
    "v_mfma_f32_16x16x32_bf16 v[44:47], v[208:211], v[68:71], 0\n"
    "s_waitcnt lgkmcnt(4)\n"
    "ds_read_b128 v[208:211], v2 offset:14336\n"
    "v_fma_f32 v50, v50, v245, v245\n"
    "v_mul_f32 v58, v246, v58\n"
    "v_mfma_f32_16x16x32_bf16 v[20:23], v[212:215], v[80:83], v[20:23]\n"
    "v_rcp_f32 v50, v50\n"
    "v_mfma_f32_16x16x32_bf16 v[28:31], v[212:215], v[84:87], v[28:31]\n"
    "s_waitcnt lgkmcnt(4)\n"
    "ds_read_b128 v[212:215], v2 offset:24576\n"
    "buffer_load_dwordx4 v[132:135], v235, s[8:11], s16 offen offset:256 sc1\n"
    "v_mul_f32 v50, v50, v54\n"
    "v_mul_f32 v63, v59, v63\n"
    "v_mfma_f32_16x16x32_bf16 v[32:35], v[216:219], v[72:75], v[32:35]\n"
    "v_exp_f32 v51, v51\n"
    "v_mul_f32 v59, v246, v59\n"
    "v_mfma_f32_16x16x32_bf16 v[40:43], v[216:219], v[76:79], v[40:43]\n"
    "s_waitcnt vmcnt(14) lgkmcnt(4)\n"
    "ds_read_b128 v[216:219], v2 offset:15360\n"
    "v_fma_f32 v51, v51, v245, v245\n"
    "v_cvt_pk_bf16_f32 v52, v48, v49\n"
    "v_mfma_f32_16x16x32_bf16 v[16:19], v[220:223], v[88:91], v[16:19]\n"
    "v_rcp_f32 v51, v51\n"
    "v_mfma_f32_16x16x32_bf16 v[24:27], v[220:223], v[92:95], v[24:27]\n"
    "s_waitcnt lgkmcnt(4)\n"
    "ds_read_b128 v[220:223], v2 offset:25600\n"
    "buffer_load_dwordx4 v[136:139], v235, s[8:11], s16 offen offset:2048 sc1\n"
    "v_mul_f32 v51, v51, v55\n"
    "v_exp_f32 v56, v56\n"
    "v_mfma_f32_16x16x32_bf16 v[36:39], v[224:227], v[72:75], v[36:39]\n"
    "v_exp_f32 v57, v57\n"
    "v_mfma_f32_16x16x32_bf16 v[44:47], v[224:227], v[76:79], v[44:47]\n"
    "s_waitcnt lgkmcnt(4)\n"
    "ds_read_b128 v[224:227], v2 offset:20480\n"
    "v_fma_f32 v56, v56, v247, v247\n"
    "v_fma_f32 v57, v57, v247, v247\n"
    "v_mfma_f32_16x16x32_bf16 v[20:23], v[228:231], v[88:91], v[20:23]\n"
    "v_rcp_f32 v56, v56\n"
    "v_mfma_f32_16x16x32_bf16 v[28:31], v[228:231], v[92:95], v[28:31]\n"
    "s_waitcnt lgkmcnt(4)\n"
    "ds_read_b128 v[228:231], v2 offset:30720\n"
    "v_mul_f32 v56, v56, v60\n"
    "v_cvt_pk_bf16_f32 v53, v50, v51\n"
    "v_mfma_f32_16x16x32_bf16 v[32:35], v[208:211], v[80:83], v[32:35]\n"
    "s_add_u32 s67, s22, 393216\n"
    "buffer_load_dwordx4 v[200:203], v2, s[8:11], s67 offen offset:2048 sc1\n"
    "v_rcp_f32 v57, v57\n"
    "v_exp_f32 v58, v58\n"
    "v_mfma_f32_16x16x32_bf16 v[40:43], v[208:211], v[84:87], v[40:43]\n"
    "s_waitcnt vmcnt(13) lgkmcnt(4)\n"
    "ds_read_b128 v[208:211], v2 offset:21504\n"
    "buffer_load_dwordx4 v[140:143], v235, s[8:11], s16 offen offset:2304 sc1\n"
    "v_mul_f32 v57, v57, v61\n"
    "v_mfma_f32_16x16x32_bf16 v[16:19], v[212:215], v[96:99], v[16:19]\n"
    "v_exp_f32 v59, v59\n"
    "v_mfma_f32_16x16x32_bf16 v[24:27], v[212:215], v[100:103], v[24:27]\n"
    "s_waitcnt lgkmcnt(4)\n"
    "ds_read_b128 v[212:215], v2 offset:31744\n"
    "v_fma_f32 v58, v58, v247, v247\n"
    "v_fma_f32 v59, v59, v247, v247\n"
    "v_mfma_f32_16x16x32_bf16 v[36:39], v[216:219], v[80:83], v[36:39]\n"
    "v_rcp_f32 v58, v58\n"
    "v_cvt_pk_bf16_f32 v54, v56, v57\n"
    "v_mfma_f32_16x16x32_bf16 v[44:47], v[216:219], v[84:87], v[44:47]\n"
    "s_waitcnt lgkmcnt(4)\n"
    "ds_read_b128 v[216:219], v2 offset:36864\n"
    "buffer_load_dwordx4 v[144:147], v235, s[8:11], s17 offen offset:0 sc1\n"
    "v_mul_f32 v58, v58, v62\n"
    "v_rcp_f32 v59, v59\n"
    "v_mfma_f32_16x16x32_bf16 v[20:23], v[220:223], v[96:99], v[20:23]\n"
    "v_permlane16_swap_b32 v52, v54\n"
    "v_mfma_f32_16x16x32_bf16 v[28:31], v[220:223], v[100:103], v[28:31]\n"
    "s_waitcnt lgkmcnt(4)\n"
    "ds_read_b128 v[220:223], v2 offset:4096\n"
    "v_mul_f32 v59, v59, v63\n"
    "v_mfma_f32_16x16x32_bf16 v[32:35], v[224:227], v[88:91], v[32:35]\n"
    "v_cvt_pk_bf16_f32 v55, v58, v59\n"
    "v_mfma_f32_16x16x32_bf16 v[40:43], v[224:227], v[92:95], v[40:43]\n"
    "s_waitcnt vmcnt(13) lgkmcnt(4)\n"
    "ds_read_b128 v[224:227], v2 offset:37888\n"
    "buffer_load_dwordx4 v[148:151], v235, s[8:11], s17 offen offset:256 sc1\n"
    "v_permlane16_swap_b32 v53, v55\n"
    "v_mfma_f32_16x16x32_bf16 v[16:19], v[228:231], v[104:107], v[16:19]\n"
    "s_add_u32 s71, s26, 6291456\n"
    "buffer_store_dwordx4 v[52:55], v10, s[8:11], s71 offen sc1\n"
    "v_mfma_f32_16x16x32_bf16 v[24:27], v[228:231], v[108:111], v[24:27]\n"
    "s_waitcnt lgkmcnt(4)\n"
    "ds_read_b128 v[228:231], v2 offset:5120\n"
    "v_mfma_f32_16x16x32_bf16 v[36:39], v[208:211], v[88:91], v[36:39]\n"
    "v_mfma_f32_16x16x32_bf16 v[44:47], v[208:211], v[92:95], v[44:47]\n"
    "s_waitcnt lgkmcnt(4)\n"
    "ds_read_b128 v[208:211], v2 offset:26624\n"
    "v_mfma_f32_16x16x32_bf16 v[20:23], v[212:215], v[104:107], v[20:23]\n"
    "s_add_u32 s68, s22, 393216\n"
    "buffer_load_dwordx4 v[204:207], v2, s[8:11], s68 offen offset:3072 sc1\n"
    "v_mfma_f32_16x16x32_bf16 v[28:31], v[212:215], v[108:111], v[28:31]\n"
    "s_waitcnt vmcnt(13) lgkmcnt(4)\n"
    "ds_read_b128 v[212:215], v2 offset:43008\n"
    "buffer_load_dwordx4 v[152:155], v235, s[8:11], s17 offen offset:2048 sc1\n"
    "v_mfma_f32_16x16x32_bf16 v[16:19], v[216:219], v[112:115], v[16:19]\n"
    "v_mfma_f32_16x16x32_bf16 v[24:27], v[216:219], v[116:119], v[24:27]\n"
    "s_waitcnt lgkmcnt(4)\n"
    "ds_read_b128 v[216:219], v2 offset:10240\n"
    "v_mfma_f32_16x16x32_bf16 v[48:51], v[220:223], v[64:67], 0\n"
    "v_mfma_f32_16x16x32_bf16 v[56:59], v[220:223], v[68:71], 0\n"
    "s_waitcnt lgkmcnt(4)\n"
    "ds_read_b128 v[220:223], v2 offset:44032\n"
    "buffer_load_dwordx4 v[156:159], v235, s[8:11], s17 offen offset:2304 sc1\n"
    "v_mfma_f32_16x16x32_bf16 v[20:23], v[224:227], v[112:115], v[20:23]\n"
    "v_mfma_f32_16x16x32_bf16 v[28:31], v[224:227], v[116:119], v[28:31]\n"
    "s_waitcnt lgkmcnt(4)\n"
    "ds_read_b128 v[224:227], v2 offset:27648\n"
    "v_mfma_f32_16x16x32_bf16 v[52:55], v[228:231], v[64:67], 0\n"
    "v_mfma_f32_16x16x32_bf16 v[60:63], v[228:231], v[68:71], 0\n"
    "s_waitcnt lgkmcnt(4)\n"
    "ds_read_b128 v[228:231], v2 offset:49152\n"
    "buffer_load_dwordx4 v[160:163], v235, s[8:11], s18 offen offset:0 sc1\n"
    "v_mfma_f32_16x16x32_bf16 v[32:35], v[208:211], v[96:99], v[32:35]\n"
    "s_add_u32 s72, s29, 6291456\n"
    "v_add_u32 v236, s72, v2\n"
    "v_xor_b32 v237, 0x400, v236\n"
    "v_mfma_f32_16x16x32_bf16 v[40:43], v[208:211], v[100:103], v[40:43]\n"
    "s_waitcnt vmcnt(13) lgkmcnt(4)\n"
    "ds_read_b128 v[208:211], v2 offset:11264\n"
    "v_mfma_f32_16x16x32_bf16 v[16:19], v[212:215], v[120:123], v[16:19]\n"
    "v_mfma_f32_16x16x32_bf16 v[24:27], v[212:215], v[124:127], v[24:27]\n"
    "s_waitcnt lgkmcnt(4)\n"
    "ds_read_b128 v[212:215], v2 offset:50176\n"
    "v_mfma_f32_16x16x32_bf16 v[48:51], v[216:219], v[72:75], v[48:51]\n"
    "v_mfma_f32_16x16x32_bf16 v[56:59], v[216:219], v[76:79], v[56:59]\n"
    "s_waitcnt lgkmcnt(4)\n"
    "ds_read_b128 v[216:219], v2 offset:16384\n"
    "buffer_load_dwordx4 v[164:167], v235, s[8:11], s18 offen offset:256 sc1\n"
    "v_mfma_f32_16x16x32_bf16 v[20:23], v[220:223], v[120:123], v[20:23]\n"
    "v_mfma_f32_16x16x32_bf16 v[28:31], v[220:223], v[124:127], v[28:31]\n"
    "s_waitcnt lgkmcnt(4)\n"
    "ds_read_b128 v[220:223], v2 offset:32768\n"
    "v_mfma_f32_16x16x32_bf16 v[36:39], v[224:227], v[96:99], v[36:39]\n"
    "v_mfma_f32_16x16x32_bf16 v[44:47], v[224:227], v[100:103], v[44:47]\n"
    "s_waitcnt vmcnt(11) lgkmcnt(4)\n"
    "ds_read_b128 v[224:227], v2 offset:55296\n"
    "buffer_load_dwordx4 a[240:243], v235, s[8:11], s18 offen offset:2048 sc1\n"
    "v_mfma_f32_16x16x32_bf16 v[16:19], v[228:231], v[128:131], v[16:19]\n"
    "v_mfma_f32_16x16x32_bf16 v[24:27], v[228:231], v[132:135], v[24:27]\n"
    "s_waitcnt lgkmcnt(4)\n"
    "ds_read_b128 v[228:231], v2 offset:17408\n"
    "v_mfma_f32_16x16x32_bf16 v[52:55], v[208:211], v[72:75], v[52:55]\n"
    "v_mfma_f32_16x16x32_bf16 v[60:63], v[208:211], v[76:79], v[60:63]\n"
    "s_waitcnt lgkmcnt(4)\n"
    "ds_read_b128 v[208:211], v2 offset:56320\n"
    "buffer_load_dwordx4 a[244:247], v235, s[8:11], s18 offen offset:2304 sc1\n"
    "v_mfma_f32_16x16x32_bf16 v[20:23], v[212:215], v[128:131], v[20:23]\n"
    "v_mfma_f32_16x16x32_bf16 v[28:31], v[212:215], v[132:135], v[28:31]\n"
    "s_waitcnt lgkmcnt(4)\n"
    "ds_read_b128 v[212:215], v2 offset:33792\n"
    "v_mfma_f32_16x16x32_bf16 v[48:51], v[216:219], v[80:83], v[48:51]\n"
    "v_mfma_f32_16x16x32_bf16 v[56:59], v[216:219], v[84:87], v[56:59]\n"
    "s_waitcnt lgkmcnt(4)\n"
    "ds_read_b128 v[216:219], v2 offset:61440\n"
    "v_mfma_f32_16x16x32_bf16 v[32:35], v[220:223], v[104:107], v[32:35]\n"
    "v_mfma_f32_16x16x32_bf16 v[40:43], v[220:223], v[108:111], v[40:43]\n"
    "s_waitcnt vmcnt(10) lgkmcnt(4)\n"
    "ds_read_b128 v[220:223], v2 offset:22528\n"
    "buffer_load_dwordx4 a[248:251], v235, s[8:11], s19 offen offset:0 sc1\n"
    "v_mfma_f32_16x16x32_bf16 v[16:19], v[224:227], v[136:139], v[16:19]\n"
    "v_mfma_f32_16x16x32_bf16 v[24:27], v[224:227], v[140:143], v[24:27]\n"
    "s_waitcnt lgkmcnt(4)\n"
    "ds_read_b128 v[224:227], v2 offset:62464\n"
    "v_mfma_f32_16x16x32_bf16 v[52:55], v[228:231], v[80:83], v[52:55]\n"
    "v_mfma_f32_16x16x32_bf16 v[60:63], v[228:231], v[84:87], v[60:63]\n"
    "s_waitcnt lgkmcnt(4)\n"
    "ds_read_b128 v[228:231], v2 offset:23552\n"
    "buffer_load_dwordx4 a[252:255], v235, s[8:11], s19 offen offset:256 sc1\n"
    "v_mfma_f32_16x16x32_bf16 v[20:23], v[208:211], v[136:139], v[20:23]\n"
    "v_mfma_f32_16x16x32_bf16 v[28:31], v[208:211], v[140:143], v[28:31]\n"
    "s_waitcnt lgkmcnt(4)\n"
    "ds_read_b128 v[208:211], v2 offset:38912\n"
    "v_mfma_f32_16x16x32_bf16 v[36:39], v[212:215], v[104:107], v[36:39]\n"
    "v_mfma_f32_16x16x32_bf16 v[44:47], v[212:215], v[108:111], v[44:47]\n"
    "s_waitcnt vmcnt(10) lgkmcnt(4)\n"
    "ds_read_b128 v[212:215], v3 offset:2048\n"
    "buffer_load_dwordx4 v[168:171], v235, s[8:11], s19 offen offset:2048 sc1\n"
    "v_mfma_f32_16x16x32_bf16 v[16:19], v[216:219], v[144:147], v[16:19]\n"
    "v_mfma_f32_16x16x32_bf16 v[24:27], v[216:219], v[148:151], v[24:27]\n"
    "s_waitcnt lgkmcnt(4)\n"
    "ds_read_b128 v[216:219], v2 offset:28672\n"
    "v_mfma_f32_16x16x32_bf16 v[48:51], v[220:223], v[88:91], v[48:51]\n"
    "v_add_f32 v240, v192, v196\n"
    "v_add_f32 v241, v193, v197\n"
    "v_mfma_f32_16x16x32_bf16 v[56:59], v[220:223], v[92:95], v[56:59]\n"
    "s_waitcnt lgkmcnt(4)\n"
    "ds_read_b128 v[220:223], v3 offset:3072\n"
    "v_mfma_f32_16x16x32_bf16 v[20:23], v[224:227], v[144:147], v[20:23]\n"
    "v_add_f32 v242, v194, v198\n"
    "v_add_f32 v243, v195, v199\n"
    "v_mfma_f32_16x16x32_bf16 v[28:31], v[224:227], v[148:151], v[28:31]\n"
    "s_waitcnt lgkmcnt(4)\n"
    "ds_read_b128 v[224:227], v2 offset:39936\n"
    "buffer_load_dwordx4 v[172:175], v235, s[8:11], s19 offen offset:2304 sc1\n"
    "v_mfma_f32_16x16x32_bf16 v[52:55], v[228:231], v[88:91], v[52:55]\n"
    "v_add_f32 v240, v240, v200\n"
    "v_add_f32 v241, v241, v201\n"
    "v_mfma_f32_16x16x32_bf16 v[60:63], v[228:231], v[92:95], v[60:63]\n"
    "s_waitcnt lgkmcnt(4)\n"
    "ds_read_b128 v[228:231], v3 offset:8192\n"
    "v_mfma_f32_16x16x32_bf16 v[32:35], v[208:211], v[112:115], v[32:35]\n"
    "v_add_f32 v242, v242, v202\n"
    "v_add_f32 v243, v243, v203\n"
    "v_mfma_f32_16x16x32_bf16 v[40:43], v[208:211], v[116:119], v[40:43]\n"
    "s_waitcnt vmcnt(8) lgkmcnt(4)\n"
    "ds_read_b128 v[208:211], v2 offset:29696\n"
    "buffer_load_dwordx4 v[176:179], v235, s[8:11], s20 offen offset:0 sc1\n"
    "v_mfma_f32_16x16x32_bf16 v[16:19], v[212:215], v[152:155], v[16:19]\n"
    "v_add_f32 v240, v240, v204\n"
    "v_add_f32 v241, v241, v205\n"
    "v_mfma_f32_16x16x32_bf16 v[24:27], v[212:215], v[156:159], v[24:27]\n"
    "s_waitcnt lgkmcnt(4)\n"
    "ds_read_b128 v[212:215], v3 offset:9216\n"
    "v_mfma_f32_16x16x32_bf16 v[48:51], v[216:219], v[96:99], v[48:51]\n"
    "v_add_f32 v242, v242, v206\n"
    "v_add_f32 v243, v243, v207\n"
    "v_mfma_f32_16x16x32_bf16 v[56:59], v[216:219], v[100:103], v[56:59]\n"
    "s_waitcnt lgkmcnt(4)\n"
    "ds_read_b128 v[216:219], v2 offset:34816\n"
    "buffer_load_dwordx4 v[180:183], v235, s[8:11], s20 offen offset:256 sc1\n"
    "v_mfma_f32_16x16x32_bf16 v[20:23], v[220:223], v[152:155], v[20:23]\n"
    "s_add_u32 s66, s27, 524288\n"
    "s_mov_b64 exec, 0xff\n"
    "buffer_store_dwordx4 v[252:255], v2, s[8:11], s66 offen sc1\n"
    "s_mov_b64 exec, -1\n"
    "v_mfma_f32_16x16x32_bf16 v[28:31], v[220:223], v[156:159], v[28:31]\n"
    "s_waitcnt lgkmcnt(4)\n"
    "ds_read_b128 v[220:223], v2 offset:45056\n"
    "v_permlane16_swap_b32 v240, v242\n"
    "v_mfma_f32_16x16x32_bf16 v[36:39], v[224:227], v[112:115], v[36:39]\n"
    "v_permlane16_swap_b32 v241, v243\n"
    "v_mfma_f32_16x16x32_bf16 v[44:47], v[224:227], v[116:119], v[44:47]\n"
    "s_waitcnt vmcnt(9) lgkmcnt(4)\n"
    "ds_read_b128 v[224:227], v3 offset:14336\n"
    "v_add_f32 v240, v240, v242\n"
    "v_mfma_f32_16x16x32_bf16 v[16:19], v[228:231], v[160:163], v[16:19]\n"
    "v_add_f32 v241, v241, v243\n"
    "v_mfma_f32_16x16x32_bf16 v[24:27], v[228:231], v[164:167], v[24:27]\n"
    "s_waitcnt lgkmcnt(4)\n"
    "ds_read_b128 v[228:231], v2 offset:35840\n"
    "buffer_load_dwordx4 v[184:187], v235, s[8:11], s20 offen offset:2048 sc1\n"
    "v_mfma_f32_16x16x32_bf16 v[52:55], v[208:211], v[96:99], v[52:55]\n"
    "v_permlane32_swap_b32 v240, v241\n"
    "v_mfma_f32_16x16x32_bf16 v[60:63], v[208:211], v[100:103], v[60:63]\n"
    "s_waitcnt lgkmcnt(4)\n"
    "ds_read_b128 v[208:211], v3 offset:15360\n"
    "v_add_f32 v240, v240, v241\n"
    "v_mfma_f32_16x16x32_bf16 v[20:23], v[212:215], v[160:163], v[20:23]\n"
    "v_mov_b32 v241, v240\n"
    "v_mfma_f32_16x16x32_bf16 v[28:31], v[212:215], v[164:167], v[28:31]\n"
    "s_waitcnt lgkmcnt(4)\n"
    "ds_read_b128 v[212:215], v2 offset:46080\n"
    "buffer_load_dwordx4 v[188:191], v235, s[8:11], s20 offen offset:2304 sc1\n"
    "v_mfma_f32_16x16x32_bf16 v[48:51], v[216:219], v[104:107], v[48:51]\n"
    "v_permlane32_swap_b32 v240, v241\n"
    "v_mfma_f32_16x16x32_bf16 v[56:59], v[216:219], v[108:111], v[56:59]\n"
    "s_waitcnt lgkmcnt(4)\n"
    "ds_read_b128 v[216:219], v3 offset:20480\n"
    "v_mul_f32 v240, 0x3acccccd, v240\n"
    "v_mfma_f32_16x16x32_bf16 v[32:35], v[220:223], v[120:123], v[32:35]\n"
    "v_mul_f32 v241, 0x3acccccd, v241\n"
    "v_mfma_f32_16x16x32_bf16 v[40:43], v[220:223], v[124:127], v[40:43]\n"
    "s_waitcnt vmcnt(9) lgkmcnt(4)\n"
    "ds_read_b128 v[220:223], v2 offset:40960\n"
    "buffer_load_dwordx4 v[192:195], v235, s[8:11], s40 offen offset:0 sc1\n"
    "v_mfma_f32_16x16x32_bf16 v[16:19], v[224:227], a[240:243], v[16:19]\n"
    "v_fma_f32 v241, -v240, v240, v241\n"
    "v_mfma_f32_16x16x32_bf16 v[24:27], v[224:227], a[244:247], v[24:27]\n"
    "s_waitcnt lgkmcnt(4)\n"
    "ds_read_b128 v[224:227], v3 offset:21504\n"
    "v_max_f32 v241, 0, v241\n"
    "v_mfma_f32_16x16x32_bf16 v[52:55], v[228:231], v[104:107], v[52:55]\n"
    "v_cvt_pk_bf16_f32 v241, v241, v241\n"
    "v_mfma_f32_16x16x32_bf16 v[60:63], v[228:231], v[108:111], v[60:63]\n"
    "s_waitcnt lgkmcnt(4)\n"
    "ds_read_b128 v[228:231], v2 offset:41984\n"
    "v_lshlrev_b32 v241, 16, v241\n"
    "v_mfma_f32_16x16x32_bf16 v[20:23], v[208:211], a[240:243], v[20:23]\n"
    "v_add_f32 v241, 0x3727c5ac, v241\n"
    "v_mfma_f32_16x16x32_bf16 v[28:31], v[208:211], a[244:247], v[28:31]\n"
    "s_waitcnt lgkmcnt(4)\n"
    "ds_read_b128 v[208:211], v2 offset:51200\n"
    "buffer_load_dwordx4 v[196:199], v235, s[8:11], s40 offen offset:256 sc1\n"
    "v_mfma_f32_16x16x32_bf16 v[36:39], v[212:215], v[120:123], v[36:39]\n"
    "v_cvt_pk_bf16_f32 v241, v241, v241\n"
    "v_mfma_f32_16x16x32_bf16 v[44:47], v[212:215], v[124:127], v[44:47]\n"
    "s_waitcnt vmcnt(9) lgkmcnt(4)\n"
    "ds_read_b128 v[212:215], v3 offset:26624\n"
    "v_lshlrev_b32 v241, 16, v241\n"
    "v_mfma_f32_16x16x32_bf16 v[16:19], v[216:219], a[248:251], v[16:19]\n"
    "v_sqrt_f32 v241, v241\n"
    "v_mfma_f32_16x16x32_bf16 v[24:27], v[216:219], a[252:255], v[24:27]\n"
    "s_waitcnt lgkmcnt(4)\n"
    "ds_read_b128 v[216:219], v2 offset:47104\n"
    "buffer_load_dwordx4 v[200:203], v235, s[8:11], s40 offen offset:2048 sc1\n"
    "v_mfma_f32_16x16x32_bf16 v[48:51], v[220:223], v[112:115], v[48:51]\n"
    "v_cvt_pk_bf16_f32 v241, v241, v241\n"
    "v_mfma_f32_16x16x32_bf16 v[56:59], v[220:223], v[116:119], v[56:59]\n"
    "s_waitcnt lgkmcnt(4)\n"
    "ds_read_b128 v[220:223], v3 offset:27648\n"
    "v_lshlrev_b32 v241, 16, v241\n"
    "v_mfma_f32_16x16x32_bf16 v[20:23], v[224:227], a[248:251], v[20:23]\n"
    "v_rcp_f32 v244, v241\n"
    "v_mfma_f32_16x16x32_bf16 v[28:31], v[224:227], a[252:255], v[28:31]\n"
    "s_waitcnt lgkmcnt(4)\n"
    "ds_read_b128 v[224:227], v2 offset:52224\n"
    "buffer_load_dwordx4 v[204:207], v235, s[8:11], s40 offen offset:2304 sc1\n"
    "v_mfma_f32_16x16x32_bf16 v[52:55], v[228:231], v[112:115], v[52:55]\n"
    "v_mul_f32 v240, -0.5, v240\n"
    "v_mfma_f32_16x16x32_bf16 v[60:63], v[228:231], v[116:119], v[60:63]\n"
    "s_waitcnt lgkmcnt(4)\n"
    "ds_read_b128 v[228:231], v3 offset:32768\n"
    "v_mul_f32 v245, v241, v241\n"
    "v_mfma_f32_16x16x32_bf16 v[32:35], v[208:211], v[128:131], v[32:35]\n"
    "v_mul_f32 v247, v241, v241\n"
    "v_mfma_f32_16x16x32_bf16 v[40:43], v[208:211], v[132:135], v[40:43]\n"
    "s_waitcnt vmcnt(9) lgkmcnt(4)\n"
    "ds_read_b128 v[208:211], v2 offset:48128\n"
    "v_mov_b32 v248, v240\n"
    "v_mfma_f32_16x16x32_bf16 v[16:19], v[212:215], v[168:171], v[16:19]\n"
    "v_mul_f32 v246, 0xbfb8aa3b, v244\n"
    "v_mfma_f32_16x16x32_bf16 v[24:27], v[212:215], v[172:175], v[24:27]\n"
    "s_waitcnt lgkmcnt(4)\n"
    "ds_read_b128 v[212:215], v3 offset:33792\n"
    "v_mul_f32 v244, 0xbfb8aa3b, v244\n"
    "v_mfma_f32_16x16x32_bf16 v[48:51], v[216:219], v[120:123], v[48:51]\n"
    "v_permlane16_swap_b32 v240, v248\n"
    "v_mfma_f32_16x16x32_bf16 v[56:59], v[216:219], v[124:127], v[56:59]\n"
    "s_waitcnt lgkmcnt(4)\n"
    "ds_read_b128 v[216:219], v2 offset:53248\n"
    "v_permlane16_swap_b32 v245, v247\n"
    "v_mfma_f32_16x16x32_bf16 v[20:23], v[220:223], v[168:171], v[20:23]\n"
    "v_permlane16_swap_b32 v244, v246\n"
    "v_mfma_f32_16x16x32_bf16 v[28:31], v[220:223], v[172:175], v[28:31]\n"
    "s_waitcnt lgkmcnt(4)\n"
    "ds_read_b128 v[220:223], v2 offset:57344\n"
    "v_cvt_pk_bf16_f32 v243, v240, v240\n"
    "v_mfma_f32_16x16x32_bf16 v[36:39], v[224:227], v[128:131], v[36:39]\n"
    "v_cvt_pk_bf16_f32 v251, v248, v248\n"
    "v_mfma_f32_16x16x32_bf16 v[44:47], v[224:227], v[132:135], v[44:47]\n"
    "s_waitcnt vmcnt(7) lgkmcnt(4)\n"
    "ds_read_b128 v[224:227], v3 offset:38912\n"
    "v_cvt_pk_bf16_f32 v242, v240, v240\n"
    "v_mfma_f32_16x16x32_bf16 v[16:19], v[228:231], v[176:179], v[16:19]\n"
    "v_cvt_pk_bf16_f32 v250, v248, v248\n"
    "v_mfma_f32_16x16x32_bf16 v[24:27], v[228:231], v[180:183], v[24:27]\n"
    "s_waitcnt lgkmcnt(4)\n"
    "ds_read_b128 v[228:231], v2 offset:54272\n"
    "v_cvt_pk_bf16_f32 v241, v240, v240\n"
    "v_mfma_f32_16x16x32_bf16 v[52:55], v[208:211], v[120:123], v[52:55]\n"
    "v_cvt_pk_bf16_f32 v249, v248, v248\n"
    "v_mfma_f32_16x16x32_bf16 v[60:63], v[208:211], v[124:127], v[60:63]\n"
    "s_waitcnt lgkmcnt(4)\n"
    "ds_read_b128 v[208:211], v3 offset:39936\n"
    "v_cvt_pk_bf16_f32 v240, v240, v240\n"
    "v_mfma_f32_16x16x32_bf16 v[20:23], v[212:215], v[176:179], v[20:23]\n"
    "v_cvt_pk_bf16_f32 v248, v248, v248\n"
    "v_mfma_f32_16x16x32_bf16 v[28:31], v[212:215], v[180:183], v[28:31]\n"
    "s_waitcnt lgkmcnt(4)\n"
    "ds_read_b128 v[212:215], v2 offset:58368\n"
    "v_mfma_f32_16x16x32_bf16 v[48:51], v[216:219], v[128:131], v[48:51]\n"
    "v_mfma_f32_16x16x32_bf16 v[56:59], v[216:219], v[132:135], v[56:59]\n"
    "s_waitcnt lgkmcnt(4)\n"
    "ds_read_b128 v[216:219], v3 offset:45056\n"
    "v_mfma_f32_16x16x32_bf16 v[32:35], v[220:223], v[136:139], v[32:35]\n"
    "v_mfma_f32_16x16x32_bf16 v[40:43], v[220:223], v[140:143], v[40:43]\n"
    "s_waitcnt vmcnt(4) lgkmcnt(4)\n"
    "ds_read_b128 v[220:223], v2 offset:59392\n"
    "v_mfma_f32_16x16x32_bf16 v[16:19], v[224:227], v[184:187], v[16:19]\n"
    "v_mfma_f32_16x16x32_bf16 v[24:27], v[224:227], v[188:191], v[24:27]\n"
    "s_waitcnt lgkmcnt(4)\n"
    "ds_read_b128 v[224:227], v3 offset:46080\n"
    "v_mfma_f32_16x16x32_bf16 v[52:55], v[228:231], v[128:131], v[52:55]\n"
    "v_mfma_f32_16x16x32_bf16 v[60:63], v[228:231], v[132:135], v[60:63]\n"
    "s_waitcnt lgkmcnt(4)\n"
    "ds_read_b128 v[228:231], v2 offset:60416\n"
    "v_mfma_f32_16x16x32_bf16 v[20:23], v[208:211], v[184:187], v[20:23]\n"
    "v_mfma_f32_16x16x32_bf16 v[28:31], v[208:211], v[188:191], v[28:31]\n"
    "s_waitcnt lgkmcnt(4)\n"
    "ds_read_b128 v[208:211], v2 offset:63488\n"
    "v_mfma_f32_16x16x32_bf16 v[36:39], v[212:215], v[136:139], v[36:39]\n"
    "v_mfma_f32_16x16x32_bf16 v[44:47], v[212:215], v[140:143], v[44:47]\n"
    "s_waitcnt vmcnt(2) lgkmcnt(4)\n"
    "ds_read_b128 v[212:215], v3 offset:51200\n"
    "v_mfma_f32_16x16x32_bf16 v[16:19], v[216:219], v[192:195], v[16:19]\n"
    "v_mfma_f32_16x16x32_bf16 v[24:27], v[216:219], v[196:199], v[24:27]\n"
    "s_waitcnt lgkmcnt(4)\n"
    "ds_read_b128 v[216:219], v3 offset:0\n"
    "v_mfma_f32_16x16x32_bf16 v[48:51], v[220:223], v[136:139], v[48:51]\n"
    "v_mfma_f32_16x16x32_bf16 v[56:59], v[220:223], v[140:143], v[56:59]\n"
    "s_waitcnt lgkmcnt(4)\n"
    "ds_read_b128 v[220:223], v3 offset:52224\n"
    "v_mfma_f32_16x16x32_bf16 v[20:23], v[224:227], v[192:195], v[20:23]\n"
    "v_mfma_f32_16x16x32_bf16 v[28:31], v[224:227], v[196:199], v[28:31]\n"
    "s_waitcnt lgkmcnt(4)\n"
    "ds_read_b128 v[224:227], v2 offset:64512\n"
    "v_mfma_f32_16x16x32_bf16 v[52:55], v[228:231], v[136:139], v[52:55]\n"
    "v_mfma_f32_16x16x32_bf16 v[60:63], v[228:231], v[140:143], v[60:63]\n"
    "s_waitcnt lgkmcnt(4)\n"
    "ds_read_b128 v[228:231], v3 offset:57472\n"
    "v_mfma_f32_16x16x32_bf16 v[32:35], v[208:211], v[144:147], v[32:35]\n"
    "v_mfma_f32_16x16x32_bf16 v[40:43], v[208:211], v[148:151], v[40:43]\n"
    "s_waitcnt vmcnt(0) lgkmcnt(4)\n"
    "ds_read_b128 v[208:211], v3 offset:1024\n"
    "v_mfma_f32_16x16x32_bf16 v[16:19], v[212:215], v[200:203], v[16:19]\n"
    "v_mfma_f32_16x16x32_bf16 v[24:27], v[212:215], v[204:207], v[24:27]\n"
    "s_waitcnt lgkmcnt(4)\n"
    "ds_read_b128 v[212:215], v3 offset:58496\n"
    "v_mfma_f32_16x16x32_bf16 v[48:51], v[216:219], v[144:147], v[48:51]\n"
    "v_mfma_f32_16x16x32_bf16 v[56:59], v[216:219], v[148:151], v[56:59]\n"
    "s_waitcnt lgkmcnt(4)\n"
    "ds_read_b128 v[216:219], v3 offset:6144\n"
    "v_mfma_f32_16x16x32_bf16 v[20:23], v[220:223], v[200:203], v[20:23]\n"
    "v_mfma_f32_16x16x32_bf16 v[28:31], v[220:223], v[204:207], v[28:31]\n"
    "s_waitcnt lgkmcnt(4)\n"
    "ds_read_b128 v[220:223], v3 offset:4096\n"
    "v_mfma_f32_16x16x32_bf16 v[36:39], v[224:227], v[144:147], v[36:39]\n"
    "v_mfma_f32_16x16x32_bf16 v[44:47], v[224:227], v[148:151], v[44:47]\n"
    "s_waitcnt lgkmcnt(4)\n"
    "ds_read_b128 v[224:227], v3 offset:5120\n"
    "v_mfma_f32_16x16x32_bf16 v[16:19], v[228:231], v[240:243], v[16:19]\n"
    "v_mfma_f32_16x16x32_bf16 v[24:27], v[228:231], v[248:251], v[24:27]\n"
    "s_waitcnt lgkmcnt(4)\n"
    "ds_read_b128 v[228:231], v3 offset:10240\n"
    "v_mfma_f32_16x16x32_bf16 v[52:55], v[208:211], v[144:147], v[52:55]\n"
    "v_mfma_f32_16x16x32_bf16 v[60:63], v[208:211], v[148:151], v[60:63]\n"
    "s_waitcnt lgkmcnt(4)\n"
    "ds_read_b128 v[208:211], v3 offset:11264\n"
    "v_mfma_f32_16x16x32_bf16 v[20:23], v[212:215], v[240:243], v[20:23]\n"
    "s_add_u32 s83, s28, 524288\n"
    "buffer_load_dwordx2 v[238:239], v4, s[8:11], s83 offen sc1\n"
    "v_mfma_f32_16x16x32_bf16 v[28:31], v[212:215], v[248:251], v[28:31]\n"
    "s_waitcnt lgkmcnt(4)\n"
    "ds_read_b128 v[212:215], v3 offset:16384\n"
    "v_mfma_f32_16x16x32_bf16 v[48:51], v[216:219], v[152:155], v[48:51]\n"
    "v_mfma_f32_16x16x32_bf16 v[56:59], v[216:219], v[156:159], v[56:59]\n"
    "s_waitcnt lgkmcnt(4)\n"
    "ds_read_b128 v[216:219], v3 offset:17408\n"
    "v_mul_f32 v20, v16, v20\n"
    "v_mul_f32 v16, v244, v16\n"
    "v_mfma_f32_16x16x32_bf16 v[32:35], v[220:223], v[152:155], v[32:35]\n"
    "v_exp_f32 v16, v16\n"
    "v_mfma_f32_16x16x32_bf16 v[40:43], v[220:223], v[156:159], v[40:43]\n"
    "s_waitcnt lgkmcnt(4)\n"
    "ds_read_b128 v[220:223], v3 offset:22528\n"
    "v_fma_f32 v16, v16, v245, v245\n"
    "v_mul_f32 v21, v17, v21\n"
    "v_mfma_f32_16x16x32_bf16 v[36:39], v[224:227], v[152:155], v[36:39]\n"
    "v_rcp_f32 v16, v16\n"
    "v_mul_f32 v17, v244, v17\n"
    "v_mfma_f32_16x16x32_bf16 v[44:47], v[224:227], v[156:159], v[44:47]\n"
    "s_waitcnt lgkmcnt(4)\n"
    "ds_read_b128 v[224:227], v3 offset:23552\n"
    "v_mul_f32 v16, v16, v20\n"
    "v_mul_f32 v22, v18, v22\n"
    "v_mfma_f32_16x16x32_bf16 v[32:35], v[228:231], v[160:163], v[32:35]\n"
    "v_exp_f32 v17, v17\n"
    "v_mfma_f32_16x16x32_bf16 v[40:43], v[228:231], v[164:167], v[40:43]\n"
    "s_waitcnt lgkmcnt(4)\n"
    "ds_read_b128 v[228:231], v3 offset:28672\n"
    "v_fma_f32 v17, v17, v245, v245\n"
    "v_mul_f32 v18, v244, v18\n"
    "v_mul_f32 v23, v19, v23\n"
    "v_mfma_f32_16x16x32_bf16 v[36:39], v[208:211], v[160:163], v[36:39]\n"
    "v_rcp_f32 v17, v17\n"
    "v_mfma_f32_16x16x32_bf16 v[44:47], v[208:211], v[164:167], v[44:47]\n"
    "s_waitcnt lgkmcnt(4)\n"
    "ds_read_b128 v[208:211], v3 offset:29696\n"
    "v_mul_f32 v17, v17, v21\n"
    "v_mul_f32 v19, v244, v19\n"
    "v_mfma_f32_16x16x32_bf16 v[32:35], v[212:215], a[240:243], v[32:35]\n"
    "v_exp_f32 v18, v18\n"
    "v_mul_f32 v28, v24, v28\n"
    "v_mfma_f32_16x16x32_bf16 v[40:43], v[212:215], a[244:247], v[40:43]\n"
    "s_waitcnt lgkmcnt(4)\n"
    "ds_read_b128 v[212:215], v3 offset:34816\n"
    "v_fma_f32 v18, v18, v245, v245\n"
    "v_mul_f32 v24, v246, v24\n"
    "v_mfma_f32_16x16x32_bf16 v[36:39], v[216:219], a[240:243], v[36:39]\n"
    "v_rcp_f32 v18, v18\n"
    "v_mfma_f32_16x16x32_bf16 v[44:47], v[216:219], a[244:247], v[44:47]\n"
    "s_waitcnt lgkmcnt(4)\n"
    "ds_read_b128 v[216:219], v3 offset:35840\n"
    "v_mul_f32 v18, v18, v22\n"
    "v_mul_f32 v29, v25, v29\n"
    "v_mul_f32 v25, v246, v25\n"
    "v_mfma_f32_16x16x32_bf16 v[32:35], v[220:223], a[248:251], v[32:35]\n"
    "v_exp_f32 v19, v19\n"
    "v_mfma_f32_16x16x32_bf16 v[40:43], v[220:223], a[252:255], v[40:43]\n"
    "s_waitcnt lgkmcnt(4)\n"
    "ds_read_b128 v[220:223], v3 offset:40960\n"
    "v_fma_f32 v19, v19, v245, v245\n"
    "v_mul_f32 v30, v26, v30\n"
    "v_mfma_f32_16x16x32_bf16 v[36:39], v[224:227], a[248:251], v[36:39]\n"
    "v_rcp_f32 v19, v19\n"
    "v_mul_f32 v26, v246, v26\n"
    "v_mfma_f32_16x16x32_bf16 v[44:47], v[224:227], a[252:255], v[44:47]\n"
    "s_waitcnt lgkmcnt(4)\n"
    "ds_read_b128 v[224:227], v3 offset:41984\n"
    "v_mul_f32 v19, v19, v23\n"
    "v_mul_f32 v31, v27, v31\n"
    "v_mfma_f32_16x16x32_bf16 v[32:35], v[228:231], v[168:171], v[32:35]\n"
    "v_exp_f32 v24, v24\n"
    "v_mfma_f32_16x16x32_bf16 v[40:43], v[228:231], v[172:175], v[40:43]\n"
    "s_waitcnt lgkmcnt(4)\n"
    "ds_read_b128 v[228:231], v3 offset:47104\n"
    "v_fma_f32 v24, v24, v247, v247\n"
    "v_mul_f32 v27, v246, v27\n"
    "v_mfma_f32_16x16x32_bf16 v[36:39], v[208:211], v[168:171], v[36:39]\n"
    "v_rcp_f32 v24, v24\n"
    "v_cvt_pk_bf16_f32 v20, v16, v17\n"
    "v_mfma_f32_16x16x32_bf16 v[44:47], v[208:211], v[172:175], v[44:47]\n"
    "s_waitcnt lgkmcnt(4)\n"
    "ds_read_b128 v[208:211], v3 offset:48128\n"
    "v_mul_f32 v24, v24, v28\n"
    "v_cvt_pk_bf16_f32 v21, v18, v19\n"
    "v_mfma_f32_16x16x32_bf16 v[32:35], v[212:215], v[176:179], v[32:35]\n"
    "v_exp_f32 v25, v25\n"
    "v_mfma_f32_16x16x32_bf16 v[40:43], v[212:215], v[180:183], v[40:43]\n"
    "s_waitcnt lgkmcnt(4)\n"
    "ds_read_b128 v[212:215], v3 offset:53248\n"
    "v_fma_f32 v25, v25, v247, v247\n"
    "v_min_u32 v20, -2, v20\n"
    "v_exp_f32 v26, v26\n"
    "v_mfma_f32_16x16x32_bf16 v[36:39], v[216:219], v[176:179], v[36:39]\n"
    "v_rcp_f32 v25, v25\n"
    "v_mfma_f32_16x16x32_bf16 v[44:47], v[216:219], v[180:183], v[44:47]\n"
    "s_waitcnt lgkmcnt(4)\n"
    "ds_read_b128 v[216:219], v3 offset:54272\n"
    "v_mul_f32 v25, v25, v29\n"
    "v_mfma_f32_16x16x32_bf16 v[32:35], v[220:223], v[184:187], v[32:35]\n"
    "v_exp_f32 v27, v27\n"
    "v_fma_f32 v26, v26, v247, v247\n"
    "v_mfma_f32_16x16x32_bf16 v[40:43], v[220:223], v[188:191], v[40:43]\n"
    "s_waitcnt lgkmcnt(4)\n"
    "ds_read_b128 v[220:223], v3 offset:59520\n"
    "v_fma_f32 v27, v27, v247, v247\n"
    "v_cvt_pk_bf16_f32 v22, v24, v25\n"
    "v_mfma_f32_16x16x32_bf16 v[36:39], v[224:227], v[184:187], v[36:39]\n"
    "v_rcp_f32 v26, v26\n"
    "v_mfma_f32_16x16x32_bf16 v[44:47], v[224:227], v[188:191], v[44:47]\n"
    "s_waitcnt lgkmcnt(4)\n"
    "ds_read_b128 v[224:227], v3 offset:60544\n"
    "v_mul_f32 v26, v26, v30\n"
    "v_min_u32 v22, -2, v22\n"
    "v_rcp_f32 v27, v27\n"
    "v_mfma_f32_16x16x32_bf16 v[32:35], v[228:231], v[192:195], v[32:35]\n"
    "s_waitcnt vmcnt(0)\n"
    "v_cmp_eq_u64 s[86:87], s[50:51], v[238:239]\n"
    "v_mul_f32 v27, v27, v31\n"
    "v_mfma_f32_16x16x32_bf16 v[40:43], v[228:231], v[196:199], v[40:43]\n"
    "s_waitcnt lgkmcnt(4)\n"
    "ds_read_b128 v[228:231], v3 offset:7168\n"
    "v_cvt_pk_bf16_f32 v23, v26, v27\n"
    "v_permlane16_swap_b32 v20, v22\n"
    "v_mfma_f32_16x16x32_bf16 v[36:39], v[208:211], v[192:195], v[36:39]\n"
    "s_andn2_b32 s79, 0xff, s86\n"
    "s_cbranch_scc1 .Lf_slow55\n"
    ".Lf_back56:\n"
    "v_permlane16_swap_b32 v21, v23\n"
    "v_mfma_f32_16x16x32_bf16 v[44:47], v[208:211], v[196:199], v[44:47]\n"
    "s_waitcnt lgkmcnt(4)\n"
    "ds_read_b128 v[208:211], v3 offset:12288\n"
    "buffer_load_dwordx4 v[64:67], v236, s[8:11], s41 offen offset:0 sc1\n"
    "v_mfma_f32_16x16x32_bf16 v[32:35], v[212:215], v[200:203], v[32:35]\n"
    "s_add_u32 s73, s24, 9437184\n"
    "buffer_store_dwordx4 v[20:23], v10, s[8:11], s73 offen sc1\n"
    "v_mfma_f32_16x16x32_bf16 v[40:43], v[212:215], v[204:207], v[40:43]\n"
    "s_waitcnt lgkmcnt(4)\n"
    "ds_read_b128 v[212:215], v3 offset:13312\n"
    "v_mfma_f32_16x16x32_bf16 v[36:39], v[216:219], v[200:203], v[36:39]\n"
    "v_mfma_f32_16x16x32_bf16 v[44:47], v[216:219], v[204:207], v[44:47]\n"
    "s_waitcnt lgkmcnt(4)\n"
    "ds_read_b128 v[216:219], v3 offset:18432\n"
    "buffer_load_dwordx4 v[68:71], v237, s[8:11], s41 offen offset:0 sc1\n"
    "v_mfma_f32_16x16x32_bf16 v[32:35], v[220:223], v[240:243], v[32:35]\n"
    "v_mfma_f32_16x16x32_bf16 v[40:43], v[220:223], v[248:251], v[40:43]\n"
    "s_waitcnt lgkmcnt(4)\n"
    "ds_read_b128 v[220:223], v3 offset:19456\n"
    "v_mfma_f32_16x16x32_bf16 v[36:39], v[224:227], v[240:243], v[36:39]\n"
    "v_mfma_f32_16x16x32_bf16 v[44:47], v[224:227], v[248:251], v[44:47]\n"
    "s_waitcnt lgkmcnt(4)\n"
    "ds_read_b128 v[224:227], v3 offset:24576\n"
    "buffer_load_dwordx4 v[72:75], v236, s[8:11], s41 offen offset:2048 sc1\n"
    "v_mfma_f32_16x16x32_bf16 v[52:55], v[228:231], v[152:155], v[52:55]\n"
    "v_mfma_f32_16x16x32_bf16 v[60:63], v[228:231], v[156:159], v[60:63]\n"
    "s_waitcnt lgkmcnt(4)\n"
    "ds_read_b128 v[228:231], v3 offset:25600\n"
    "v_mul_f32 v36, v32, v36\n"
    "v_mul_f32 v32, v244, v32\n"
    "v_mfma_f32_16x16x32_bf16 v[48:51], v[208:211], v[160:163], v[48:51]\n"
    "v_exp_f32 v32, v32\n"
    "v_mfma_f32_16x16x32_bf16 v[56:59], v[208:211], v[164:167], v[56:59]\n"
    "s_waitcnt lgkmcnt(4)\n"
    "ds_read_b128 v[208:211], v3 offset:30720\n"
    "buffer_load_dwordx4 v[76:79], v237, s[8:11], s41 offen offset:2048 sc1\n"
    "v_fma_f32 v32, v32, v245, v245\n"
    "v_mul_f32 v37, v33, v37\n"
    "v_mfma_f32_16x16x32_bf16 v[52:55], v[212:215], v[160:163], v[52:55]\n"
    "v_rcp_f32 v32, v32\n"
    "v_mul_f32 v33, v244, v33\n"
    "v_mfma_f32_16x16x32_bf16 v[60:63], v[212:215], v[164:167], v[60:63]\n"
    "s_waitcnt lgkmcnt(4)\n"
    "ds_read_b128 v[212:215], v3 offset:31744\n"
    "v_mul_f32 v32, v32, v36\n"
    "v_mul_f32 v38, v34, v38\n"
    "v_mfma_f32_16x16x32_bf16 v[48:51], v[216:219], a[240:243], v[48:51]\n"
    "v_exp_f32 v33, v33\n"
    "v_mfma_f32_16x16x32_bf16 v[56:59], v[216:219], a[244:247], v[56:59]\n"
    "s_waitcnt lgkmcnt(4)\n"
    "ds_read_b128 v[216:219], v3 offset:36864\n"
    "buffer_load_dwordx4 v[80:83], v236, s[8:11], s42 offen offset:0 sc1\n"
    "v_fma_f32 v33, v33, v245, v245\n"
    "v_mul_f32 v34, v244, v34\n"
    "v_mul_f32 v39, v35, v39\n"
    "v_mfma_f32_16x16x32_bf16 v[52:55], v[220:223], a[240:243], v[52:55]\n"
    "v_rcp_f32 v33, v33\n"
    "v_mfma_f32_16x16x32_bf16 v[60:63], v[220:223], a[244:247], v[60:63]\n"
    "s_waitcnt lgkmcnt(4)\n"
    "ds_read_b128 v[220:223], v3 offset:37888\n"
    "v_mul_f32 v33, v33, v37\n"
    "v_mul_f32 v35, v244, v35\n"
    "v_mfma_f32_16x16x32_bf16 v[48:51], v[224:227], a[248:251], v[48:51]\n"
    "v_exp_f32 v34, v34\n"
    "v_mul_f32 v44, v40, v44\n"
    "v_mfma_f32_16x16x32_bf16 v[56:59], v[224:227], a[252:255], v[56:59]\n"
    "s_waitcnt lgkmcnt(4)\n"
    "ds_read_b128 v[224:227], v3 offset:43008\n"
    "buffer_load_dwordx4 v[84:87], v237, s[8:11], s42 offen offset:0 sc1\n"
    "v_fma_f32 v34, v34, v245, v245\n"
    "v_mul_f32 v40, v246, v40\n"
    "v_mfma_f32_16x16x32_bf16 v[52:55], v[228:231], a[248:251], v[52:55]\n"
    "v_rcp_f32 v34, v34\n"
    "v_mfma_f32_16x16x32_bf16 v[60:63], v[228:231], a[252:255], v[60:63]\n"
    "s_waitcnt lgkmcnt(4)\n"
    "ds_read_b128 v[228:231], v3 offset:44032\n"
    "v_mul_f32 v34, v34, v38\n"
    "v_mul_f32 v45, v41, v45\n"
    "v_mul_f32 v41, v246, v41\n"
    "v_mfma_f32_16x16x32_bf16 v[48:51], v[208:211], v[168:171], v[48:51]\n"
    "v_exp_f32 v35, v35\n"
    "v_mfma_f32_16x16x32_bf16 v[56:59], v[208:211], v[172:175], v[56:59]\n"
    "s_waitcnt lgkmcnt(4)\n"
    "ds_read_b128 v[208:211], v3 offset:49152\n"
    "buffer_load_dwordx4 v[88:91], v236, s[8:11], s42 offen offset:2048 sc1\n"
    "v_fma_f32 v35, v35, v245, v245\n"
    "v_mul_f32 v46, v42, v46\n"
    "v_mfma_f32_16x16x32_bf16 v[52:55], v[212:215], v[168:171], v[52:55]\n"
    "v_rcp_f32 v35, v35\n"
    "v_mul_f32 v42, v246, v42\n"
    "v_mfma_f32_16x16x32_bf16 v[60:63], v[212:215], v[172:175], v[60:63]\n"
    "s_waitcnt lgkmcnt(4)\n"
    "ds_read_b128 v[212:215], v3 offset:50176\n"
    "v_mul_f32 v35, v35, v39\n"
    "v_mul_f32 v47, v43, v47\n"
    "v_mfma_f32_16x16x32_bf16 v[48:51], v[216:219], v[176:179], v[48:51]\n"
    "v_exp_f32 v40, v40\n"
    "v_mfma_f32_16x16x32_bf16 v[56:59], v[216:219], v[180:183], v[56:59]\n"
    "s_waitcnt lgkmcnt(4)\n"
    "ds_read_b128 v[216:219], v3 offset:55296\n"
    "buffer_load_dwordx4 v[92:95], v237, s[8:11], s42 offen offset:2048 sc1\n"
    "v_fma_f32 v40, v40, v247, v247\n"
    "v_mul_f32 v43, v246, v43\n"
    "v_mfma_f32_16x16x32_bf16 v[52:55], v[220:223], v[176:179], v[52:55]\n"
    "v_rcp_f32 v40, v40\n"
    "v_cvt_pk_bf16_f32 v36, v32, v33\n"
    "v_mfma_f32_16x16x32_bf16 v[60:63], v[220:223], v[180:183], v[60:63]\n"
    "s_waitcnt lgkmcnt(4)\n"
    "ds_read_b128 v[220:223], v3 offset:56320\n"
    "v_mul_f32 v40, v40, v44\n"
    "v_cvt_pk_bf16_f32 v37, v34, v35\n"
    "v_mfma_f32_16x16x32_bf16 v[48:51], v[224:227], v[184:187], v[48:51]\n"
    "v_exp_f32 v41, v41\n"
    "v_mfma_f32_16x16x32_bf16 v[56:59], v[224:227], v[188:191], v[56:59]\n"
    "s_waitcnt lgkmcnt(4)\n"
    "ds_read_b128 v[224:227], v3 offset:61568\n"
    "buffer_load_dwordx4 v[96:99], v236, s[8:11], s43 offen offset:0 sc1\n"
    "v_fma_f32 v41, v41, v247, v247\n"
    "v_min_u32 v36, -2, v36\n"
    "v_exp_f32 v42, v42\n"
    "v_mfma_f32_16x16x32_bf16 v[52:55], v[228:231], v[184:187], v[52:55]\n"
    "v_rcp_f32 v41, v41\n"
    "v_mfma_f32_16x16x32_bf16 v[60:63], v[228:231], v[188:191], v[60:63]\n"
    "s_waitcnt lgkmcnt(4)\n"
    "ds_read_b128 v[228:231], v3 offset:62592\n"
    "v_mul_f32 v41, v41, v45\n"
    "v_mfma_f32_16x16x32_bf16 v[48:51], v[208:211], v[192:195], v[48:51]\n"
    "v_exp_f32 v43, v43\n"
    "v_fma_f32 v42, v42, v247, v247\n"
    "v_mfma_f32_16x16x32_bf16 v[56:59], v[208:211], v[196:199], v[56:59]\n"
    "s_waitcnt lgkmcnt(4)\n"
    "buffer_load_dwordx4 v[100:103], v237, s[8:11], s43 offen offset:0 sc1\n"
    "v_fma_f32 v43, v43, v247, v247\n"
    "v_cvt_pk_bf16_f32 v38, v40, v41\n"
    "v_mfma_f32_16x16x32_bf16 v[52:55], v[212:215], v[192:195], v[52:55]\n"
    "v_rcp_f32 v42, v42\n"
    "v_mfma_f32_16x16x32_bf16 v[60:63], v[212:215], v[196:199], v[60:63]\n"
    "s_waitcnt lgkmcnt(3)\n"
    "v_rcp_f32 v43, v43\n"
    "v_mul_f32 v42, v42, v46\n"
    "v_mfma_f32_16x16x32_bf16 v[48:51], v[216:219], v[200:203], v[48:51]\n"
    "v_mul_f32 v43, v43, v47\n"
    "v_min_u32 v38, -2, v38\n"
    "v_mfma_f32_16x16x32_bf16 v[56:59], v[216:219], v[204:207], v[56:59]\n"
    "s_waitcnt lgkmcnt(2)\n"
    "buffer_load_dwordx4 v[104:107], v236, s[8:11], s43 offen offset:2048 sc1\n"
    "v_cvt_pk_bf16_f32 v39, v42, v43\n"
    "v_permlane16_swap_b32 v36, v38\n"
    "v_mfma_f32_16x16x32_bf16 v[52:55], v[220:223], v[200:203], v[52:55]\n"
    "v_permlane16_swap_b32 v37, v39\n"
    "v_mfma_f32_16x16x32_bf16 v[60:63], v[220:223], v[204:207], v[60:63]\n"
    "s_waitcnt lgkmcnt(1)\n"
    "s_add_u32 s88, s25, 9437184\n"
    "buffer_store_dwordx4 v[36:39], v10, s[8:11], s88 offen sc1\n"
    "v_mfma_f32_16x16x32_bf16 v[48:51], v[224:227], v[240:243], v[48:51]\n"
    "v_mfma_f32_16x16x32_bf16 v[56:59], v[224:227], v[248:251], v[56:59]\n"
    "s_waitcnt lgkmcnt(0)\n"
    "buffer_load_dwordx4 v[108:111], v237, s[8:11], s43 offen offset:2048 sc1\n"
    "v_mfma_f32_16x16x32_bf16 v[52:55], v[228:231], v[240:243], v[52:55]\n"
    "v_mfma_f32_16x16x32_bf16 v[60:63], v[228:231], v[248:251], v[60:63]\n"
    "s_waitcnt vmcnt(13)\n"
    "v_mfma_f32_16x16x32_bf16 v[16:19], a[0:3], v[64:67], 0\n"
    "v_mfma_f32_16x16x32_bf16 v[20:23], a[4:7], v[64:67], 0\n"
    "buffer_load_dwordx4 v[112:115], v236, s[8:11], s48 offen offset:0 sc1\n"
    "v_mfma_f32_16x16x32_bf16 v[24:27], a[8:11], v[64:67], 0\n"
    "v_pk_mul_f32 v[52:53], v[48:49], v[52:53]\n"
    "v_pk_mul_f32 v[48:49], v[48:49], v[244:245] op_sel_hi:[1,0]\n"
    "v_pk_mul_f32 v[54:55], v[50:51], v[54:55]\n"
    "v_pk_mul_f32 v[50:51], v[50:51], v[244:245] op_sel_hi:[1,0]\n"
    "v_pk_mul_f32 v[60:61], v[56:57], v[60:61]\n"
    "v_pk_mul_f32 v[56:57], v[56:57], v[246:247] op_sel_hi:[1,0]\n"
    "v_pk_mul_f32 v[62:63], v[58:59], v[62:63]\n"
    "v_pk_mul_f32 v[58:59], v[58:59], v[246:247] op_sel_hi:[1,0]\n"
    "v_exp_f32 v48, v48\n"
    "v_exp_f32 v49, v49\n"
    "v_exp_f32 v50, v50\n"
    "v_exp_f32 v51, v51\n"
    "v_exp_f32 v56, v56\n"
    "v_exp_f32 v57, v57\n"
    "v_exp_f32 v58, v58\n"
    "v_exp_f32 v59, v59\n"
    "v_pk_fma_f32 v[48:49], v[48:49], v[244:245], v[244:245] op_sel:[0,1,1] op_sel_hi:[1,1,1]\n"
    "v_pk_fma_f32 v[50:51], v[50:51], v[244:245], v[244:245] op_sel:[0,1,1] op_sel_hi:[1,1,1]\n"
    "v_pk_fma_f32 v[56:57], v[56:57], v[246:247], v[246:247] op_sel:[0,1,1] op_sel_hi:[1,1,1]\n"
    "v_pk_fma_f32 v[58:59], v[58:59], v[246:247], v[246:247] op_sel:[0,1,1] op_sel_hi:[1,1,1]\n"
    "v_rcp_f32 v48, v48\n"
    "v_rcp_f32 v49, v49\n"
    "v_rcp_f32 v50, v50\n"
    "v_rcp_f32 v51, v51\n"
    "v_rcp_f32 v56, v56\n"
    "v_rcp_f32 v57, v57\n"
    "v_rcp_f32 v58, v58\n"
    "v_rcp_f32 v59, v59\n"
    "v_pk_mul_f32 v[48:49], v[48:49], v[52:53]\n"
    "v_pk_mul_f32 v[50:51], v[50:51], v[54:55]\n"
    "v_pk_mul_f32 v[56:57], v[56:57], v[60:61]\n"
    "v_pk_mul_f32 v[58:59], v[58:59], v[62:63]\n"
    "v_cvt_pk_bf16_f32 v52, v48, v49\n"
    "v_cvt_pk_bf16_f32 v54, v56, v57\n"
    "v_cvt_pk_bf16_f32 v53, v50, v51\n"
    "v_cvt_pk_bf16_f32 v55, v58, v59\n"
    "v_permlane16_swap_b32 v52, v54\n"
    "s_nop 0\n"
    "v_permlane16_swap_b32 v53, v55\n"
    "v_min_u32 v52, -2, v52\n"
    "v_min_u32 v54, -2, v54\n"
    "s_add_u32 s89, s26, 9437184\n"
    "buffer_store_dwordx4 v[52:55], v10, s[8:11], s89 offen sc1\n"
    "v_mfma_f32_16x16x32_bf16 v[28:31], a[12:15], v[64:67], 0\n"
    "v_mfma_f32_16x16x32_bf16 v[208:211], a[16:19], v[64:67], 0\n"
    "s_waitcnt vmcnt(13)\n"
    "v_mfma_f32_16x16x32_bf16 v[212:215], a[0:3], v[68:71], 0\n"
    "buffer_load_dwordx4 v[116:119], v237, s[8:11], s48 offen offset:0 sc1\n"
    "v_mfma_f32_16x16x32_bf16 v[216:219], a[4:7], v[68:71], 0\n"
    "v_mfma_f32_16x16x32_bf16 v[220:223], a[8:11], v[68:71], 0\n"
    "v_mfma_f32_16x16x32_bf16 v[224:227], a[12:15], v[68:71], 0\n"
    "v_mfma_f32_16x16x32_bf16 v[228:231], a[16:19], v[68:71], 0\n"
    "buffer_load_dwordx4 v[120:123], v236, s[8:11], s48 offen offset:2048 sc1\n"
    "s_waitcnt vmcnt(14)\n"
    "v_mfma_f32_16x16x32_bf16 v[16:19], a[20:23], v[72:75], v[16:19]\n"
    "v_mfma_f32_16x16x32_bf16 v[20:23], a[24:27], v[72:75], v[20:23]\n"
    "v_mfma_f32_16x16x32_bf16 v[24:27], a[28:31], v[72:75], v[24:27]\n"
    "v_mfma_f32_16x16x32_bf16 v[28:31], a[32:35], v[72:75], v[28:31]\n"
    "buffer_load_dwordx4 v[124:127], v237, s[8:11], s48 offen offset:2048 sc1\n"
    "v_mfma_f32_16x16x32_bf16 v[208:211], a[36:39], v[72:75], v[208:211]\n"
    "s_waitcnt vmcnt(14)\n"
    "v_mfma_f32_16x16x32_bf16 v[212:215], a[20:23], v[76:79], v[212:215]\n"
    "v_mfma_f32_16x16x32_bf16 v[216:219], a[24:27], v[76:79], v[216:219]\n"
    "s_add_u32 s90, s30, 2621440\n"
    "buffer_load_dwordx4 v[160:163], v12, s[8:11], s90 offen sc1\n"
    "v_mfma_f32_16x16x32_bf16 v[220:223], a[28:31], v[76:79], v[220:223]\n"
    "buffer_load_dwordx4 v[128:131], v236, s[8:11], s49 offen offset:0 sc1\n"
    "v_mfma_f32_16x16x32_bf16 v[224:227], a[32:35], v[76:79], v[224:227]\n"
    "v_mfma_f32_16x16x32_bf16 v[228:231], a[36:39], v[76:79], v[228:231]\n"
    "s_waitcnt vmcnt(15)\n"
    "v_mfma_f32_16x16x32_bf16 v[16:19], a[40:43], v[80:83], v[16:19]\n"
    "s_add_u32 s91, s31, 2621440\n"
    "buffer_load_dword v173, v13, s[8:11], s91 offen sc1\n"
    "v_mfma_f32_16x16x32_bf16 v[20:23], a[44:47], v[80:83], v[20:23]\n"
    "buffer_load_dwordx4 v[132:135], v237, s[8:11], s49 offen offset:0 sc1\n"
    "v_mfma_f32_16x16x32_bf16 v[24:27], a[48:51], v[80:83], v[24:27]\n"
    "v_mfma_f32_16x16x32_bf16 v[28:31], a[52:55], v[80:83], v[28:31]\n"
    "v_mfma_f32_16x16x32_bf16 v[208:211], a[56:59], v[80:83], v[208:211]\n"
    "s_waitcnt vmcnt(16)\n"
    "v_mfma_f32_16x16x32_bf16 v[212:215], a[40:43], v[84:87], v[212:215]\n"
    "buffer_load_dwordx4 v[136:139], v236, s[8:11], s49 offen offset:2048 sc1\n"
    "v_mfma_f32_16x16x32_bf16 v[216:219], a[44:47], v[84:87], v[216:219]\n"
    "v_mfma_f32_16x16x32_bf16 v[220:223], a[48:51], v[84:87], v[220:223]\n"
    "v_mfma_f32_16x16x32_bf16 v[224:227], a[52:55], v[84:87], v[224:227]\n"
    "v_mfma_f32_16x16x32_bf16 v[228:231], a[56:59], v[84:87], v[228:231]\n"
    "buffer_load_dwordx4 v[140:143], v237, s[8:11], s49 offen offset:2048 sc1\n"
    "s_waitcnt vmcnt(17)\n"
    "v_mfma_f32_16x16x32_bf16 v[16:19], a[60:63], v[88:91], v[16:19]\n"
    "v_mfma_f32_16x16x32_bf16 v[20:23], a[64:67], v[88:91], v[20:23]\n"
    "v_mfma_f32_16x16x32_bf16 v[24:27], a[68:71], v[88:91], v[24:27]\n"
    "v_mfma_f32_16x16x32_bf16 v[28:31], a[72:75], v[88:91], v[28:31]\n"
    "buffer_load_dwordx4 v[144:147], v236, s[8:11], s85 offen offset:0 sc1\n"
    "v_mfma_f32_16x16x32_bf16 v[208:211], a[76:79], v[88:91], v[208:211]\n"
    "s_waitcnt vmcnt(17)\n"
    "v_mfma_f32_16x16x32_bf16 v[212:215], a[60:63], v[92:95], v[212:215]\n"
    "v_mfma_f32_16x16x32_bf16 v[216:219], a[64:67], v[92:95], v[216:219]\n"
    "v_mfma_f32_16x16x32_bf16 v[220:223], a[68:71], v[92:95], v[220:223]\n"
    "buffer_load_dwordx4 v[148:151], v237, s[8:11], s85 offen offset:0 sc1\n"
    "v_mfma_f32_16x16x32_bf16 v[224:227], a[72:75], v[92:95], v[224:227]\n"
    "v_mfma_f32_16x16x32_bf16 v[228:231], a[76:79], v[92:95], v[228:231]\n"
    "s_waitcnt vmcnt(17)\n"
    "v_mfma_f32_16x16x32_bf16 v[16:19], a[80:83], v[96:99], v[16:19]\n"
    "v_mfma_f32_16x16x32_bf16 v[20:23], a[84:87], v[96:99], v[20:23]\n"
    "buffer_load_dwordx4 v[152:155], v236, s[8:11], s85 offen offset:2048 sc1\n"
    "v_mfma_f32_16x16x32_bf16 v[24:27], a[88:91], v[96:99], v[24:27]\n"
    "v_mfma_f32_16x16x32_bf16 v[28:31], a[92:95], v[96:99], v[28:31]\n"
    "v_mfma_f32_16x16x32_bf16 v[208:211], a[96:99], v[96:99], v[208:211]\n"
    "s_waitcnt vmcnt(17)\n"
    "v_mfma_f32_16x16x32_bf16 v[212:215], a[80:83], v[100:103], v[212:215]\n"
    "buffer_load_dwordx4 v[156:159], v237, s[8:11], s85 offen offset:2048 sc1\n"
    "v_mfma_f32_16x16x32_bf16 v[216:219], a[84:87], v[100:103], v[216:219]\n"
    "s_add_u32 s92, s29, 9437184\n"
    "v_add_u32 v236, s92, v2\n"
    "v_mfma_f32_16x16x32_bf16 v[220:223], a[88:91], v[100:103], v[220:223]\n"
    "v_xor_b32 v237, 0x400, v236\n"
    "v_mfma_f32_16x16x32_bf16 v[224:227], a[92:95], v[100:103], v[224:227]\n"
    "v_mfma_f32_16x16x32_bf16 v[228:231], a[96:99], v[100:103], v[228:231]\n"
    "s_waitcnt vmcnt(17)\n"
    "v_mfma_f32_16x16x32_bf16 v[16:19], a[100:103], v[104:107], v[16:19]\n"
    "v_mfma_f32_16x16x32_bf16 v[20:23], a[104:107], v[104:107], v[20:23]\n"
    "v_mfma_f32_16x16x32_bf16 v[24:27], a[108:111], v[104:107], v[24:27]\n"
    "buffer_load_dwordx4 v[64:67], v236, s[8:11], s41 offen offset:0 sc1\n"
    "v_mfma_f32_16x16x32_bf16 v[28:31], a[112:115], v[104:107], v[28:31]\n"
    "v_mfma_f32_16x16x32_bf16 v[208:211], a[116:119], v[104:107], v[208:211]\n"
    "s_waitcnt vmcnt(16)\n"
    "v_mfma_f32_16x16x32_bf16 v[212:215], a[100:103], v[108:111], v[212:215]\n"
    "v_mfma_f32_16x16x32_bf16 v[216:219], a[104:107], v[108:111], v[216:219]\n"
    "buffer_load_dwordx4 v[68:71], v237, s[8:11], s41 offen offset:0 sc1\n"
    "v_mfma_f32_16x16x32_bf16 v[220:223], a[108:111], v[108:111], v[220:223]\n"
    "v_mfma_f32_16x16x32_bf16 v[224:227], a[112:115], v[108:111], v[224:227]\n"
    "v_mfma_f32_16x16x32_bf16 v[228:231], a[116:119], v[108:111], v[228:231]\n"
    "s_waitcnt vmcnt(16)\n"
    "v_mfma_f32_16x16x32_bf16 v[16:19], a[120:123], v[112:115], v[16:19]\n"
    "buffer_load_dwordx4 v[72:75], v236, s[8:11], s41 offen offset:2048 sc1\n"
    "v_mfma_f32_16x16x32_bf16 v[20:23], a[124:127], v[112:115], v[20:23]\n"
    "v_mfma_f32_16x16x32_bf16 v[24:27], a[128:131], v[112:115], v[24:27]\n"
    "v_mfma_f32_16x16x32_bf16 v[28:31], a[132:135], v[112:115], v[28:31]\n"
    "v_mfma_f32_16x16x32_bf16 v[208:211], a[136:139], v[112:115], v[208:211]\n"
    "buffer_load_dwordx4 v[76:79], v237, s[8:11], s41 offen offset:2048 sc1\n"
    "s_waitcnt vmcnt(16)\n"
    "v_mfma_f32_16x16x32_bf16 v[212:215], a[120:123], v[116:119], v[212:215]\n"
    "v_mfma_f32_16x16x32_bf16 v[216:219], a[124:127], v[116:119], v[216:219]\n"
    "v_mfma_f32_16x16x32_bf16 v[220:223], a[128:131], v[116:119], v[220:223]\n"
    "v_mfma_f32_16x16x32_bf16 v[224:227], a[132:135], v[116:119], v[224:227]\n"
    "buffer_load_dwordx4 v[80:83], v236, s[8:11], s42 offen offset:0 sc1\n"
    "v_mfma_f32_16x16x32_bf16 v[228:231], a[136:139], v[116:119], v[228:231]\n"
    "s_waitcnt vmcnt(16)\n"
    "v_mfma_f32_16x16x32_bf16 v[16:19], a[140:143], v[120:123], v[16:19]\n"
    "s_waitcnt vmcnt(14)\n"
    "v_lshlrev_b32 v174, 16, v160\n"
    "v_and_b32 v175, 0xffff0000, v160\n"
    "v_mfma_f32_16x16x32_bf16 v[20:23], a[144:147], v[120:123], v[20:23]\n"
    "v_lshlrev_b32 v176, 16, v161\n"
    "v_and_b32 v177, 0xffff0000, v161\n"
    "v_mfma_f32_16x16x32_bf16 v[24:27], a[148:151], v[120:123], v[24:27]\n"
    "buffer_load_dwordx4 v[84:87], v237, s[8:11], s42 offen offset:0 sc1\n"
    "v_lshlrev_b32 v178, 16, v162\n"
    "v_and_b32 v179, 0xffff0000, v162\n"
    "v_mfma_f32_16x16x32_bf16 v[28:31], a[152:155], v[120:123], v[28:31]\n"
    "v_lshlrev_b32 v180, 16, v163\n"
    "v_and_b32 v181, 0xffff0000, v163\n"
    "v_mfma_f32_16x16x32_bf16 v[208:211], a[156:159], v[120:123], v[208:211]\n"
    "s_waitcnt vmcnt(13)\n"
    "v_lshlrev_b32 v182, 16, v173\n"
    "v_mfma_f32_16x16x32_bf16 v[212:215], a[140:143], v[124:127], v[212:215]\n"
    "v_and_b32 v183, 0xffff0000, v173\n"
    "v_bfe_u32 v14, v12, 4, 5\n"
    "v_mfma_f32_16x16x32_bf16 v[216:219], a[144:147], v[124:127], v[216:219]\n"
    "buffer_load_dwordx4 v[88:91], v236, s[8:11], s42 offen offset:2048 sc1\n"
    "v_mul_u32_u24 v14, 1280, v14\n"
    "v_and_b32 v5, 48, v1\n"
    "v_mfma_f32_16x16x32_bf16 v[220:223], a[148:151], v[124:127], v[220:223]\n"
    "v_add_u32 v14, v14, v5\n"
    "v_mfma_f32_16x16x32_bf16 v[224:227], a[152:155], v[124:127], v[224:227]\n"
    "v_mfma_f32_16x16x32_bf16 v[228:231], a[156:159], v[124:127], v[228:231]\n"
    "v_mfma_f32_16x16x32_bf16 v[16:19], a[160:163], v[128:131], v[16:19]\n"
    "buffer_load_dwordx4 v[92:95], v237, s[8:11], s42 offen offset:2048 sc1\n"
    "v_mfma_f32_16x16x32_bf16 v[20:23], a[164:167], v[128:131], v[20:23]\n"
    "v_mfma_f32_16x16x32_bf16 v[24:27], a[168:171], v[128:131], v[24:27]\n"
    "v_mfma_f32_16x16x32_bf16 v[28:31], a[172:175], v[128:131], v[28:31]\n"
    "v_mfma_f32_16x16x32_bf16 v[208:211], a[176:179], v[128:131], v[208:211]\n"
    "buffer_load_dwordx4 v[96:99], v236, s[8:11], s43 offen offset:0 sc1\n"
    "s_waitcnt vmcnt(15)\n"
    "v_mfma_f32_16x16x32_bf16 v[212:215], a[160:163], v[132:135], v[212:215]\n"
    "v_mfma_f32_16x16x32_bf16 v[216:219], a[164:167], v[132:135], v[216:219]\n"
    "v_mfma_f32_16x16x32_bf16 v[220:223], a[168:171], v[132:135], v[220:223]\n"
    "v_mfma_f32_16x16x32_bf16 v[224:227], a[172:175], v[132:135], v[224:227]\n"
    "buffer_load_dwordx4 v[100:103], v237, s[8:11], s43 offen offset:0 sc1\n"
    "v_mfma_f32_16x16x32_bf16 v[228:231], a[176:179], v[132:135], v[228:231]\n"
    "s_waitcnt vmcnt(15)\n"
    "v_mfma_f32_16x16x32_bf16 v[24:27], a[188:191], v[136:139], v[24:27]\n"
    "s_waitcnt vmcnt(13)\n"
    "v_mfma_f32_16x16x32_bf16 v[24:27], a[208:211], v[144:147], v[24:27]\n"
    "s_waitcnt vmcnt(11)\n"
    "v_mfma_f32_16x16x32_bf16 v[24:27], a[228:231], v[152:155], v[24:27]\n"
    "buffer_load_dwordx4 v[104:107], v236, s[8:11], s43 offen offset:2048 sc1\n"
    "v_mfma_f32_16x16x32_bf16 v[212:215], a[180:183], v[140:143], v[212:215]\n"
    "s_barrier\n"
    "v_mfma_f32_16x16x32_bf16 v[212:215], a[200:203], v[148:151], v[212:215]\n"
    "ds_write_b128 v232, v[24:27]\n"
    "s_waitcnt vmcnt(11)\n"
    "v_mfma_f32_16x16x32_bf16 v[212:215], a[220:223], v[156:159], v[212:215]\n"
    "v_mfma_f32_16x16x32_bf16 v[216:219], a[184:187], v[140:143], v[216:219]\n"
    "buffer_load_dwordx4 v[108:111], v237, s[8:11], s43 offen offset:2048 sc1\n"
    "v_mfma_f32_16x16x32_bf16 v[216:219], a[204:207], v[148:151], v[216:219]\n"
    "ds_write_b128 v233, v[212:215] offset:8192\n"
    "v_mfma_f32_16x16x32_bf16 v[216:219], a[224:227], v[156:159], v[216:219]\n"
    "v_mfma_f32_16x16x32_bf16 v[220:223], a[188:191], v[140:143], v[220:223]\n"
    "v_mfma_f32_16x16x32_bf16 v[220:223], a[208:211], v[148:151], v[220:223]\n"
    "ds_write_b128 v233, v[216:219] offset:9216\n"
    "buffer_load_dwordx4 v[112:115], v236, s[8:11], s48 offen offset:0 sc1\n"
    "v_mfma_f32_16x16x32_bf16 v[220:223], a[228:231], v[156:159], v[220:223]\n"
    "v_mfma_f32_16x16x32_bf16 v[28:31], a[192:195], v[136:139], v[28:31]\n"
    "v_mfma_f32_16x16x32_bf16 v[28:31], a[212:215], v[144:147], v[28:31]\n"
    "ds_write_b128 v234, v[220:223] offset:16384\n"
    "v_mfma_f32_16x16x32_bf16 v[28:31], a[232:235], v[152:155], v[28:31]\n"
    "buffer_load_dwordx4 v[116:119], v237, s[8:11], s48 offen offset:0 sc1\n"
    "v_mfma_f32_16x16x32_bf16 v[224:227], a[192:195], v[140:143], v[224:227]\n"
    "v_mfma_f32_16x16x32_bf16 v[224:227], a[212:215], v[148:151], v[224:227]\n"
    "ds_write_b128 v232, v[28:31] offset:1024\n"
    "v_mfma_f32_16x16x32_bf16 v[224:227], a[232:235], v[156:159], v[224:227]\n"
    "v_mfma_f32_16x16x32_bf16 v[208:211], a[196:199], v[136:139], v[208:211]\n"
    "buffer_load_dwordx4 v[120:123], v236, s[8:11], s48 offen offset:2048 sc1\n"
    "v_mfma_f32_16x16x32_bf16 v[208:211], a[216:219], v[144:147], v[208:211]\n"
    "ds_write_b128 v234, v[224:227] offset:17408\n"
    "v_mfma_f32_16x16x32_bf16 v[208:211], a[236:239], v[152:155], v[208:211]\n"
    "v_mfma_f32_16x16x32_bf16 v[228:231], a[196:199], v[140:143], v[228:231]\n"
    "v_mfma_f32_16x16x32_bf16 v[228:231], a[216:219], v[148:151], v[228:231]\n"
    "ds_write_b64 v232, v[210:211] offset:24576\n"
    "buffer_load_dwordx4 v[124:127], v237, s[8:11], s48 offen offset:2048 sc1\n"
    "v_mfma_f32_16x16x32_bf16 v[228:231], a[236:239], v[156:159], v[228:231]\n"
    "v_mfma_f32_16x16x32_bf16 v[16:19], a[180:183], v[136:139], v[16:19]\n"
    "v_mfma_f32_16x16x32_bf16 v[16:19], a[200:203], v[144:147], v[16:19]\n"
    "ds_write_b64 v233, v[228:229] offset:24584\n"
    "ds_write_b64 v234, v[230:231] offset:25600\n"
    "v_mfma_f32_16x16x32_bf16 v[16:19], a[220:223], v[152:155], v[16:19]\n"
    "buffer_load_dwordx4 v[128:131], v236, s[8:11], s49 offen offset:0 sc1\n"
    "s_waitcnt lgkmcnt(0)\n"
    "s_barrier\n"
    "v_mfma_f32_16x16x32_bf16 v[20:23], a[184:187], v[136:139], v[20:23]\n"
    "ds_read_b128 v[24:27], v254 offset:0\n"
    "ds_read_b128 v[212:215], v254 offset:8192\n"
    "ds_read_b128 v[220:223], v254 offset:16384\n"
    "ds_read_b128 v[28:31], v254 offset:1024\n"
    "v_mfma_f32_16x16x32_bf16 v[20:23], a[204:207], v[144:147], v[20:23]\n"
    "ds_read_b128 v[216:219], v254 offset:9216\n"
    "ds_read_b128 v[224:227], v254 offset:17408\n"
    "ds_read_b128 v[228:231], v254 offset:24576\n"
    "ds_read_b64 v[210:211], v254 offset:25600\n"
    "v_mfma_f32_16x16x32_bf16 v[20:23], a[224:227], v[152:155], v[20:23]\n"
    "s_waitcnt vmcnt(13)\n"
    "v_max3_u32 v238, v78, v76, v64\n"
    "v_max3_u32 v238, v238, v66, v68\n"
    "v_max3_u32 v238, v238, v70, v72\n"
    "v_max_u32 v238, v238, v74\n"
    "v_cmp_eq_u32 vcc, -1, v238\n"
    "s_nop 0\n"
    "s_cbranch_vccnz .Lf_hslow57\n"
    ".Lf_hback58:\n"
    "v_mfma_f32_16x16x32_bf16 v[32:35], a[0:3], v[64:67], 0\n"
    "buffer_load_dwordx4 v[132:135], v237, s[8:11], s49 offen offset:0 sc1\n"
    "v_mfma_f32_16x16x32_bf16 v[36:39], a[4:7], v[64:67], 0\n"
    "s_or_b32 s98, s5, s6\n"
    "s_or_b32 s98, s98, s4\n"
    "v_mfma_f32_16x16x32_bf16 v[40:43], a[8:11], v[64:67], 0\n"
    "s_cmp_eq_u32 s98, 0\n"
    "s_cselect_b32 s98, -1, 0\n"
    "v_mfma_f32_16x16x32_bf16 v[44:47], a[12:15], v[64:67], 0\n"
    "s_mov_b32 s99, 0\n"
    "s_mov_b32 s64, 970752\n"
    "v_mfma_f32_16x16x32_bf16 v[48:51], a[16:19], v[64:67], 0\n"
    "buffer_load_dwordx4 v[136:139], v236, s[8:11], s49 offen offset:2048 sc1\n"
    "v_mfma_f32_16x16x32_bf16 v[52:55], a[0:3], v[68:71], 0\n"
    "v_lshlrev_b32 v9, 8, v1\n"
    "v_mfma_f32_16x16x32_bf16 v[56:59], a[4:7], v[68:71], 0\n"
    "v_mfma_f32_16x16x32_bf16 v[60:63], a[8:11], v[68:71], 0\n"
    "v_mfma_f32_16x16x32_bf16 v[240:243], a[12:15], v[68:71], 0\n"
    "buffer_load_dwordx4 v[140:143], v237, s[8:11], s49 offen offset:2048 sc1\n"
    "v_mfma_f32_16x16x32_bf16 v[244:247], a[16:19], v[68:71], 0\n"
    "v_mfma_f32_16x16x32_bf16 v[32:35], a[20:23], v[72:75], v[32:35]\n"
    "v_mfma_f32_16x16x32_bf16 v[36:39], a[24:27], v[72:75], v[36:39]\n"
    "v_mfma_f32_16x16x32_bf16 v[40:43], a[28:31], v[72:75], v[40:43]\n"
    "buffer_load_dwordx4 v[144:147], v236, s[8:11], s85 offen offset:0 sc1\n"
    "v_mfma_f32_16x16x32_bf16 v[44:47], a[32:35], v[72:75], v[44:47]\n"
    "v_mfma_f32_16x16x32_bf16 v[48:51], a[36:39], v[72:75], v[48:51]\n"
    "s_add_u32 s67, s30, 3932160\n"
    "buffer_load_dwordx4 v[184:187], v12, s[8:11], s67 offen sc1\n"
    "v_mfma_f32_16x16x32_bf16 v[52:55], a[20:23], v[76:79], v[52:55]\n"
    "v_mfma_f32_16x16x32_bf16 v[56:59], a[24:27], v[76:79], v[56:59]\n"
    "buffer_load_dwordx4 v[148:151], v237, s[8:11], s85 offen offset:0 sc1\n"
    "s_waitcnt vmcnt(15)\n"
    "v_max3_u32 v238, v94, v92, v80\n"
    "v_max3_u32 v238, v238, v82, v84\n"
    "v_mfma_f32_16x16x32_bf16 v[60:63], a[28:31], v[76:79], v[60:63]\n"
    "s_add_u32 s68, s31, 3932160\n"
    "buffer_load_dword v197, v13, s[8:11], s68 offen sc1\n"
    "v_max3_u32 v238, v238, v86, v88\n"
    "v_max_u32 v238, v238, v90\n"
    "v_mfma_f32_16x16x32_bf16 v[240:243], a[32:35], v[76:79], v[240:243]\n"
    "v_cmp_eq_u32 vcc, -1, v238\n"
    "v_mfma_f32_16x16x32_bf16 v[244:247], a[36:39], v[76:79], v[244:247]\n"
    "s_cbranch_vccnz .Lf_hslow60\n"
    ".Lf_hback61:\n"
    "v_mfma_f32_16x16x32_bf16 v[32:35], a[40:43], v[80:83], v[32:35]\n"
    "buffer_load_dwordx4 v[152:155], v236, s[8:11], s85 offen offset:2048 sc1\n"
    "v_mfma_f32_16x16x32_bf16 v[36:39], a[44:47], v[80:83], v[36:39]\n"
    "v_mfma_f32_16x16x32_bf16 v[40:43], a[48:51], v[80:83], v[40:43]\n"
    "v_mfma_f32_16x16x32_bf16 v[44:47], a[52:55], v[80:83], v[44:47]\n"
    "v_mfma_f32_16x16x32_bf16 v[48:51], a[56:59], v[80:83], v[48:51]\n"
    "buffer_load_dwordx4 v[156:159], v237, s[8:11], s85 offen offset:2048 sc1\n"
    "v_mfma_f32_16x16x32_bf16 v[52:55], a[40:43], v[84:87], v[52:55]\n"
    "v_mfma_f32_16x16x32_bf16 v[56:59], a[44:47], v[84:87], v[56:59]\n"
    "v_mfma_f32_16x16x32_bf16 v[60:63], a[48:51], v[84:87], v[60:63]\n"
    "v_mfma_f32_16x16x32_bf16 v[240:243], a[52:55], v[84:87], v[240:243]\n"
    "v_mfma_f32_16x16x32_bf16 v[244:247], a[56:59], v[84:87], v[244:247]\n"
    "v_mfma_f32_16x16x32_bf16 v[32:35], a[60:63], v[88:91], v[32:35]\n"
    "s_mov_b64 exec, s[98:99]\n"
    "buffer_load_dwordx2 v[248:249], v9, s[8:11], s64 offen sc1\n"
    "s_mov_b64 exec, -1\n"
    "v_mfma_f32_16x16x32_bf16 v[36:39], a[64:67], v[88:91], v[36:39]\n"
    "v_mfma_f32_16x16x32_bf16 v[40:43], a[68:71], v[88:91], v[40:43]\n"
    "v_mfma_f32_16x16x32_bf16 v[44:47], a[72:75], v[88:91], v[44:47]\n"
    "v_mfma_f32_16x16x32_bf16 v[48:51], a[76:79], v[88:91], v[48:51]\n"
    "v_mfma_f32_16x16x32_bf16 v[52:55], a[60:63], v[92:95], v[52:55]\n"
    "v_mfma_f32_16x16x32_bf16 v[56:59], a[64:67], v[92:95], v[56:59]\n"
    "s_waitcnt vmcnt(15)\n"
    "v_max3_u32 v238, v110, v108, v96\n"
    "v_max3_u32 v238, v238, v98, v100\n"
    "v_add_f32 v174, v174, v16\n"
    "v_mfma_f32_16x16x32_bf16 v[60:63], a[68:71], v[92:95], v[60:63]\n"
    "v_max3_u32 v238, v238, v102, v104\n"
    "v_max_u32 v238, v238, v106\n"
    "v_add_f32 v175, v175, v17\n"
    "v_mfma_f32_16x16x32_bf16 v[240:243], a[72:75], v[92:95], v[240:243]\n"
    "v_cmp_eq_u32 vcc, -1, v238\n"
    "v_add_f32 v176, v176, v18\n"
    "v_mfma_f32_16x16x32_bf16 v[244:247], a[76:79], v[92:95], v[244:247]\n"
    "s_cbranch_vccnz .Lf_hslow62\n"
    ".Lf_hback63:\n"
    "v_add_f32 v177, v177, v19\n"
    "v_mfma_f32_16x16x32_bf16 v[32:35], a[80:83], v[96:99], v[32:35]\n"
    "v_add_f32 v178, v178, v20\n"
    "v_add_f32 v179, v179, v21\n"
    "v_mfma_f32_16x16x32_bf16 v[36:39], a[84:87], v[96:99], v[36:39]\n"
    "v_add_f32 v180, v180, v22\n"
    "v_add_f32 v181, v181, v23\n"
    "v_mfma_f32_16x16x32_bf16 v[40:43], a[88:91], v[96:99], v[40:43]\n"
    "s_waitcnt lgkmcnt(7)\n"
    "v_add_f32 v174, v174, v24\n"
    "v_add_f32 v175, v175, v25\n"
    "v_mfma_f32_16x16x32_bf16 v[44:47], a[92:95], v[96:99], v[44:47]\n"
    "v_add_f32 v176, v176, v26\n"
    "v_add_f32 v177, v177, v27\n"
    "v_mfma_f32_16x16x32_bf16 v[48:51], a[96:99], v[96:99], v[48:51]\n"
    "s_waitcnt lgkmcnt(4)\n"
    "v_add_f32 v178, v178, v28\n"
    "v_add_f32 v179, v179, v29\n"
    "v_mfma_f32_16x16x32_bf16 v[52:55], a[80:83], v[100:103], v[52:55]\n"
    "v_add_f32 v180, v180, v30\n"
    "v_add_f32 v181, v181, v31\n"
    "v_mfma_f32_16x16x32_bf16 v[56:59], a[84:87], v[100:103], v[56:59]\n"
    "v_add_f32 v174, v174, v212\n"
    "v_add_f32 v175, v175, v213\n"
    "v_mfma_f32_16x16x32_bf16 v[60:63], a[88:91], v[100:103], v[60:63]\n"
    "v_add_f32 v176, v176, v214\n"
    "v_add_f32 v177, v177, v215\n"
    "v_mfma_f32_16x16x32_bf16 v[240:243], a[92:95], v[100:103], v[240:243]\n"
    "s_waitcnt lgkmcnt(3)\n"
    "v_add_f32 v178, v178, v216\n"
    "v_add_f32 v179, v179, v217\n"
    "v_mfma_f32_16x16x32_bf16 v[244:247], a[96:99], v[100:103], v[244:247]\n"
    "v_add_f32 v180, v180, v218\n"
    "v_add_f32 v181, v181, v219\n"
    "v_mfma_f32_16x16x32_bf16 v[32:35], a[100:103], v[104:107], v[32:35]\n"
    "v_add_f32 v174, v174, v220\n"
    "v_add_f32 v175, v175, v221\n"
    "v_mfma_f32_16x16x32_bf16 v[36:39], a[104:107], v[104:107], v[36:39]\n"
    "v_add_f32 v176, v176, v222\n"
    "v_add_f32 v177, v177, v223\n"
    "v_mfma_f32_16x16x32_bf16 v[40:43], a[108:111], v[104:107], v[40:43]\n"
    "s_waitcnt lgkmcnt(2)\n"
    "v_add_f32 v178, v178, v224\n"
    "v_add_f32 v179, v179, v225\n"
    "v_mfma_f32_16x16x32_bf16 v[44:47], a[112:115], v[104:107], v[44:47]\n"
    "v_add_f32 v180, v180, v226\n"
    "v_add_f32 v181, v181, v227\n"
    "v_mfma_f32_16x16x32_bf16 v[48:51], a[116:119], v[104:107], v[48:51]\n"
    "v_add_f32 v182, v182, v208\n"
    "v_add_f32 v183, v183, v209\n"
    "v_mfma_f32_16x16x32_bf16 v[52:55], a[100:103], v[108:111], v[52:55]\n"
    "s_waitcnt lgkmcnt(1)\n"
    "v_add_f32 v182, v182, v228\n"
    "v_add_f32 v183, v183, v229\n"
    "v_mfma_f32_16x16x32_bf16 v[56:59], a[104:107], v[108:111], v[56:59]\n"
    "s_waitcnt vmcnt(11)\n"
    "v_max3_u32 v238, v126, v124, v112\n"
    "v_max3_u32 v238, v238, v114, v116\n"
    "v_add_f32 v182, v182, v230\n"
    "v_mfma_f32_16x16x32_bf16 v[60:63], a[108:111], v[108:111], v[60:63]\n"
    "v_max3_u32 v238, v238, v118, v120\n"
    "v_max_u32 v238, v238, v122\n"
    "v_add_f32 v183, v183, v231\n"
    "v_mfma_f32_16x16x32_bf16 v[240:243], a[112:115], v[108:111], v[240:243]\n"
    "v_cmp_eq_u32 vcc, -1, v238\n"
    "s_waitcnt lgkmcnt(0)\n"
    "v_add_f32 v182, v182, v210\n"
    "v_mfma_f32_16x16x32_bf16 v[244:247], a[116:119], v[108:111], v[244:247]\n"
    "s_cbranch_vccnz .Lf_hslow64\n"
    ".Lf_hback65:\n"
    "v_add_f32 v183, v183, v211\n"
    "v_mfma_f32_16x16x32_bf16 v[32:35], a[120:123], v[112:115], v[32:35]\n"
    "v_cvt_pk_bf16_f32 v164, v182, v183\n"
    "v_cvt_pk_bf16_f32 v160, v174, v175\n"
    "v_mfma_f32_16x16x32_bf16 v[36:39], a[124:127], v[112:115], v[36:39]\n"
    "v_mov_b32 v165, v164\n"
    "v_cvt_pk_bf16_f32 v161, v176, v177\n"
    "v_mfma_f32_16x16x32_bf16 v[40:43], a[128:131], v[112:115], v[40:43]\n"
    "v_cvt_pk_bf16_f32 v162, v178, v179\n"
    "v_cvt_pk_bf16_f32 v163, v180, v181\n"
    "v_mfma_f32_16x16x32_bf16 v[44:47], a[132:135], v[112:115], v[44:47]\n"
    "v_permlane16_swap_b32 v164, v165\n"
    "v_mfma_f32_16x16x32_bf16 v[48:51], a[136:139], v[112:115], v[48:51]\n"
    "v_mov_b32 v166, v164\n"
    "v_mov_b32 v167, v165\n"
    "v_mfma_f32_16x16x32_bf16 v[52:55], a[120:123], v[116:119], v[52:55]\n"
    "buffer_store_dwordx4 v[160:163], v14, s[44:47], s38 offen sc1\n"
    "v_mfma_f32_16x16x32_bf16 v[56:59], a[124:127], v[116:119], v[56:59]\n"
    "v_permlane32_swap_b32 v164, v166\n"
    "v_mfma_f32_16x16x32_bf16 v[60:63], a[128:131], v[116:119], v[60:63]\n"
    "v_permlane32_swap_b32 v165, v167\n"
    "v_mfma_f32_16x16x32_bf16 v[240:243], a[132:135], v[116:119], v[240:243]\n"
    "s_mov_b64 exec, 0xffff\n"
    "buffer_store_dwordx4 v[164:167], v14, s[44:47], s39 offen sc1\n"
    "s_mov_b64 exec, -1\n"
    "v_mfma_f32_16x16x32_bf16 v[244:247], a[136:139], v[116:119], v[244:247]\n"
    "v_mfma_f32_16x16x32_bf16 v[32:35], a[140:143], v[120:123], v[32:35]\n"
    "v_mfma_f32_16x16x32_bf16 v[36:39], a[144:147], v[120:123], v[36:39]\n"
    "v_mfma_f32_16x16x32_bf16 v[40:43], a[148:151], v[120:123], v[40:43]\n"
    "v_mfma_f32_16x16x32_bf16 v[44:47], a[152:155], v[120:123], v[44:47]\n"
    "v_mfma_f32_16x16x32_bf16 v[48:51], a[156:159], v[120:123], v[48:51]\n"
    "v_mfma_f32_16x16x32_bf16 v[52:55], a[140:143], v[124:127], v[52:55]\n"
    "v_mfma_f32_16x16x32_bf16 v[56:59], a[144:147], v[124:127], v[56:59]\n"
    "v_mfma_f32_16x16x32_bf16 v[60:63], a[148:151], v[124:127], v[60:63]\n"
    "s_waitcnt vmcnt(11)\n"
    "v_max3_u32 v238, v134, v132, v128\n"
    "v_max_u32 v238, v238, v130\n"
    "s_waitcnt vmcnt(7)\n"
    "v_lshlrev_b32 v198, 16, v184\n"
    "v_mfma_f32_16x16x32_bf16 v[240:243], a[152:155], v[124:127], v[240:243]\n"
    "v_cmp_eq_u32 vcc, -1, v238\n"
    "v_and_b32 v199, 0xffff0000, v184\n"
    "v_mfma_f32_16x16x32_bf16 v[244:247], a[156:159], v[124:127], v[244:247]\n"
    "s_cbranch_vccnz .Lf_hslow66\n"
    ".Lf_hback67:\n"
    "v_lshlrev_b32 v200, 16, v185\n"
    "v_mfma_f32_16x16x32_bf16 v[32:35], a[160:163], v[128:131], v[32:35]\n"
    "v_and_b32 v201, 0xffff0000, v185\n"
    "v_lshlrev_b32 v202, 16, v186\n"
    "v_mfma_f32_16x16x32_bf16 v[36:39], a[164:167], v[128:131], v[36:39]\n"
    "v_and_b32 v203, 0xffff0000, v186\n"
    "v_lshlrev_b32 v204, 16, v187\n"
    "v_mfma_f32_16x16x32_bf16 v[40:43], a[168:171], v[128:131], v[40:43]\n"
    "v_and_b32 v205, 0xffff0000, v187\n"
    "s_waitcnt vmcnt(5)\n"
    "v_lshlrev_b32 v206, 16, v197\n"
    "v_mfma_f32_16x16x32_bf16 v[44:47], a[172:175], v[128:131], v[44:47]\n"
    "v_and_b32 v207, 0xffff0000, v197\n"
    "v_mfma_f32_16x16x32_bf16 v[48:51], a[176:179], v[128:131], v[48:51]\n"
    "v_mfma_f32_16x16x32_bf16 v[52:55], a[160:163], v[132:135], v[52:55]\n"
    "s_waitcnt vmcnt(3)\n"
    "v_max3_u32 v238, v158, v156, v136\n"
    "v_max3_u32 v238, v238, v138, v140\n"
    "v_mfma_f32_16x16x32_bf16 v[56:59], a[164:167], v[132:135], v[56:59]\n"
    "v_max3_u32 v238, v238, v142, v144\n"
    "v_max3_u32 v238, v238, v146, v148\n"
    "v_mfma_f32_16x16x32_bf16 v[60:63], a[168:171], v[132:135], v[60:63]\n"
    "v_max3_u32 v238, v238, v150, v152\n"
    "v_max_u32 v238, v238, v154\n"
    "v_mfma_f32_16x16x32_bf16 v[240:243], a[172:175], v[132:135], v[240:243]\n"
    "v_cmp_eq_u32 vcc, -1, v238\n"
    "v_mfma_f32_16x16x32_bf16 v[244:247], a[176:179], v[132:135], v[244:247]\n"
    "s_cbranch_vccnz .Lf_hslow68\n"
    ".Lf_hback69:\n"
    "v_mfma_f32_16x16x32_bf16 v[40:43], a[188:191], v[136:139], v[40:43]\n"
    "v_mfma_f32_16x16x32_bf16 v[40:43], a[208:211], v[144:147], v[40:43]\n"
    "v_mfma_f32_16x16x32_bf16 v[40:43], a[228:231], v[152:155], v[40:43]\n"
    "v_mfma_f32_16x16x32_bf16 v[52:55], a[180:183], v[140:143], v[52:55]\n"
    "s_barrier\n"
    "v_mfma_f32_16x16x32_bf16 v[52:55], a[200:203], v[148:151], v[52:55]\n"
    "ds_write_b128 v232, v[40:43]\n"
    "v_mfma_f32_16x16x32_bf16 v[52:55], a[220:223], v[156:159], v[52:55]\n"
    "v_mfma_f32_16x16x32_bf16 v[56:59], a[184:187], v[140:143], v[56:59]\n"
    "v_mfma_f32_16x16x32_bf16 v[56:59], a[204:207], v[148:151], v[56:59]\n"
    "ds_write_b128 v233, v[52:55] offset:8192\n"
    "v_mfma_f32_16x16x32_bf16 v[56:59], a[224:227], v[156:159], v[56:59]\n"
    "v_mfma_f32_16x16x32_bf16 v[60:63], a[188:191], v[140:143], v[60:63]\n"
    "v_mfma_f32_16x16x32_bf16 v[60:63], a[208:211], v[148:151], v[60:63]\n"
    "ds_write_b128 v233, v[56:59] offset:9216\n"
    "v_mfma_f32_16x16x32_bf16 v[60:63], a[228:231], v[156:159], v[60:63]\n"
    "v_mfma_f32_16x16x32_bf16 v[44:47], a[192:195], v[136:139], v[44:47]\n"
    "v_mfma_f32_16x16x32_bf16 v[44:47], a[212:215], v[144:147], v[44:47]\n"
    "ds_write_b128 v234, v[60:63] offset:16384\n"
    "v_mfma_f32_16x16x32_bf16 v[44:47], a[232:235], v[152:155], v[44:47]\n"
    "v_mfma_f32_16x16x32_bf16 v[240:243], a[192:195], v[140:143], v[240:243]\n"
    "v_mfma_f32_16x16x32_bf16 v[240:243], a[212:215], v[148:151], v[240:243]\n"
    "ds_write_b128 v232, v[44:47] offset:1024\n"
    "s_waitcnt vmcnt(2)\n"
    "v_cmp_eq_u64 s[86:87], s[50:51], v[248:249]\n"
    "v_mfma_f32_16x16x32_bf16 v[240:243], a[232:235], v[156:159], v[240:243]\n"
    "v_mfma_f32_16x16x32_bf16 v[48:51], a[196:199], v[136:139], v[48:51]\n"
    "s_andn2_b32 s79, s98, s86\n"
    "s_cbranch_scc1 .Lf_slow70\n"
    ".Lf_back71:\n"
    "v_mfma_f32_16x16x32_bf16 v[48:51], a[216:219], v[144:147], v[48:51]\n"
    "ds_write_b128 v234, v[240:243] offset:17408\n"
    "v_mfma_f32_16x16x32_bf16 v[48:51], a[236:239], v[152:155], v[48:51]\n"
    "s_and_b64 exec, s[98:99], 1\n"
    "buffer_store_dword v252, off, s[8:11], 0 sc1\n"
    "s_mov_b64 exec, -1\n"
    "v_mfma_f32_16x16x32_bf16 v[244:247], a[196:199], v[140:143], v[244:247]\n"
    "v_mfma_f32_16x16x32_bf16 v[244:247], a[216:219], v[148:151], v[244:247]\n"
    "ds_write_b64 v232, v[50:51] offset:24576\n"
    "v_mfma_f32_16x16x32_bf16 v[244:247], a[236:239], v[156:159], v[244:247]\n"
    "v_mfma_f32_16x16x32_bf16 v[32:35], a[180:183], v[136:139], v[32:35]\n"
    "v_mfma_f32_16x16x32_bf16 v[32:35], a[200:203], v[144:147], v[32:35]\n"
    "ds_write_b64 v233, v[244:245] offset:24584\n"
    "ds_write_b64 v234, v[246:247] offset:25600\n"
    "v_mfma_f32_16x16x32_bf16 v[32:35], a[220:223], v[152:155], v[32:35]\n"
    "s_waitcnt lgkmcnt(0)\n"
    "s_barrier\n"
    "v_mfma_f32_16x16x32_bf16 v[36:39], a[184:187], v[136:139], v[36:39]\n"
    "ds_read_b128 v[40:43], v254 offset:0\n"
    "ds_read_b128 v[52:55], v254 offset:8192\n"
    "ds_read_b128 v[60:63], v254 offset:16384\n"
    "ds_read_b128 v[44:47], v254 offset:1024\n"
    "v_mfma_f32_16x16x32_bf16 v[36:39], a[204:207], v[144:147], v[36:39]\n"
    "ds_read_b128 v[56:59], v254 offset:9216\n"
    "ds_read_b128 v[240:243], v254 offset:17408\n"
    "ds_read_b128 v[244:247], v254 offset:24576\n"
    "ds_read_b64 v[50:51], v254 offset:25600\n"
    "v_mfma_f32_16x16x32_bf16 v[36:39], a[224:227], v[152:155], v[36:39]\n"
    "v_pk_add_f32 v[198:199], v[198:199], v[32:33]\n"
    "v_pk_add_f32 v[200:201], v[200:201], v[34:35]\n"
    "s_waitcnt lgkmcnt(7)\n"
    "v_pk_add_f32 v[198:199], v[198:199], v[40:41]\n"
    "v_pk_add_f32 v[200:201], v[200:201], v[42:43]\n"
    "s_waitcnt lgkmcnt(6)\n"
    "v_pk_add_f32 v[198:199], v[198:199], v[52:53]\n"
    "v_pk_add_f32 v[200:201], v[200:201], v[54:55]\n"
    "s_waitcnt lgkmcnt(5)\n"
    "v_pk_add_f32 v[198:199], v[198:199], v[60:61]\n"
    "v_pk_add_f32 v[200:201], v[200:201], v[62:63]\n"
    "v_pk_add_f32 v[202:203], v[202:203], v[36:37]\n"
    "v_pk_add_f32 v[204:205], v[204:205], v[38:39]\n"
    "s_waitcnt lgkmcnt(4)\n"
    "v_pk_add_f32 v[202:203], v[202:203], v[44:45]\n"
    "v_pk_add_f32 v[204:205], v[204:205], v[46:47]\n"
    "s_waitcnt lgkmcnt(3)\n"
    "v_pk_add_f32 v[202:203], v[202:203], v[56:57]\n"
    "v_pk_add_f32 v[204:205], v[204:205], v[58:59]\n"
    "s_waitcnt lgkmcnt(2)\n"
    "v_pk_add_f32 v[202:203], v[202:203], v[240:241]\n"
    "v_pk_add_f32 v[204:205], v[204:205], v[242:243]\n"
    "v_pk_add_f32 v[206:207], v[206:207], v[48:49]\n"
    "s_waitcnt lgkmcnt(1)\n"
    "v_pk_add_f32 v[206:207], v[206:207], v[244:245]\n"
    "v_pk_add_f32 v[206:207], v[206:207], v[246:247]\n"
    "s_waitcnt lgkmcnt(0)\n"
    "v_pk_add_f32 v[206:207], v[206:207], v[50:51]\n"
    "v_cvt_pk_bf16_f32 v188, v206, v207\n"
    "v_cvt_pk_bf16_f32 v184, v198, v199\n"
    "v_mov_b32 v189, v188\n"
    "v_cvt_pk_bf16_f32 v185, v200, v201\n"
    "v_cvt_pk_bf16_f32 v186, v202, v203\n"
    "v_cvt_pk_bf16_f32 v187, v204, v205\n"
    "v_permlane16_swap_b32 v188, v189\n"
    "v_mov_b32 v190, v188\n"
    "v_mov_b32 v191, v189\n"
    "s_add_u32 s69, s38, 1310720\n"
    "buffer_store_dwordx4 v[184:187], v14, s[44:47], s69 offen\n"
    "v_permlane32_swap_b32 v188, v190\n"
    "v_permlane32_swap_b32 v189, v191\n"
    "s_add_u32 s70, s39, 1310720\n"
    "s_mov_b64 exec, 0xffff\n"
    "buffer_store_dwordx4 v[188:191], v14, s[44:47], s70 offen\n"
    "s_mov_b64 exec, -1\n"
    ".Lf_fin:\n"
    "s_endpgm\n"
    ".Lf_main_end:\n"
    ".Lf_gslow3:\n"
    "s_sleep 2\n"
    "buffer_load_dwordx4 v[208:211], v254, s[8:11], s66 offen offset:0 sc1\n"
    "buffer_load_dwordx4 v[212:215], v254, s[8:11], s66 offen offset:128 sc1\n"
    "buffer_load_dwordx4 v[216:219], v254, s[8:11], s66 offen offset:2048 sc1\n"
    "buffer_load_dwordx4 v[220:223], v254, s[8:11], s66 offen offset:2176 sc1\n"
    "s_waitcnt vmcnt(3)\n"
    "v_xor_b32 v0, s50, v209\n"
    "v_bitop3_b32 v0, v0, v211, s50 bitop3:0xf6\n"
    "s_waitcnt vmcnt(2)\n"
    "v_bitop3_b32 v0, v0, v213, s50 bitop3:0xf6\n"
    "v_bitop3_b32 v0, v0, v215, s50 bitop3:0xf6\n"
    "s_waitcnt vmcnt(1)\n"
    "v_bitop3_b32 v0, v0, v217, s50 bitop3:0xf6\n"
    "v_bitop3_b32 v0, v0, v219, s50 bitop3:0xf6\n"
    "s_waitcnt vmcnt(0)\n"
    "v_bitop3_b32 v0, v0, v221, s50 bitop3:0xf6\n"
    "v_bitop3_b32 v0, v0, v223, s50 bitop3:0xf6\n"
    "v_cmp_ne_u32 vcc, 0, v0\n"
    "s_nop 0\n"
    "s_cmp_lg_u64 vcc, 0\n"
    "s_cbranch_scc0 .Lf_gback4\n"
    "s_add_i32 s7, s7, -1\n"
    "s_cmp_gt_i32 s7, 0\n"
    "s_cbranch_scc1 .Lf_gslow3\n"
    "s_branch .Lf_gback4\n"
    ".Lf_gslow5:\n"
    "s_sleep 2\n"
    "buffer_load_dwordx4 v[224:227], v254, s[8:11], s67 offen offset:0 sc1\n"
    "buffer_load_dwordx4 v[228:231], v254, s[8:11], s67 offen offset:128 sc1\n"
    "buffer_load_dwordx4 v[232:235], v254, s[8:11], s67 offen offset:2048 sc1\n"
    "buffer_load_dwordx4 v[236:239], v254, s[8:11], s67 offen offset:2176 sc1\n"
    "s_waitcnt vmcnt(3)\n"
    "v_xor_b32 v0, s50, v225\n"
    "v_bitop3_b32 v0, v0, v227, s50 bitop3:0xf6\n"
    "s_waitcnt vmcnt(2)\n"
    "v_bitop3_b32 v0, v0, v229, s50 bitop3:0xf6\n"
    "v_bitop3_b32 v0, v0, v231, s50 bitop3:0xf6\n"
    "s_waitcnt vmcnt(1)\n"
    "v_bitop3_b32 v0, v0, v233, s50 bitop3:0xf6\n"
    "v_bitop3_b32 v0, v0, v235, s50 bitop3:0xf6\n"
    "s_waitcnt vmcnt(0)\n"
    "v_bitop3_b32 v0, v0, v237, s50 bitop3:0xf6\n"
    "v_bitop3_b32 v0, v0, v239, s50 bitop3:0xf6\n"
    "v_cmp_ne_u32 vcc, 0, v0\n"
    "s_nop 0\n"
    "s_cmp_lg_u64 vcc, 0\n"
    "s_cbranch_scc0 .Lf_gback6\n"
    "s_add_i32 s7, s7, -1\n"
    "s_cmp_gt_i32 s7, 0\n"
    "s_cbranch_scc1 .Lf_gslow5\n"
    "s_branch .Lf_gback6\n"
    ".Lf_gslow7:\n"
    "s_sleep 4\n"
    "buffer_load_dwordx4 v[232:235], v158, s[8:11], s64 offen sc1\n"
    "buffer_load_dwordx4 v[212:215], v153, s[8:11], s68 offen sc1\n"
    "buffer_load_dwordx4 v[216:219], v153, s[8:11], s69 offen sc1\n"
    "buffer_load_dwordx2 v[220:221], v154, s[8:11], s70 offen sc1\n"
    "s_waitcnt vmcnt(3)\n"
    "v_xor_b32 v246, s50, v233\n"
    "v_bitop3_b32 v246, v246, v235, s50 bitop3:0xf6\n"
    "s_waitcnt vmcnt(2)\n"
    "v_bitop3_b32 v246, v246, v213, s50 bitop3:0xf6\n"
    "v_bitop3_b32 v246, v246, v215, s50 bitop3:0xf6\n"
    "s_waitcnt vmcnt(1)\n"
    "v_bitop3_b32 v246, v246, v217, s50 bitop3:0xf6\n"
    "v_bitop3_b32 v246, v246, v219, s50 bitop3:0xf6\n"
    "s_waitcnt vmcnt(0)\n"
    "v_bitop3_b32 v246, v246, v221, s50 bitop3:0xf6\n"
    "v_cmp_ne_u32 vcc, 0, v246\n"
    "s_nop 0\n"
    "s_cmp_lg_u64 vcc, 0\n"
    "s_cbranch_scc0 .Lf_gback8\n"
    "s_add_i32 s7, s7, -1\n"
    "s_cmp_gt_i32 s7, 0\n"
    "s_cbranch_scc1 .Lf_gslow7\n"
    "s_branch .Lf_gback8\n"
    ".Lf_slow9:\n"
    "s_sleep 4\n"
    "buffer_load_dwordx2 v[250:251], v175, s[8:11], s78 offen sc1\n"
    "s_waitcnt vmcnt(0)\n"
    "v_cmp_eq_u64 vcc, s[50:51], v[250:251]\n"
    "s_nop 0\n"
    "s_andn2_b32 s79, 0xfff, vcc_lo\n"
    "s_cbranch_scc0 .Lf_back10\n"
    "s_add_i32 s7, s7, -1\n"
    "s_cmp_gt_i32 s7, 0\n"
    "s_cbranch_scc1 .Lf_slow9\n"
    "s_branch .Lf_back10\n"
    ".Lf_gslow12:\n"
    "s_sleep 4\n"
    "buffer_load_dwordx4 v[188:191], v158, s[8:11], s72 offen sc1\n"
    "buffer_load_dwordx4 v[212:215], v153, s[8:11], s68 offen sc1\n"
    "buffer_load_dwordx4 v[216:219], v153, s[8:11], s69 offen sc1\n"
    "buffer_load_dwordx2 v[220:221], v154, s[8:11], s70 offen sc1\n"
    "s_waitcnt vmcnt(3)\n"
    "v_xor_b32 v246, s50, v189\n"
    "v_bitop3_b32 v246, v246, v191, s50 bitop3:0xf6\n"
    "s_waitcnt vmcnt(2)\n"
    "v_bitop3_b32 v246, v246, v213, s50 bitop3:0xf6\n"
    "v_bitop3_b32 v246, v246, v215, s50 bitop3:0xf6\n"
    "s_waitcnt vmcnt(1)\n"
    "v_bitop3_b32 v246, v246, v217, s50 bitop3:0xf6\n"
    "v_bitop3_b32 v246, v246, v219, s50 bitop3:0xf6\n"
    "s_waitcnt vmcnt(0)\n"
    "v_bitop3_b32 v246, v246, v221, s50 bitop3:0xf6\n"
    "v_cmp_ne_u32 vcc, 0, v246\n"
    "s_nop 0\n"
    "s_cmp_lg_u64 vcc, 0\n"
    "s_cbranch_scc0 .Lf_gback13\n"
    "s_add_i32 s7, s7, -1\n"
    "s_cmp_gt_i32 s7, 0\n"
    "s_cbranch_scc1 .Lf_gslow12\n"
    "s_branch .Lf_gback13\n"
    ".Lf_slow14:\n"
    "s_sleep 4\n"
    "buffer_load_dwordx2 v[244:245], v175, s[8:11], s78 offen sc1\n"
    "s_waitcnt vmcnt(0)\n"
    "v_cmp_eq_u64 vcc, s[50:51], v[244:245]\n"
    "s_nop 0\n"
    "s_andn2_b32 s79, 0xfff, vcc_lo\n"
    "s_cbranch_scc0 .Lf_back15\n"
    "s_add_i32 s7, s7, -1\n"
    "s_cmp_gt_i32 s7, 0\n"
    "s_cbranch_scc1 .Lf_slow14\n"
    "s_branch .Lf_back15\n"
    ".Lf_slow16:\n"
    "s_sleep 4\n"
    "buffer_load_dwordx2 v[244:245], v175, s[8:11], s78 offen sc1\n"
    "s_waitcnt vmcnt(0)\n"
    "v_cmp_eq_u64 vcc, s[50:51], v[244:245]\n"
    "s_nop 0\n"
    "s_andn2_b32 s79, 0xfff, vcc_lo\n"
    "s_cbranch_scc0 .Lf_back17\n"
    "s_add_i32 s7, s7, -1\n"
    "s_cmp_gt_i32 s7, 0\n"
    "s_cbranch_scc1 .Lf_slow16\n"
    "s_branch .Lf_back17\n"
    ".Lf_gslow18:\n"
    "s_sleep 4\n"
    "buffer_load_dwordx4 v[188:191], v158, s[8:11], s72 offen sc1\n"
    "buffer_load_dwordx4 v[212:215], v153, s[8:11], s68 offen sc1\n"
    "buffer_load_dwordx4 v[216:219], v153, s[8:11], s69 offen sc1\n"
    "buffer_load_dwordx2 v[220:221], v154, s[8:11], s70 offen sc1\n"
    "s_waitcnt vmcnt(3)\n"
    "v_xor_b32 v246, s50, v189\n"
    "v_bitop3_b32 v246, v246, v191, s50 bitop3:0xf6\n"
    "s_waitcnt vmcnt(2)\n"
    "v_bitop3_b32 v246, v246, v213, s50 bitop3:0xf6\n"
    "v_bitop3_b32 v246, v246, v215, s50 bitop3:0xf6\n"
    "s_waitcnt vmcnt(1)\n"
    "v_bitop3_b32 v246, v246, v217, s50 bitop3:0xf6\n"
    "v_bitop3_b32 v246, v246, v219, s50 bitop3:0xf6\n"
    "s_waitcnt vmcnt(0)\n"
    "v_bitop3_b32 v246, v246, v221, s50 bitop3:0xf6\n"
    "v_cmp_ne_u32 vcc, 0, v246\n"
    "s_nop 0\n"
    "s_cmp_lg_u64 vcc, 0\n"
    "s_cbranch_scc0 .Lf_gback19\n"
    "s_add_i32 s7, s7, -1\n"
    "s_cmp_gt_i32 s7, 0\n"
    "s_cbranch_scc1 .Lf_gslow18\n"
    "s_branch .Lf_gback19\n"
    ".Lf_slow20:\n"
    "s_sleep 2\n"
    "buffer_load_dwordx2 v[238:239], v4, s[8:11], s83 offen sc1\n"
    "s_waitcnt vmcnt(0)\n"
    "v_cmp_eq_u64 s[86:87], s[50:51], v[238:239]\n"
    "s_nop 0\n"
    "s_andn2_b32 s79, 0xff, s86\n"
    "s_cbranch_scc0 .Lf_back21\n"
    "s_add_i32 s7, s7, -1\n"
    "s_cmp_gt_i32 s7, 0\n"
    "s_cbranch_scc1 .Lf_slow20\n"
    "s_branch .Lf_back21\n"
    ".Lf_gslow22:\n"
    "s_sleep 4\n"
    "buffer_load_dwordx4 v[48:51], v62, s[8:11], s84 offen sc1\n"
    "buffer_load_dwordx4 v[188:191], v14, s[8:11], s74 offen sc1\n"
    "buffer_load_dwordx4 v[192:195], v14, s[8:11], s75 offen sc1\n"
    "buffer_load_dwordx2 v[196:197], v5, s[8:11], s77 offen sc1\n"
    "s_waitcnt vmcnt(3)\n"
    "v_xor_b32 v248, s50, v49\n"
    "v_bitop3_b32 v248, v248, v51, s50 bitop3:0xf6\n"
    "s_waitcnt vmcnt(2)\n"
    "v_bitop3_b32 v248, v248, v189, s50 bitop3:0xf6\n"
    "v_bitop3_b32 v248, v248, v191, s50 bitop3:0xf6\n"
    "s_waitcnt vmcnt(1)\n"
    "v_bitop3_b32 v248, v248, v193, s50 bitop3:0xf6\n"
    "v_bitop3_b32 v248, v248, v195, s50 bitop3:0xf6\n"
    "s_waitcnt vmcnt(0)\n"
    "v_bitop3_b32 v248, v248, v197, s50 bitop3:0xf6\n"
    "v_cmp_ne_u32 vcc, 0, v248\n"
    "s_nop 0\n"
    "s_cmp_lg_u64 vcc, 0\n"
    "s_cbranch_scc0 .Lf_gback23\n"
    "s_add_i32 s7, s7, -1\n"
    "s_cmp_gt_i32 s7, 0\n"
    "s_cbranch_scc1 .Lf_gslow22\n"
    "s_branch .Lf_gback23\n"
    ".Lf_slow24:\n"
    "buffer_load_dwordx2 v[238:239], v4, s[8:11], s83 offen sc1\n"
    "s_waitcnt vmcnt(0)\n"
    "v_cmp_eq_u64 s[86:87], s[50:51], v[238:239]\n"
    "s_nop 0\n"
    "s_andn2_b32 s79, 0xff, s86\n"
    "s_cbranch_scc0 .Lf_back25\n"
    "s_add_i32 s7, s7, -1\n"
    "s_cmp_gt_i32 s7, 0\n"
    "s_cbranch_scc1 .Lf_slow24\n"
    "s_branch .Lf_back25\n"
    ".Lf_slow26:\n"
    "buffer_load_dwordx2 v[238:239], v4, s[8:11], s83 offen sc1\n"
    "s_waitcnt vmcnt(0)\n"
    "v_cmp_eq_u64 s[86:87], s[50:51], v[238:239]\n"
    "s_nop 0\n"
    "s_andn2_b32 s79, 0xff, s86\n"
    "s_cbranch_scc0 .Lf_back27\n"
    "s_add_i32 s7, s7, -1\n"
    "s_cmp_gt_i32 s7, 0\n"
    "s_cbranch_scc1 .Lf_slow26\n"
    "s_branch .Lf_back27\n"
    ".Lf_slow28:\n"
    "buffer_load_dwordx2 v[238:239], v4, s[8:11], s83 offen sc1\n"
    "s_waitcnt vmcnt(0)\n"
    "v_cmp_eq_u64 s[86:87], s[50:51], v[238:239]\n"
    "s_nop 0\n"
    "s_andn2_b32 s79, 0xff, s86\n"
    "s_cbranch_scc0 .Lf_back29\n"
    "s_add_i32 s7, s7, -1\n"
    "s_cmp_gt_i32 s7, 0\n"
    "s_cbranch_scc1 .Lf_slow28\n"
    "s_branch .Lf_back29\n"
    ".Lf_slow30:\n"
    "buffer_load_dwordx2 v[238:239], v4, s[8:11], s83 offen sc1\n"
    "s_waitcnt vmcnt(0)\n"
    "v_cmp_eq_u64 s[86:87], s[50:51], v[238:239]\n"
    "s_nop 0\n"
    "s_andn2_b32 s79, 0xff, s86\n"
    "s_cbranch_scc0 .Lf_back31\n"
    "s_add_i32 s7, s7, -1\n"
    "s_cmp_gt_i32 s7, 0\n"
    "s_cbranch_scc1 .Lf_slow30\n"
    "s_branch .Lf_back31\n"
    ".Lf_gslow32:\n"
    "buffer_load_dwordx4 v[48:51], v8, s[8:11], s84 offen offset:0 sc1\n"
    "buffer_load_dwordx4 v[52:55], v8, s[8:11], s84 offen offset:512 sc1\n"
    "buffer_load_dwordx4 v[56:59], v8, s[8:11], s84 offen offset:1024 sc1\n"
    "buffer_load_dwordx4 v[60:63], v8, s[8:11], s84 offen offset:1536 sc1\n"
    "s_waitcnt vmcnt(3)\n"
    "v_xor_b32 v248, s50, v49\n"
    "v_bitop3_b32 v248, v248, v51, s50 bitop3:0xf6\n"
    "s_waitcnt vmcnt(2)\n"
    "v_bitop3_b32 v248, v248, v53, s50 bitop3:0xf6\n"
    "v_bitop3_b32 v248, v248, v55, s50 bitop3:0xf6\n"
    "s_waitcnt vmcnt(1)\n"
    "v_bitop3_b32 v248, v248, v57, s50 bitop3:0xf6\n"
    "v_bitop3_b32 v248, v248, v59, s50 bitop3:0xf6\n"
    "s_waitcnt vmcnt(0)\n"
    "v_bitop3_b32 v248, v248, v61, s50 bitop3:0xf6\n"
    "v_bitop3_b32 v248, v248, v63, s50 bitop3:0xf6\n"
    "v_cmp_ne_u32 vcc, 0, v248\n"
    "s_nop 0\n"
    "s_cmp_lg_u64 vcc, 0\n"
    "s_cbranch_scc0 .Lf_gback33\n"
    "s_add_i32 s7, s7, -1\n"
    "s_cmp_gt_i32 s7, 0\n"
    "s_cbranch_scc1 .Lf_gslow32\n"
    "s_branch .Lf_gback33\n"
    ".Lf_xall36:\n"
    "buffer_load_dwordx4 v[64:67], v235, s[8:11], s12 offen offset:0 sc1\n"
    "buffer_load_dwordx4 v[68:71], v235, s[8:11], s12 offen offset:256 sc1\n"
    "buffer_load_dwordx4 v[72:75], v235, s[8:11], s12 offen offset:2048 sc1\n"
    "buffer_load_dwordx4 v[76:79], v235, s[8:11], s12 offen offset:2304 sc1\n"
    "buffer_load_dwordx4 v[80:83], v235, s[8:11], s13 offen offset:0 sc1\n"
    "buffer_load_dwordx4 v[84:87], v235, s[8:11], s13 offen offset:256 sc1\n"
    "buffer_load_dwordx4 v[88:91], v235, s[8:11], s13 offen offset:2048 sc1\n"
    "buffer_load_dwordx4 v[92:95], v235, s[8:11], s13 offen offset:2304 sc1\n"
    "buffer_load_dwordx4 v[96:99], v235, s[8:11], s14 offen offset:0 sc1\n"
    "buffer_load_dwordx4 v[100:103], v235, s[8:11], s14 offen offset:256 sc1\n"
    "buffer_load_dwordx4 v[104:107], v235, s[8:11], s14 offen offset:2048 sc1\n"
    "buffer_load_dwordx4 v[108:111], v235, s[8:11], s14 offen offset:2304 sc1\n"
    "buffer_load_dwordx4 v[112:115], v235, s[8:11], s15 offen offset:0 sc1\n"
    "buffer_load_dwordx4 v[116:119], v235, s[8:11], s15 offen offset:256 sc1\n"
    "s_waitcnt vmcnt(0)\n"
    "v_max3_u32 v236, v64, v66, v68\n"
    "v_max3_u32 v236, v236, v70, v72\n"
    "v_max3_u32 v236, v236, v74, v76\n"
    "v_max3_u32 v236, v236, v78, v80\n"
    "v_max3_u32 v236, v236, v82, v84\n"
    "v_max3_u32 v236, v236, v86, v88\n"
    "v_max3_u32 v236, v236, v90, v92\n"
    "v_max3_u32 v236, v236, v94, v96\n"
    "v_max3_u32 v236, v236, v98, v100\n"
    "v_max3_u32 v236, v236, v102, v104\n"
    "v_max3_u32 v236, v236, v106, v108\n"
    "v_max3_u32 v236, v236, v110, v112\n"
    "v_max3_u32 v236, v236, v114, v116\n"
    "v_max_u32 v236, v236, v118\n"
    "v_cmp_eq_u32 vcc, -1, v236\n"
    "s_nop 0\n"
    "s_cbranch_vccz .Lf_xout72\n"
    "s_add_i32 s7, s7, -1\n"
    "s_cmp_gt_i32 s7, 0\n"
    "s_cbranch_scc1 .Lf_xall36\n"
    ".Lf_xout72:\n"
    "s_setpc_b64 s[62:63]\n"
    ".Lf_xslow34:\n"
    "s_call_b64 s[62:63], .Lf_xall36\n"
    "s_branch .Lf_xback35\n"
    ".Lf_xslow37:\n"
    "s_call_b64 s[62:63], .Lf_xall36\n"
    "s_branch .Lf_xback38\n"
    ".Lf_xslow39:\n"
    "s_call_b64 s[62:63], .Lf_xall36\n"
    "s_branch .Lf_xback40\n"
    ".Lf_xslow41:\n"
    "s_call_b64 s[62:63], .Lf_xall36\n"
    "s_branch .Lf_xback42\n"
    ".Lf_slow43:\n"
    "buffer_load_dwordx2 v[238:239], v4, s[8:11], s83 offen sc1\n"
    "s_waitcnt vmcnt(0)\n"
    "v_cmp_eq_u64 s[86:87], s[50:51], v[238:239]\n"
    "s_nop 0\n"
    "s_andn2_b32 s79, 0xff, s86\n"
    "s_cbranch_scc0 .Lf_back44\n"
    "s_add_i32 s7, s7, -1\n"
    "s_cmp_gt_i32 s7, 0\n"
    "s_cbranch_scc1 .Lf_slow43\n"
    "s_branch .Lf_back44\n"
    ".Lf_xslow45:\n"
    "s_call_b64 s[62:63], .Lf_xall36\n"
    "s_branch .Lf_xback46\n"
    ".Lf_gslow47:\n"
    "buffer_load_dwordx4 v[144:147], v8, s[8:11], s84 offen offset:0 sc1\n"
    "buffer_load_dwordx4 v[148:151], v8, s[8:11], s84 offen offset:512 sc1\n"
    "buffer_load_dwordx4 v[152:155], v8, s[8:11], s84 offen offset:1024 sc1\n"
    "buffer_load_dwordx4 v[156:159], v8, s[8:11], s84 offen offset:1536 sc1\n"
    "s_waitcnt vmcnt(3)\n"
    "v_xor_b32 v248, s50, v145\n"
    "v_bitop3_b32 v248, v248, v147, s50 bitop3:0xf6\n"
    "s_waitcnt vmcnt(2)\n"
    "v_bitop3_b32 v248, v248, v149, s50 bitop3:0xf6\n"
    "v_bitop3_b32 v248, v248, v151, s50 bitop3:0xf6\n"
    "s_waitcnt vmcnt(1)\n"
    "v_bitop3_b32 v248, v248, v153, s50 bitop3:0xf6\n"
    "v_bitop3_b32 v248, v248, v155, s50 bitop3:0xf6\n"
    "s_waitcnt vmcnt(0)\n"
    "v_bitop3_b32 v248, v248, v157, s50 bitop3:0xf6\n"
    "v_bitop3_b32 v248, v248, v159, s50 bitop3:0xf6\n"
    "v_cmp_ne_u32 vcc, 0, v248\n"
    "s_nop 0\n"
    "s_cmp_lg_u64 vcc, 0\n"
    "s_cbranch_scc0 .Lf_gback48\n"
    "s_add_i32 s7, s7, -1\n"
    "s_cmp_gt_i32 s7, 0\n"
    "s_cbranch_scc1 .Lf_gslow47\n"
    "s_branch .Lf_gback48\n"
    ".Lf_xslow49:\n"
    "s_call_b64 s[62:63], .Lf_xall36\n"
    "s_branch .Lf_xback50\n"
    ".Lf_xslow51:\n"
    "s_call_b64 s[62:63], .Lf_xall36\n"
    "s_branch .Lf_xback52\n"
    ".Lf_slow53:\n"
    "buffer_load_dwordx2 v[238:239], v4, s[8:11], s83 offen sc1\n"
    "s_waitcnt vmcnt(0)\n"
    "v_cmp_eq_u64 s[86:87], s[50:51], v[238:239]\n"
    "s_nop 0\n"
    "s_andn2_b32 s79, 0xff, s86\n"
    "s_cbranch_scc0 .Lf_back54\n"
    "s_add_i32 s7, s7, -1\n"
    "s_cmp_gt_i32 s7, 0\n"
    "s_cbranch_scc1 .Lf_slow53\n"
    "s_branch .Lf_back54\n"
    ".Lf_slow55:\n"
    "buffer_load_dwordx2 v[238:239], v4, s[8:11], s83 offen sc1\n"
    "s_waitcnt vmcnt(0)\n"
    "v_cmp_eq_u64 s[86:87], s[50:51], v[238:239]\n"
    "s_nop 0\n"
    "s_andn2_b32 s79, 0xff, s86\n"
    "s_cbranch_scc0 .Lf_back56\n"
    "s_add_i32 s7, s7, -1\n"
    "s_cmp_gt_i32 s7, 0\n"
    "s_cbranch_scc1 .Lf_slow55\n"
    "s_branch .Lf_back56\n"
    ".Lf_hall59:\n"
    "buffer_load_dwordx4 v[64:67], v236, s[8:11], s41 offen offset:0 sc1\n"
    "buffer_load_dwordx4 v[68:71], v237, s[8:11], s41 offen offset:0 sc1\n"
    "buffer_load_dwordx4 v[72:75], v236, s[8:11], s41 offen offset:2048 sc1\n"
    "buffer_load_dwordx4 v[76:79], v237, s[8:11], s41 offen offset:2048 sc1\n"
    "buffer_load_dwordx4 v[80:83], v236, s[8:11], s42 offen offset:0 sc1\n"
    "buffer_load_dwordx4 v[84:87], v237, s[8:11], s42 offen offset:0 sc1\n"
    "buffer_load_dwordx4 v[88:91], v236, s[8:11], s42 offen offset:2048 sc1\n"
    "buffer_load_dwordx4 v[92:95], v237, s[8:11], s42 offen offset:2048 sc1\n"
    "buffer_load_dwordx4 v[96:99], v236, s[8:11], s43 offen offset:0 sc1\n"
    "buffer_load_dwordx4 v[100:103], v237, s[8:11], s43 offen offset:0 sc1\n"
    "buffer_load_dwordx4 v[104:107], v236, s[8:11], s43 offen offset:2048 sc1\n"
    "buffer_load_dwordx4 v[108:111], v237, s[8:11], s43 offen offset:2048 sc1\n"
    "buffer_load_dwordx4 v[112:115], v236, s[8:11], s48 offen offset:0 sc1\n"
    "buffer_load_dwordx4 v[116:119], v237, s[8:11], s48 offen offset:0 sc1\n"
    "buffer_load_dwordx4 v[120:123], v236, s[8:11], s48 offen offset:2048 sc1\n"
    "buffer_load_dwordx4 v[124:127], v237, s[8:11], s48 offen offset:2048 sc1\n"
    "buffer_load_dwordx4 v[128:131], v236, s[8:11], s49 offen offset:0 sc1\n"
    "buffer_load_dwordx4 v[132:135], v237, s[8:11], s49 offen offset:0 sc1\n"
    "buffer_load_dwordx4 v[136:139], v236, s[8:11], s49 offen offset:2048 sc1\n"
    "buffer_load_dwordx4 v[140:143], v237, s[8:11], s49 offen offset:2048 sc1\n"
    "buffer_load_dwordx4 v[144:147], v236, s[8:11], s85 offen offset:0 sc1\n"
    "buffer_load_dwordx4 v[148:151], v237, s[8:11], s85 offen offset:0 sc1\n"
    "buffer_load_dwordx4 v[152:155], v236, s[8:11], s85 offen offset:2048 sc1\n"
    "buffer_load_dwordx4 v[156:159], v237, s[8:11], s85 offen offset:2048 sc1\n"
    "s_waitcnt vmcnt(0)\n"
    "v_max3_u32 v238, v64, v66, v68\n"
    "v_max3_u32 v238, v238, v70, v72\n"
    "v_max3_u32 v238, v238, v74, v76\n"
    "v_max3_u32 v238, v238, v78, v80\n"
    "v_max3_u32 v238, v238, v82, v84\n"
    "v_max3_u32 v238, v238, v86, v88\n"
    "v_max3_u32 v238, v238, v90, v92\n"
    "v_max3_u32 v238, v238, v94, v96\n"
    "v_max3_u32 v238, v238, v98, v100\n"
    "v_max3_u32 v238, v238, v102, v104\n"
    "v_max3_u32 v238, v238, v106, v108\n"
    "v_max3_u32 v238, v238, v110, v112\n"
    "v_max3_u32 v238, v238, v114, v116\n"
    "v_max3_u32 v238, v238, v118, v120\n"
    "v_max3_u32 v238, v238, v122, v124\n"
    "v_max3_u32 v238, v238, v126, v128\n"
    "v_max3_u32 v238, v238, v130, v132\n"
    "v_max3_u32 v238, v238, v134, v136\n"
    "v_max3_u32 v238, v238, v138, v140\n"
    "v_max3_u32 v238, v238, v142, v144\n"
    "v_max3_u32 v238, v238, v146, v148\n"
    "v_max3_u32 v238, v238, v150, v152\n"
    "v_max3_u32 v238, v238, v154, v156\n"
    "v_max_u32 v238, v238, v158\n"
    "v_cmp_eq_u32 vcc, -1, v238\n"
    "s_nop 0\n"
    "s_cbranch_vccz .Lf_hout73\n"
    "s_add_i32 s7, s7, -1\n"
    "s_cmp_gt_i32 s7, 0\n"
    "s_cbranch_scc1 .Lf_hall59\n"
    ".Lf_hout73:\n"
    "s_setpc_b64 s[62:63]\n"
    ".Lf_hslow57:\n"
    "s_call_b64 s[62:63], .Lf_hall59\n"
    "s_branch .Lf_hback58\n"
    ".Lf_hslow60:\n"
    "s_call_b64 s[62:63], .Lf_hall59\n"
    "s_branch .Lf_hback61\n"
    ".Lf_hslow62:\n"
    "s_call_b64 s[62:63], .Lf_hall59\n"
    "s_branch .Lf_hback63\n"
    ".Lf_hslow64:\n"
    "s_call_b64 s[62:63], .Lf_hall59\n"
    "s_branch .Lf_hback65\n"
    ".Lf_hslow66:\n"
    "s_call_b64 s[62:63], .Lf_hall59\n"
    "s_branch .Lf_hback67\n"
    ".Lf_hslow68:\n"
    "s_call_b64 s[62:63], .Lf_hall59\n"
    "s_branch .Lf_hback69\n"
    ".Lf_slow70:\n"
    "buffer_load_dwordx2 v[248:249], v9, s[8:11], s64 offen sc1\n"
    "s_waitcnt vmcnt(0)\n"
    "v_cmp_eq_u64 s[86:87], s[50:51], v[248:249]\n"
    "s_nop 0\n"
    "s_andn2_b32 s79, s98, s86\n"
    "s_cbranch_scc0 .Lf_back71\n"
    "s_add_i32 s7, s7, -1\n"
    "s_cmp_gt_i32 s7, 0\n"
    "s_cbranch_scc1 .Lf_slow70\n"
    "s_branch .Lf_back71\n"
    : "+v"(tid)
    : "s"(__builtin_amdgcn_kernarg_segment_ptr()), "s"(blockIdx.x)
    : "v1", "v2", "v3", "v4", "v5", "v6", "v7", "v8", "v9", "v10", "v11", "v12", "v13", "v14", "v15", "v16", "v17", "v18", "v19", "v20", "v21", "v22", "v23", "v24", "v25", "v26", "v27", "v28", "v29", "v30", "v31", "v32", "v33", "v34", "v35", "v36", "v37", "v38", "v39", "v40", "v41", "v42", "v43", "v44", "v45", "v46", "v47", "v48", "v49", "v50", "v51", "v52", "v53", "v54", "v55", "v56", "v57", "v58", "v59", "v60", "v61", "v62", "v63", "v64", "v65", "v66", "v67", "v68", "v69", "v70", "v71", "v72", "v73", "v74", "v75", "v76", "v77", "v78", "v79", "v80", "v81", "v82", "v83", "v84", "v85", "v86", "v87", "v88", "v89", "v90", "v91", "v92", "v93", "v94", "v95", "v96", "v97", "v98", "v99", "v100", "v101", "v102", "v103", "v104", "v105", "v106", "v107", "v108", "v109", "v110", "v111", "v112", "v113", "v114", "v115", "v116", "v117", "v118", "v119", "v120", "v121", "v122", "v123", "v124", "v125", "v126", "v127", "v128", "v129", "v130", "v131", "v132", "v133", "v134", "v135", "v136", "v137", "v138", "v139", "v140", "v141", "v142", "v143", "v144", "v145", "v146", "v147", "v148", "v149", "v150", "v151", "v152", "v153", "v154", "v155", "v156", "v157", "v158", "v159", "v160", "v161", "v162", "v163", "v164", "v165", "v166", "v167", "v168", "v169", "v170", "v171", "v172", "v173", "v174", "v175", "v176", "v177", "v178", "v179", "v180", "v181", "v182", "v183", "v184", "v185", "v186", "v187", "v188", "v189", "v190", "v191", "v192", "v193", "v194", "v195", "v196", "v197", "v198", "v199", "v200", "v201", "v202", "v203", "v204", "v205", "v206", "v207", "v208", "v209", "v210", "v211", "v212", "v213", "v214", "v215", "v216", "v217", "v218", "v219", "v220", "v221", "v222", "v223", "v224", "v225", "v226", "v227", "v228", "v229", "v230", "v231", "v232", "v233", "v234", "v235", "v236", "v237", "v238", "v239", "v240", "v241", "v242", "v243", "v244", "v245", "v246", "v247", "v248", "v249", "v250", "v251", "v252", "v253", "v254", "v255", "a0", "a1", "a2", "a3", "a4", "a5", "a6", "a7", "a8", "a9", "a10", "a11", "a12", "a13", "a14", "a15", "a16", "a17", "a18", "a19", "a20", "a21", "a22", "a23", "a24", "a25", "a26", "a27", "a28", "a29", "a30", "a31", "a32", "a33", "a34", "a35", "a36", "a37", "a38", "a39", "a40", "a41", "a42", "a43", "a44", "a45", "a46", "a47", "a48", "a49", "a50", "a51", "a52", "a53", "a54", "a55", "a56", "a57", "a58", "a59", "a60", "a61", "a62", "a63", "a64", "a65", "a66", "a67", "a68", "a69", "a70", "a71", "a72", "a73", "a74", "a75", "a76", "a77", "a78", "a79", "a80", "a81", "a82", "a83", "a84", "a85", "a86", "a87", "a88", "a89", "a90", "a91", "a92", "a93", "a94", "a95", "a96", "a97", "a98", "a99", "a100", "a101", "a102", "a103", "a104", "a105", "a106", "a107", "a108", "a109", "a110", "a111", "a112", "a113", "a114", "a115", "a116", "a117", "a118", "a119", "a120", "a121", "a122", "a123", "a124", "a125", "a126", "a127", "a128", "a129", "a130", "a131", "a132", "a133", "a134", "a135", "a136", "a137", "a138", "a139", "a140", "a141", "a142", "a143", "a144", "a145", "a146", "a147", "a148", "a149", "a150", "a151", "a152", "a153", "a154", "a155", "a156", "a157", "a158", "a159", "a160", "a161", "a162", "a163", "a164", "a165", "a166", "a167", "a168", "a169", "a170", "a171", "a172", "a173", "a174", "a175", "a176", "a177", "a178", "a179", "a180", "a181", "a182", "a183", "a184", "a185", "a186", "a187", "a188", "a189", "a190", "a191", "a192", "a193", "a194", "a195", "a196", "a197", "a198", "a199", "a200", "a201", "a202", "a203", "a204", "a205", "a206", "a207", "a208", "a209", "a210", "a211", "a212", "a213", "a214", "a215", "a216", "a217", "a218", "a219", "a220", "a221", "a222", "a223", "a224", "a225", "a226", "a227", "a228", "a229", "a230", "a231", "a232", "a233", "a234", "a235", "a236", "a237", "a238", "a239", "a240", "a241", "a242", "a243", "a244", "a245", "a246", "a247", "a248", "a249", "a250", "a251", "a252", "a253", "a254", "a255", "s4", "s5", "s6", "s7", "s8", "s9", "s10", "s11", "s12", "s13", "s14", "s15", "s16", "s17", "s18", "s19", "s20", "s21", "s22", "s23", "s24", "s25", "s26", "s27", "s28", "s29", "s30", "s31", "s32", "s33", "s34", "s35", "s36", "s37", "s38", "s39", "s40", "s41", "s42", "s43", "s44", "s45", "s46", "s47", "s48", "s49", "s50", "s51", "s52", "s53", "s54", "s55", "s56", "s57", "s58", "s59", "s60", "s61", "s62", "s63", "s64", "s65", "s66", "s67", "s68", "s69", "s70", "s71", "s72", "s73", "s74", "s75", "s76", "s77", "s78", "s79", "s80", "s81", "s82", "s83", "s84", "s85", "s86", "s87", "s88", "s89", "s90", "s91", "s92", "s93", "s94", "s95", "s96", "s97", "s98", "s99", "s100", "s101", "vcc", "scc", "memory");
#endif
}

extern "C" void kernel_launch(void* const* d_in, const int* in_sizes, int n_in, void* d_out, int out_size,
                              void* d_ws, size_t ws_size, hipStream_t stream) {
  (void)hipFuncSetAttribute((const void*)fused_kernel, hipFuncAttributeMaxDynamicSharedMemorySize, 163840);
  fused_kernel<<<256, 256, 163840, stream>>>((const u16*)d_in[0], (const u16*)d_in[1], (const u16*)d_in[2],
                                             (const u16*)d_in[3], (const u16*)d_in[4], (const u16*)d_in[5],
                                             (const u16*)d_in[7], (const u16*)d_in[8], (const u16*)d_in[9],
                                             (u16*)d_out, (char*)d_ws + (ws_size >= 35651584u ? 0u : 0u));
}
```
